```python
import torch

def _softmax(x, axis=-1):
    return torch.softmax(x, dim=axis)
def _gelu(x):
    return torch.nn.functional.gelu(x, approximate="tanh")
def _silu(x):
    return torch.nn.functional.silu(x)
def _sigmoid(x):
    return torch.sigmoid(x)
def _layernorm(x, axis=-1, eps=1e-5):
    m = x.mean(dim=axis, keepdim=True)
    v = x.var(dim=axis, keepdim=True, unbiased=False)
    return (x - m) / torch.sqrt(v + eps)
def _scatter_add(tgt, idx, src, axis=0):
    return tgt.clone().index_add_(axis, idx, src)
def _segment_sum(src, seg, n):
    out = torch.zeros((n,) + src.shape[1:], dtype=src.dtype)
    return out.index_add_(0, seg, src)
def _pad(x, pw):
    flat = tuple(v for pair in reversed(pw) for v in pair)
    return torch.nn.functional.pad(x, flat)

kernel_name = 'gated_mix__linear_kernel_hedgehog__grouped_kv_mqa__prompt'

def setup_inputs():

    _w = (lambda a: (lambda t: t.to(torch.bfloat16) if t.is_floating_point() else t)(torch.as_tensor(a)))
    def _A_setup():
        B, S, D, H = 1, 2048, 576, 32
        Dh = D // H
        return {
            "x": _w((lambda *s: torch.randn(*s, dtype=torch.bfloat16) * 0.05)(B, S, D)),
            "wqkv": _w((lambda *s: torch.randn(*s, dtype=torch.bfloat16) * 1.6)(D, 3 * D)),
            "w_phi": _w((lambda *s: torch.randn(*s, dtype=torch.bfloat16) * 1.6)(H, Dh, Dh) * (Dh ** -0.5)),
            "wo": _w((lambda *s: torch.randn(*s, dtype=torch.bfloat16) * 0.05)(D, D)),
        }
    _A_in = _A_setup()

    def _B_setup():
        B, S, D, H = 1, 2048, 576, 32
        Dh = D // H
        return {
            "x": _w((lambda *s: torch.randn(*s, dtype=torch.bfloat16) * 0.05)(B, S, D)),
            "wq": _w((lambda *s: torch.randn(*s, dtype=torch.bfloat16) * 1.6)(D, H * Dh)),
            "wk": _w((lambda *s: torch.randn(*s, dtype=torch.bfloat16) * 1.6)(D, Dh)),
            "wv": _w((lambda *s: torch.randn(*s, dtype=torch.bfloat16) * 0.05)(D, Dh)),
            "wo": _w((lambda *s: torch.randn(*s, dtype=torch.bfloat16) * 0.05)(H * Dh, D)),
        }
    _B_in = _B_setup()

    return {
        "A_x": _A_in["x"],
        "A_wqkv": _A_in["wqkv"],
        "A_w_phi": _A_in["w_phi"],
        "A_wo": _A_in["wo"],
        "B_x": _B_in["x"],
        "B_wq": _B_in["wq"],
        "B_wk": _B_in["wk"],
        "B_wv": _B_in["wv"],
        "B_wo": _B_in["wo"],
        "gate_w": _w((lambda *s: torch.randn(*s, dtype=torch.bfloat16) * 0.05)(576) * 16.0),
    }

def reference(A_x, A_wqkv, A_w_phi, A_wo, B_x, B_wq, B_wk, B_wv, B_wo, gate_w):
    def _A_ref(x, wqkv, w_phi, wo):
        B, S, D = x.shape
        H = 32; Dh = D // H
        qkv = (lambda x, *d: x.permute(*d))((x @ wqkv).reshape(B, S, 3, H, Dh), 2, 0, 3, 1, 4)
        q, k, v = qkv[0], qkv[1], qkv[2]
        pq = torch.einsum("bhsd,hdf->bhsf", q, w_phi)
        pk = torch.einsum("bhsd,hdf->bhsf", k, w_phi)
        phi_q = torch.concatenate([_softmax(pq, axis=-1), _softmax(-pq, axis=-1)], axis=-1)
        phi_k = torch.concatenate([_softmax(pk, axis=-1), _softmax(-pk, axis=-1)], axis=-1)
        kv = torch.einsum("bhsf,bhsd->bhfd", phi_k, v)
        z  = torch.einsum("bhsf->bhf", phi_k)
        num = torch.einsum("bhqf,bhfd->bhqd", phi_q, kv)
        den = torch.einsum("bhqf,bhf->bhq", phi_q, z)[..., None] + 1e-6
        o = num / den
        return (lambda x, *d: x.permute(*d))(o, 0, 2, 1, 3).reshape(B, S, D) @ wo

    def _B_ref(x, wq, wk, wv, wo):
        B, S, D = x.shape
        H = 32
        Dh = D // H
        q = (lambda x, *d: x.permute(*d))((x @ wq).reshape(B, S, H, Dh), 0, 2, 1, 3)
        k = x @ wk
        v = x @ wv
        scores = torch.einsum("bhqd,bkd->bhqk", q, k) * (Dh ** -0.5)
        p = _softmax(scores, axis=-1)
        o = torch.einsum("bhqk,bkd->bhqd", p, v)
        return (lambda x, *d: x.permute(*d))(o, 0, 2, 1, 3).reshape(B, S, H * Dh) @ wo

    x = A_x
    g = _sigmoid((x * gate_w).sum(-1, keepdims=True))
    a = _A_ref(x, A_wqkv, A_w_phi, A_wo)
    b = _B_ref(x, B_wq, B_wk, B_wv, B_wo)
    return g * a + (1 - g) * b
```

```cpp
#include <hip/hip_runtime.h>
#include <hip/hip_bf16.h>
#include <stdint.h>

typedef unsigned short u16;
__device__ __forceinline__ float bf2f(u16 v){ return __uint_as_float(((unsigned)v)<<16); }
__device__ __forceinline__ u16 f2bf(float f){ unsigned u=__float_as_uint(f); u += 0x7fff + ((u>>16)&1); return (u16)(u>>16); }

#define S 2048
#define D 576
#define H 32
#define DH 18
#define NC 2341

__global__ void k_proj(const u16* x, const u16* wqkv, const u16* wq, const u16* wk, const u16* wv, const u16* gw, float* P){
  int c = blockIdx.x*blockDim.x + threadIdx.x; int s = blockIdx.y;
  if (c>=NC) return;
  const u16* w; int pitch;
  if (c<1728){ w = wqkv + c; pitch=1728; }
  else if (c<1728+576){ w = wq + (c-1728); pitch=576; }
  else if (c<1728+576+18){ w = wk + (c-2304); pitch=18; }
  else if (c<1728+576+36){ w = wv + (c-2322); pitch=18; }
  else { w = gw; pitch=1; }
  float acc=0;
  for (int k=0;k<D;k++) acc += bf2f(x[s*D+k])*bf2f(w[k*pitch]);
  P[(size_t)s*NC+c]=acc;
}
__global__ __launch_bounds__(256) void k_phi(const float* P, const u16* wphi, float* phiq, float* phik){
  int t = blockIdx.x*blockDim.x + threadIdx.x;
  int which = t & 1; int h = (t>>1)&31; int s = t>>6;
  const float* q = P + (size_t)s*NC + which*576 + h*18;
  float p[18];
  #pragma unroll
  for (int f=0;f<18;f++){ float a=0;
    #pragma unroll
    for (int d=0;d<18;d++) a += q[d]*bf2f(wphi[(h*18+d)*18+f]); p[f]=a; }
  float mx=p[0], mn=p[0];
  #pragma unroll
  for (int f=1;f<18;f++){ mx=fmaxf(mx,p[f]); mn=fminf(mn,p[f]); }
  float e1[18], e2[18], s1=0, s2=0;
  #pragma unroll
  for (int f=0;f<18;f++){ e1[f]=__expf(p[f]-mx); e2[f]=__expf(mn-p[f]); s1+=e1[f]; s2+=e2[f]; }
  float* o = (which? phik: phiq) + ((size_t)h*S+s)*36;
  #pragma unroll
  for (int f=0;f<18;f++){ o[f]=e1[f]/s1; o[18+f]=e2[f]/s2; }
}
__global__ void k_kv(const float* P, const float* phik, float* kv){
  int h = blockIdx.x; int t = threadIdx.x; if (t>=36*19) return;
  int f = t/19, d = t%19; float a=0;
  for (int s=0;s<S;s++){ float v = d<18 ? P[(size_t)s*NC+1152+h*18+d] : 1.f; a += phik[((size_t)h*S+s)*36+f]*v; }
  kv[h*36*19+t]=a;
}
__global__ __launch_bounds__(256) void k_mix(const float* P, const float* phiq, const float* kv, float* O){
  int t = blockIdx.x*blockDim.x + threadIdx.x; int h = t&31; int s = t>>5;
  float g = 1.f/(1.f+__expf(-P[(size_t)s*NC+2340]));
  {
    const float* pq = phiq + ((size_t)h*S+s)*36; const float* k = kv + h*36*19;
    float den=1e-6f; float dd=0; for (int f=0;f<36;f++) dd += pq[f]*k[f*19+18]; den += dd;
    for (int d=0;d<18;d++){ float n=0; for (int f=0;f<36;f++) n += pq[f]*k[f*19+d]; O[(size_t)s*1152+h*18+d] = g*n/den; }
  }
  {
    const float* q = P + (size_t)s*NC + 1728 + h*18;
    float qq[18], o[18]; float m=-1e30f, l=0;
    #pragma unroll
    for (int d=0;d<18;d++){ qq[d]=q[d]*0.23570226039551584f; o[d]=0; }
    for (int k=0;k<S;k++){
      const float* kk = P + (size_t)k*NC + 2304; float sc=0;
      #pragma unroll
      for (int d=0;d<18;d++) sc += qq[d]*kk[d];
      float mn = fmaxf(m,sc); float c=__expf(m-mn), p=__expf(sc-mn); m=mn; l=l*c+p;
      #pragma unroll
      for (int d=0;d<18;d++) o[d]=o[d]*c+p*kk[18+d];
    }
    #pragma unroll
    for (int d=0;d<18;d++) O[(size_t)s*1152+576+h*18+d] = (1.f-g)*o[d]/l;
  }
}
__global__ void k_out(const float* O, const u16* woA, const u16* woB, u16* out){
  int c = blockIdx.x*blockDim.x + threadIdx.x; int s = blockIdx.y; if (c>=D) return;
  float a=0;
  for (int k=0;k<576;k++) a += O[(size_t)s*1152+k]*bf2f(woA[k*576+c]);
  for (int k=0;k<576;k++) a += O[(size_t)s*1152+576+k]*bf2f(woB[k*576+c]);
  out[s*D+c]=f2bf(a);
}

extern "C" void kernel_launch(void* const* d_in, const int* in_sizes, int n_in,
                              void* d_out, int out_size, void* d_ws, size_t ws_size,
                              hipStream_t stream) {
  const u16* x=(const u16*)d_in[0]; const u16* wqkv=(const u16*)d_in[1]; const u16* wphi=(const u16*)d_in[2];
  const u16* woA=(const u16*)d_in[3]; const u16* wq=(const u16*)d_in[5]; const u16* wk=(const u16*)d_in[6];
  const u16* wv=(const u16*)d_in[7]; const u16* woB=(const u16*)d_in[8]; const u16* gw=(const u16*)d_in[9];
  float* P=(float*)d_ws; float* phiq=P+(size_t)S*NC+64; float* phik=phiq+(size_t)H*S*36; float* kv=phik+(size_t)H*S*36; float* O=kv+H*36*19+64;
  k_proj<<<dim3((NC+255)/256,S),256,0,stream>>>(x,wqkv,wq,wk,wv,gw,P);
  k_phi<<<S*H*2/256,256,0,stream>>>(P,wphi,phiq,phik);
  k_kv<<<H,704,0,stream>>>(P,phik,kv);
  k_mix<<<S*H/256,256,0,stream>>>(P,phiq,kv,O);
  k_out<<<dim3(3,S),256,0,stream>>>(O,woA,woB,(u16*)d_out);
}
```

```cpp
#include <hip/hip_runtime.h>
#include <hip/hip_bf16.h>
#include <stdint.h>

typedef unsigned short u16;
typedef unsigned int u32;
typedef unsigned long long u64;
typedef float f32x4 __attribute__((ext_vector_type(4)));
typedef float f32x16 __attribute__((ext_vector_type(16)));
typedef u32 u32x2 __attribute__((ext_vector_type(2)));
typedef u32 u32x4 __attribute__((ext_vector_type(4)));
typedef __bf16 bf16x8 __attribute__((ext_vector_type(8)));

#define SEQ 2048
#define DM 576
#define NH 32
#define DH 18

#define OFF_KV   0
#define KV_BYTES 155648
#define KV_ST0 2304
#define KV_STS 18944
#define OFF_QM   (152u * 1024u)
#define OFF_QT   (OFF_QM + 32u * 64u * 1024u)
#define OFF_PQ   (OFF_QT + 32u * 64u * 128u)
#define OFF_KVP  (OFF_PQ + 32u * 64u * 2304u)
#define OFF_G    (OFF_KVP + 32u * 16u * 2736u)
#define OFF_WT   (OFF_G + 8192u)
#define OFF_OC   (OFF_WT + 8u * 36u * 4608u)
#define OFF_END  (OFF_OC + 128u * 144u * 256u)
#define OFF_DBG  (16u << 20)

__device__ __forceinline__ float bf2f(u16 v) { return __uint_as_float(((u32)v) << 16); }
__device__ __forceinline__ u16 f2bf(float f) { u32 u = __float_as_uint(f); u += 0x7fff + ((u >> 16) & 1); return (u16)(u >> 16); }
__device__ __forceinline__ u32 pk_bf16(float lo, float hi) { u32 r; asm("v_cvt_pk_bf16_f32 %0, %1, %2" : "=v"(r) : "v"(lo), "v"(hi)); return r; }
__device__ __forceinline__ u64 uni64(const void* p) { u64 a = (u64)p; u32 lo = __builtin_amdgcn_readfirstlane((u32)a), hi = __builtin_amdgcn_readfirstlane((u32)(a >> 32)); return ((u64)hi << 32) | lo; }
__device__ __forceinline__ u64 uni64v(u64 a) { u32 lo = __builtin_amdgcn_readfirstlane((u32)a), hi = __builtin_amdgcn_readfirstlane((u32)(a >> 32)); return ((u64)hi << 32) | lo; }
__device__ __forceinline__ int rho(int d) { return d < 4 ? d : d < 8 ? d + 4 : d < 12 ? d - 4 : d; }

#define STAMP(k, s) do { } while (0)

#define K1_W 65536
#define K1_WSEG 9232
#define K1_E (K1_W + 4 * 18464)
#define K1_T1 16384
#define K1_T2 65536
#define K1_T1P 296
#define K1_T2P 352
#define K1_LDS (K1_E + 18432 + 2688)
typedef short s16x4 __attribute__((ext_vector_type(4)));
typedef float f32x8 __attribute__((ext_vector_type(8)));
__device__ __forceinline__ u32x2 tr_read(const char* lds_p) {
    return __builtin_bit_cast(u32x2, __builtin_amdgcn_ds_read_tr16_b64_v4i16((__attribute__((address_space(3))) s16x4*)lds_p));
}
__device__ __forceinline__ int swap23(int k) { return (k & ~12) | ((k & 4) << 1) | ((k & 8) >> 1); }
__device__ __forceinline__ float xhalf_max(float v) { auto r = __builtin_amdgcn_permlane32_swap(__float_as_uint(v), __float_as_uint(v), false, false); return fmaxf(__uint_as_float(r[0]), __uint_as_float(r[1])); }
__device__ __forceinline__ float xhalf_min(float v) { auto r = __builtin_amdgcn_permlane32_swap(__float_as_uint(v), __float_as_uint(v), false, false); return fminf(__uint_as_float(r[0]), __uint_as_float(r[1])); }
__device__ __forceinline__ float xhalf_add(float v) { auto r = __builtin_amdgcn_permlane32_swap(__float_as_uint(v), __float_as_uint(v), false, false); return __uint_as_float(r[0]) + __uint_as_float(r[1]); }
__device__ __forceinline__ u32 kv_main_off(int kt) { int kh = kt >> 5, m = kt & 31; return m == 0 ? kh * 1024 : KV_ST0 + ((m - 1) >> 2) * KV_STS + kh * 4096 + ((m - 1) & 3) * 1024; }
__device__ __forceinline__ u32 kv_tail_off(int kt) { int kh = kt >> 5, m = kt & 31; return m == 0 ? 2048 + kh * 128 : KV_ST0 + ((m - 1) >> 2) * KV_STS + 17920 + kh * 512 + ((m - 1) & 3) * 128; }
__device__ __forceinline__ u32 kv_v_off(int kt) { int kh = kt >> 5, m = kt & 31; return KV_ST0 + (m >> 2) * KV_STS + 8192 + kh * 4864 + (m & 3) * 1216; }

static __device__ __attribute__((aligned(64))) unsigned long long g_ptrs[2];
__global__ __launch_bounds__(512, 2) void k1_proj(const u16* __restrict__ x, const u16* __restrict__ wqkv, const u16* __restrict__ wphi, const u16* __restrict__ woA,
                                                  const u16* __restrict__ wq, const u16* __restrict__ wk, const u16* __restrict__ wv, const u16* __restrict__ woB,
                                                  const u16* __restrict__ gw, char* __restrict__ ws, u16* __restrict__ outp) {
    extern __shared__ __attribute__((aligned(16))) char lds[];
    int b; asm volatile("s_mov_b32 %0, %1" : "=s"(b) : "s"(__builtin_amdgcn_workgroup_id_x()));
    STAMP(0, 0);
    const int tid = threadIdx.x, lane = tid & 63;
    const int w = __builtin_amdgcn_readfirstlane(tid >> 6);
    if (w == 0) {
        asm volatile(
        "s_getpc_b64 s[40:41]\n\t"
        "s_lshr_b32 s42, %[b], 3\n\t"
        "s_mul_i32 s42, s42, 12\n\t"
        "s_sub_u32 s42, s42, 16\n\t"
        "v_add_u32 v255, s42, %[ln]\n\t"
        "v_cmp_gt_u32 vcc, 16, %[ln]\n\t"
        "s_and_b32 s40, s40, 0xffffff80\n\t"
        "s_mov_b32 exec_hi, 0\n\t"
        "v_cndmask_b32 v255, v255, %[ln], vcc\n\t"
        "v_lshlrev_b32 v255, 7, v255\n\t"
        "v_cmp_gt_u32 vcc, 47000, v255\n\t"
        "s_and_b32 vcc_lo, vcc_lo, 0xfffffff\n\t"
        "s_mov_b32 exec_lo, vcc_lo\n\t"
        "global_load_dword v255, v255, s[40:41]\n\t"
        "s_mov_b64 exec, -1\n\t"
        "s_cmp_lg_u32 %[b], 0\n\t"
        "s_cbranch_scc1 98f\n\t"
        "s_load_dwordx4 s[44:47], %[karg], 0x48\n\t"
        "v_mov_b32 v246, 0\n\t"
        "s_waitcnt lgkmcnt(0)\n\t"
        "v_mov_b32 v248, s44\n\t"
        "v_mov_b32 v249, s45\n\t"
        "v_mov_b32 v250, s46\n\t"
        "v_mov_b32 v251, s47\n\t"
        "global_store_dwordx4 v246, v[248:251], %[gp]\n\t"
        "s_nop 1\n\t"
        "98:\n\t"
        :: [b] "s"(b), [ln] "v"(lane), [karg] "s"(__builtin_amdgcn_kernarg_segment_ptr()), [gp] "s"((unsigned long long)g_ptrs)
        : "s40", "s41", "s42", "s44", "s45", "s46", "s47", "scc", "vcc", "v246", "v248", "v249", "v250", "v251", "v255", "memory");
    }

    u32x4 PA0, PA1, EP0, EP1, EP2, EZ0, EZ1; f32x16 WO; u32x2 V6; u32 PHA;
    if (w >= 4) {
        asm volatile(
        "s_load_dwordx4 s[52:55], %[karg], 0x0\n\t"
        "s_load_dwordx2 s[56:57], %[karg], 0x20\n\t"
        "s_load_dwordx2 s[80:81], %[karg], 0x10\n\t"
        "s_setprio 3\n\t"
        "v_lshrrev_b32 v10, 3, v244\n\t"
        "v_mul_u32_u24 v10, 1152, v10\n\t"
        "v_lshrrev_b32 v11, 4, v244\n\t"
        "v_xor_b32 v11, v11, v244\n\t"
        "v_and_b32 v11, 7, v11\n\t"
        "v_lshlrev_b32 v11, 4, v11\n\t"
        "v_add_u32 v0, v10, v11\n\t"
        "v_xor_b32 v11, 64, v11\n\t"
        "v_add_u32 v10, 9216, v10\n\t"
        "v_add_u32 v1, v10, v11\n\t"
        "v_add_u32 v2, 18432, v0\n\t"
        "v_add_u32 v3, 18432, v1\n\t"
        "s_and_b32 s60, %[pw], 1\n\t"
        "s_mul_i32 s58, s60, 320\n\t"
        "v_add_u32 v12, s58, v244\n\t"
        "v_mul_u32_u24 v14, 7282, v12\n\t"
        "v_lshrrev_b32 v14, 16, v14\n\t"
        "v_mul_u32_u24 v15, 9, v14\n\t"
        "v_sub_u32 v15, v12, v15\n\t"
        "v_lshlrev_b32 v30, 4, v15\n\t"
        "v_and_b32 v15, 0x38, v14\n\t"
        "v_and_b32 v16, 1, v14\n\t"
        "v_lshl_or_b32 v15, v16, 2, v15\n\t"
        "v_bfe_u32 v16, v14, 1, 2\n\t"
        "v_or_b32 v20, v15, v16\n\t"
        "v_add_u32 v13, 64, v12\n\t"
        "v_mul_u32_u24 v14, 7282, v13\n\t"
        "v_lshrrev_b32 v14, 16, v14\n\t"
        "v_mul_u32_u24 v15, 9, v14\n\t"
        "v_sub_u32 v15, v13, v15\n\t"
        "v_lshlrev_b32 v31, 4, v15\n\t"
        "v_and_b32 v15, 0x38, v14\n\t"
        "v_and_b32 v16, 1, v14\n\t"
        "v_lshl_or_b32 v15, v16, 2, v15\n\t"
        "v_bfe_u32 v16, v14, 1, 2\n\t"
        "v_or_b32 v21, v15, v16\n\t"
        "v_add_u32 v13, 128, v12\n\t"
        "v_mul_u32_u24 v14, 7282, v13\n\t"
        "v_lshrrev_b32 v14, 16, v14\n\t"
        "v_mul_u32_u24 v15, 9, v14\n\t"
        "v_sub_u32 v15, v13, v15\n\t"
        "v_lshlrev_b32 v32, 4, v15\n\t"
        "v_and_b32 v15, 0x38, v14\n\t"
        "v_and_b32 v16, 1, v14\n\t"
        "v_lshl_or_b32 v15, v16, 2, v15\n\t"
        "v_bfe_u32 v16, v14, 1, 2\n\t"
        "v_or_b32 v22, v15, v16\n\t"
        "v_add_u32 v13, 192, v12\n\t"
        "v_mul_u32_u24 v14, 7282, v13\n\t"
        "v_lshrrev_b32 v14, 16, v14\n\t"
        "v_mul_u32_u24 v15, 9, v14\n\t"
        "v_sub_u32 v15, v13, v15\n\t"
        "v_lshlrev_b32 v33, 4, v15\n\t"
        "v_and_b32 v15, 0x38, v14\n\t"
        "v_and_b32 v16, 1, v14\n\t"
        "v_lshl_or_b32 v15, v16, 2, v15\n\t"
        "v_bfe_u32 v16, v14, 1, 2\n\t"
        "v_or_b32 v23, v15, v16\n\t"
        "v_add_u32 v13, 256, v12\n\t"
        "v_mul_u32_u24 v14, 7282, v13\n\t"
        "v_lshrrev_b32 v14, 16, v14\n\t"
        "v_mul_u32_u24 v15, 9, v14\n\t"
        "v_sub_u32 v15, v13, v15\n\t"
        "v_lshlrev_b32 v34, 4, v15\n\t"
        "v_and_b32 v15, 0x38, v14\n\t"
        "v_and_b32 v16, 1, v14\n\t"
        "v_lshl_or_b32 v15, v16, 2, v15\n\t"
        "v_bfe_u32 v16, v14, 1, 2\n\t"
        "v_or_b32 v24, v15, v16\n\t"
        "s_and_b32 s40, %[b], 7\n\t"
        "s_lshr_b32 s41, %[b], 5\n\t"
        "s_bfe_u32 s42, %[b], 0x20003\n\t"
        "s_lshr_b32 s43, s40, 1\n\t"
        "s_lshl_b32 s43, s43, 2\n\t"
        "s_or_b32 s43, s43, s42\n\t"
        "s_lshl_b32 s43, s43, 2\n\t"
        "s_add_u32 s43, s43, %[pw]\n\t"
        "s_mul_i32 s43, s43, 36864\n\t"
        "s_and_b32 s49, s40, 1\n\t"
        "s_lshl_b32 s49, s49, 2\n\t"
        "s_and_b32 s50, s41, 3\n\t"
        "s_or_b32 s49, s49, s50\n\t"
        "s_mov_b32 s82, s49\n\t"
        "s_mul_i32 s49, s49, 144\n\t"
        "s_lshr_b32 s50, %[pw], 1\n\t"
        "s_cmp_ge_u32 s41, 4\n\t"
        "s_cselect_b32 s51, 1, 0\n\t"
        "s_and_b32 s58, s51, s50\n\t"
        "s_cmp_eq_u32 s58, 1\n\t"
        "s_movk_i32 s59, 3456\n\t"
        "s_cselect_b32 s59, 1152, s59\n\t"
        "s_mul_i32 s61, s50, 1152\n\t"
        "s_add_u32 s61, s61, 1152\n\t"
        "s_cmp_eq_u32 s51, 1\n\t"
        "s_cselect_b32 s61, 0, s61\n\t"
        "s_add_u32 s49, s49, s61\n\t"
        "s_lshl_b32 s45, s59, 6\n\t"
        "s_mul_i32 s61, s42, 2\n\t"
        "s_mul_i32 s62, s41, 1\n\t"
        "s_add_u32 s61, s61, s62\n\t"
        "s_mul_i32 s62, s61, 256\n\t"
        "s_lshr_b32 s62, s62, 10\n\t"
        "s_mul_i32 s62, s62, 4\n\t"
        "s_sub_u32 s48, s61, s62\n\t"
        "s_mul_i32 s62, s61, 205\n\t"
        "s_lshr_b32 s62, s62, 10\n\t"
        "s_mul_i32 s62, s62, 5\n\t"
        "s_sub_u32 s40, s61, s62\n\t"
        "s_add_u32 s40, s40, 4\n\t"
        "s_mov_b32 s62, s48\n\t"
        "s_lshl_b32 s46, s48, 7\n\t"
        "s_mul_i32 s47, s48, s45\n\t"
        "s_lshl_b32 s44, %[pw], 12\n\t"
        "s_mul_i32 s63, s50, 9232\n\t"
        "s_mul_i32 s61, s60, 5120\n\t"
        "s_add_u32 s63, s63, s61\n\t"
        "s_add_u32 s63, s63, 65536\n\t"
        "s_mov_b32 s66, 0x100000\n\t"
        "s_mov_b32 s67, 0x20000\n\t"
        "s_mov_b32 s70, 0x400000\n\t"
        "s_mov_b32 s71, 0x20000\n\t"
        "v_mad_u32_u24 v4, v20, s59, v30\n\t"
        "v_mad_u32_u24 v5, v21, s59, v31\n\t"
        "v_mad_u32_u24 v6, v22, s59, v32\n\t"
        "v_mad_u32_u24 v7, v23, s59, v33\n\t"
        "v_mad_u32_u24 v8, v24, s59, v34\n\t"
        "s_waitcnt lgkmcnt(0)\n\t"
        "s_add_u32 s64, s52, s43\n\t"
        "s_addc_u32 s65, s53, 0\n\t"
        "s_cmp_eq_u32 s58, 1\n\t"
        "s_cselect_b32 s68, s56, s54\n\t"
        "s_cselect_b32 s69, s57, s55\n\t"
        "s_add_u32 s68, s68, s49\n\t"
        "s_addc_u32 s69, s69, 0\n\t"
        "s_cmp_eq_u32 s60, 1\n\t"
        "s_cbranch_scc1 51f\n\t"
        "s_add_u32 m0, s44, 0\n\t"
        "s_nop 0\n\t"
        "buffer_load_dwordx4 v0, s[64:67], s46 offen lds\n\t"
        "s_add_u32 m0, s44, 1024\n\t"
        "s_nop 0\n\t"
        "buffer_load_dwordx4 v1, s[64:67], s46 offen lds\n\t"
        "s_add_u32 m0, s44, 2048\n\t"
        "s_nop 0\n\t"
        "buffer_load_dwordx4 v2, s[64:67], s46 offen lds\n\t"
        "s_add_u32 m0, s44, 3072\n\t"
        "s_nop 0\n\t"
        "buffer_load_dwordx4 v3, s[64:67], s46 offen lds\n\t"
        "s_add_u32 s48, s48, 1\n\t"
        "s_cmp_eq_u32 s48, 4\n\t"
        "s_cselect_b32 s48, 0, s48\n\t"
        "s_lshl_b32 s46, s48, 7\n\t"
        "s_add_u32 m0, s63, 0\n\t"
        "s_nop 0\n\t"
        "buffer_load_dwordx4 v4, s[68:71], s47 offen lds\n\t"
        "s_add_u32 m0, s63, 1024\n\t"
        "s_nop 0\n\t"
        "buffer_load_dwordx4 v5, s[68:71], s47 offen lds\n\t"
        "s_add_u32 m0, s63, 2048\n\t"
        "s_nop 0\n\t"
        "buffer_load_dwordx4 v6, s[68:71], s47 offen lds\n\t"
        "s_add_u32 m0, s63, 3072\n\t"
        "s_nop 0\n\t"
        "buffer_load_dwordx4 v7, s[68:71], s47 offen lds\n\t"
        "s_add_u32 m0, s63, 4096\n\t"
        "s_nop 0\n\t"
        "buffer_load_dwordx4 v8, s[68:71], s47 offen lds\n\t"
        "s_add_u32 s62, s62, 1\n\t"
        "s_cmp_eq_u32 s62, 4\n\t"
        "s_cselect_b32 s62, 0, s62\n\t"
        "s_mul_i32 s47, s62, s45\n\t"
        "s_add_u32 m0, s44, 16384\n\t"
        "s_nop 0\n\t"
        "buffer_load_dwordx4 v0, s[64:67], s46 offen lds\n\t"
        "s_add_u32 m0, s44, 17408\n\t"
        "s_nop 0\n\t"
        "buffer_load_dwordx4 v1, s[64:67], s46 offen lds\n\t"
        "s_add_u32 m0, s44, 18432\n\t"
        "s_nop 0\n\t"
        "buffer_load_dwordx4 v2, s[64:67], s46 offen lds\n\t"
        "s_add_u32 m0, s44, 19456\n\t"
        "s_nop 0\n\t"
        "buffer_load_dwordx4 v3, s[64:67], s46 offen lds\n\t"
        "s_add_u32 s48, s48, 1\n\t"
        "s_cmp_eq_u32 s48, 4\n\t"
        "s_cselect_b32 s48, 0, s48\n\t"
        "s_lshl_b32 s46, s48, 7\n\t"
        "s_add_u32 m0, s63, 18464\n\t"
        "s_nop 0\n\t"
        "buffer_load_dwordx4 v4, s[68:71], s47 offen lds\n\t"
        "s_add_u32 m0, s63, 19488\n\t"
        "s_nop 0\n\t"
        "buffer_load_dwordx4 v5, s[68:71], s47 offen lds\n\t"
        "s_add_u32 m0, s63, 20512\n\t"
        "s_nop 0\n\t"
        "buffer_load_dwordx4 v6, s[68:71], s47 offen lds\n\t"
        "s_add_u32 m0, s63, 21536\n\t"
        "s_nop 0\n\t"
        "buffer_load_dwordx4 v7, s[68:71], s47 offen lds\n\t"
        "s_add_u32 m0, s63, 22560\n\t"
        "s_nop 0\n\t"
        "buffer_load_dwordx4 v8, s[68:71], s47 offen lds\n\t"
        "s_add_u32 s62, s62, 1\n\t"
        "s_cmp_eq_u32 s62, 4\n\t"
        "s_cselect_b32 s62, 0, s62\n\t"
        "s_mul_i32 s47, s62, s45\n\t"
        "s_setprio 0\n\t"
        "s_waitcnt vmcnt(9)\n\t"
        "s_barrier\n\t"
        "s_add_u32 m0, s44, 32768\n\t"
        "s_nop 0\n\t"
        "buffer_load_dwordx4 v0, s[64:67], s46 offen lds\n\t"
        "s_add_u32 m0, s44, 33792\n\t"
        "s_nop 0\n\t"
        "buffer_load_dwordx4 v1, s[64:67], s46 offen lds\n\t"
        "s_add_u32 m0, s44, 34816\n\t"
        "s_nop 0\n\t"
        "buffer_load_dwordx4 v2, s[64:67], s46 offen lds\n\t"
        "s_add_u32 m0, s44, 35840\n\t"
        "s_nop 0\n\t"
        "buffer_load_dwordx4 v3, s[64:67], s46 offen lds\n\t"
        "s_add_u32 s48, s48, 1\n\t"
        "s_cmp_eq_u32 s48, 4\n\t"
        "s_cselect_b32 s48, 0, s48\n\t"
        "s_lshl_b32 s46, s48, 7\n\t"
        "s_add_u32 m0, s63, 36928\n\t"
        "s_nop 0\n\t"
        "buffer_load_dwordx4 v4, s[68:71], s47 offen lds\n\t"
        "s_add_u32 m0, s63, 37952\n\t"
        "s_nop 0\n\t"
        "buffer_load_dwordx4 v5, s[68:71], s47 offen lds\n\t"
        "s_add_u32 m0, s63, 38976\n\t"
        "s_nop 0\n\t"
        "buffer_load_dwordx4 v6, s[68:71], s47 offen lds\n\t"
        "s_add_u32 m0, s63, 40000\n\t"
        "s_nop 0\n\t"
        "buffer_load_dwordx4 v7, s[68:71], s47 offen lds\n\t"
        "s_add_u32 m0, s63, 41024\n\t"
        "s_nop 0\n\t"
        "buffer_load_dwordx4 v8, s[68:71], s47 offen lds\n\t"
        "s_add_u32 s62, s62, 1\n\t"
        "s_cmp_eq_u32 s62, 4\n\t"
        "s_cselect_b32 s62, 0, s62\n\t"
        "s_mul_i32 s47, s62, s45\n\t"
        "s_waitcnt vmcnt(9)\n\t"
        "s_barrier\n\t"
        "s_add_u32 m0, s44, 49152\n\t"
        "s_nop 0\n\t"
        "buffer_load_dwordx4 v0, s[64:67], s46 offen lds\n\t"
        "s_add_u32 m0, s44, 50176\n\t"
        "s_nop 0\n\t"
        "buffer_load_dwordx4 v1, s[64:67], s46 offen lds\n\t"
        "s_add_u32 m0, s44, 51200\n\t"
        "s_nop 0\n\t"
        "buffer_load_dwordx4 v2, s[64:67], s46 offen lds\n\t"
        "s_add_u32 m0, s44, 52224\n\t"
        "s_nop 0\n\t"
        "buffer_load_dwordx4 v3, s[64:67], s46 offen lds\n\t"
        "s_mov_b32 s48, s40\n\t"
        "s_lshl_b32 s46, s48, 7\n\t"
        "s_add_u32 m0, s63, 55392\n\t"
        "s_nop 0\n\t"
        "buffer_load_dwordx4 v4, s[68:71], s47 offen lds\n\t"
        "s_add_u32 m0, s63, 56416\n\t"
        "s_nop 0\n\t"
        "buffer_load_dwordx4 v5, s[68:71], s47 offen lds\n\t"
        "s_add_u32 m0, s63, 57440\n\t"
        "s_nop 0\n\t"
        "buffer_load_dwordx4 v6, s[68:71], s47 offen lds\n\t"
        "s_add_u32 m0, s63, 58464\n\t"
        "s_nop 0\n\t"
        "buffer_load_dwordx4 v7, s[68:71], s47 offen lds\n\t"
        "s_add_u32 m0, s63, 59488\n\t"
        "s_nop 0\n\t"
        "buffer_load_dwordx4 v8, s[68:71], s47 offen lds\n\t"
        "s_mov_b32 s62, s40\n\t"
        "s_mul_i32 s47, s62, s45\n\t"
        "s_waitcnt vmcnt(9)\n\t"
        "s_barrier\n\t"
        "s_add_u32 m0, s44, 0\n\t"
        "s_nop 0\n\t"
        "buffer_load_dwordx4 v0, s[64:67], s46 offen lds\n\t"
        "s_add_u32 m0, s44, 1024\n\t"
        "s_nop 0\n\t"
        "buffer_load_dwordx4 v1, s[64:67], s46 offen lds\n\t"
        "s_add_u32 m0, s44, 2048\n\t"
        "s_nop 0\n\t"
        "buffer_load_dwordx4 v2, s[64:67], s46 offen lds\n\t"
        "s_add_u32 m0, s44, 3072\n\t"
        "s_nop 0\n\t"
        "buffer_load_dwordx4 v3, s[64:67], s46 offen lds\n\t"
        "s_add_u32 s48, s48, 1\n\t"
        "s_cmp_eq_u32 s48, 9\n\t"
        "s_cselect_b32 s48, 4, s48\n\t"
        "s_lshl_b32 s46, s48, 7\n\t"
        "s_add_u32 m0, s63, 0\n\t"
        "s_nop 0\n\t"
        "buffer_load_dwordx4 v4, s[68:71], s47 offen lds\n\t"
        "s_add_u32 m0, s63, 1024\n\t"
        "s_nop 0\n\t"
        "buffer_load_dwordx4 v5, s[68:71], s47 offen lds\n\t"
        "s_add_u32 m0, s63, 2048\n\t"
        "s_nop 0\n\t"
        "buffer_load_dwordx4 v6, s[68:71], s47 offen lds\n\t"
        "s_add_u32 m0, s63, 3072\n\t"
        "s_nop 0\n\t"
        "buffer_load_dwordx4 v7, s[68:71], s47 offen lds\n\t"
        "s_add_u32 m0, s63, 4096\n\t"
        "s_nop 0\n\t"
        "buffer_load_dwordx4 v8, s[68:71], s47 offen lds\n\t"
        "s_add_u32 s62, s62, 1\n\t"
        "s_cmp_eq_u32 s62, 9\n\t"
        "s_cselect_b32 s62, 4, s62\n\t"
        "s_mul_i32 s47, s62, s45\n\t"
        "s_waitcnt vmcnt(9)\n\t"
        "s_barrier\n\t"
        "s_add_u32 m0, s44, 16384\n\t"
        "s_nop 0\n\t"
        "buffer_load_dwordx4 v0, s[64:67], s46 offen lds\n\t"
        "s_add_u32 m0, s44, 17408\n\t"
        "s_nop 0\n\t"
        "buffer_load_dwordx4 v1, s[64:67], s46 offen lds\n\t"
        "s_add_u32 m0, s44, 18432\n\t"
        "s_nop 0\n\t"
        "buffer_load_dwordx4 v2, s[64:67], s46 offen lds\n\t"
        "s_add_u32 m0, s44, 19456\n\t"
        "s_nop 0\n\t"
        "buffer_load_dwordx4 v3, s[64:67], s46 offen lds\n\t"
        "s_add_u32 s48, s48, 1\n\t"
        "s_cmp_eq_u32 s48, 9\n\t"
        "s_cselect_b32 s48, 4, s48\n\t"
        "s_lshl_b32 s46, s48, 7\n\t"
        "s_add_u32 m0, s63, 18464\n\t"
        "s_nop 0\n\t"
        "buffer_load_dwordx4 v4, s[68:71], s47 offen lds\n\t"
        "s_add_u32 m0, s63, 19488\n\t"
        "s_nop 0\n\t"
        "buffer_load_dwordx4 v5, s[68:71], s47 offen lds\n\t"
        "s_add_u32 m0, s63, 20512\n\t"
        "s_nop 0\n\t"
        "buffer_load_dwordx4 v6, s[68:71], s47 offen lds\n\t"
        "s_add_u32 m0, s63, 21536\n\t"
        "s_nop 0\n\t"
        "buffer_load_dwordx4 v7, s[68:71], s47 offen lds\n\t"
        "s_add_u32 m0, s63, 22560\n\t"
        "s_nop 0\n\t"
        "buffer_load_dwordx4 v8, s[68:71], s47 offen lds\n\t"
        "s_add_u32 s62, s62, 1\n\t"
        "s_cmp_eq_u32 s62, 9\n\t"
        "s_cselect_b32 s62, 4, s62\n\t"
        "s_mul_i32 s47, s62, s45\n\t"
        "s_waitcnt vmcnt(9)\n\t"
        "s_barrier\n\t"
        "s_add_u32 m0, s44, 32768\n\t"
        "s_nop 0\n\t"
        "buffer_load_dwordx4 v0, s[64:67], s46 offen lds\n\t"
        "s_add_u32 m0, s44, 33792\n\t"
        "s_nop 0\n\t"
        "buffer_load_dwordx4 v1, s[64:67], s46 offen lds\n\t"
        "s_add_u32 m0, s44, 34816\n\t"
        "s_nop 0\n\t"
        "buffer_load_dwordx4 v2, s[64:67], s46 offen lds\n\t"
        "s_add_u32 m0, s44, 35840\n\t"
        "s_nop 0\n\t"
        "buffer_load_dwordx4 v3, s[64:67], s46 offen lds\n\t"
        "s_add_u32 s48, s48, 1\n\t"
        "s_cmp_eq_u32 s48, 9\n\t"
        "s_cselect_b32 s48, 4, s48\n\t"
        "s_lshl_b32 s46, s48, 7\n\t"
        "s_add_u32 m0, s63, 36928\n\t"
        "s_nop 0\n\t"
        "buffer_load_dwordx4 v4, s[68:71], s47 offen lds\n\t"
        "s_add_u32 m0, s63, 37952\n\t"
        "s_nop 0\n\t"
        "buffer_load_dwordx4 v5, s[68:71], s47 offen lds\n\t"
        "s_add_u32 m0, s63, 38976\n\t"
        "s_nop 0\n\t"
        "buffer_load_dwordx4 v6, s[68:71], s47 offen lds\n\t"
        "s_add_u32 m0, s63, 40000\n\t"
        "s_nop 0\n\t"
        "buffer_load_dwordx4 v7, s[68:71], s47 offen lds\n\t"
        "s_add_u32 m0, s63, 41024\n\t"
        "s_nop 0\n\t"
        "buffer_load_dwordx4 v8, s[68:71], s47 offen lds\n\t"
        "s_add_u32 s62, s62, 1\n\t"
        "s_cmp_eq_u32 s62, 9\n\t"
        "s_cselect_b32 s62, 4, s62\n\t"
        "s_mul_i32 s47, s62, s45\n\t"
        "s_waitcnt vmcnt(9)\n\t"
        "s_barrier\n\t"
        "s_add_u32 m0, s44, 49152\n\t"
        "s_nop 0\n\t"
        "buffer_load_dwordx4 v0, s[64:67], s46 offen lds\n\t"
        "s_add_u32 m0, s44, 50176\n\t"
        "s_nop 0\n\t"
        "buffer_load_dwordx4 v1, s[64:67], s46 offen lds\n\t"
        "s_add_u32 m0, s44, 51200\n\t"
        "s_nop 0\n\t"
        "buffer_load_dwordx4 v2, s[64:67], s46 offen lds\n\t"
        "s_add_u32 m0, s44, 52224\n\t"
        "s_nop 0\n\t"
        "buffer_load_dwordx4 v3, s[64:67], s46 offen lds\n\t"
        "s_add_u32 s48, s48, 1\n\t"
        "s_cmp_eq_u32 s48, 9\n\t"
        "s_cselect_b32 s48, 4, s48\n\t"
        "s_lshl_b32 s46, s48, 7\n\t"
        "s_add_u32 m0, s63, 55392\n\t"
        "s_nop 0\n\t"
        "buffer_load_dwordx4 v4, s[68:71], s47 offen lds\n\t"
        "s_add_u32 m0, s63, 56416\n\t"
        "s_nop 0\n\t"
        "buffer_load_dwordx4 v5, s[68:71], s47 offen lds\n\t"
        "s_add_u32 m0, s63, 57440\n\t"
        "s_nop 0\n\t"
        "buffer_load_dwordx4 v6, s[68:71], s47 offen lds\n\t"
        "s_add_u32 m0, s63, 58464\n\t"
        "s_nop 0\n\t"
        "buffer_load_dwordx4 v7, s[68:71], s47 offen lds\n\t"
        "s_add_u32 m0, s63, 59488\n\t"
        "s_nop 0\n\t"
        "buffer_load_dwordx4 v8, s[68:71], s47 offen lds\n\t"
        "s_add_u32 s62, s62, 1\n\t"
        "s_cmp_eq_u32 s62, 9\n\t"
        "s_cselect_b32 s62, 4, s62\n\t"
        "s_mul_i32 s47, s62, s45\n\t"
        "s_waitcnt vmcnt(9)\n\t"
        "s_barrier\n\t"
        "s_add_u32 m0, s44, 0\n\t"
        "s_nop 0\n\t"
        "buffer_load_dwordx4 v0, s[64:67], s46 offen lds\n\t"
        "s_add_u32 m0, s44, 1024\n\t"
        "s_nop 0\n\t"
        "buffer_load_dwordx4 v1, s[64:67], s46 offen lds\n\t"
        "s_add_u32 m0, s44, 2048\n\t"
        "s_nop 0\n\t"
        "buffer_load_dwordx4 v2, s[64:67], s46 offen lds\n\t"
        "s_add_u32 m0, s44, 3072\n\t"
        "s_nop 0\n\t"
        "buffer_load_dwordx4 v3, s[64:67], s46 offen lds\n\t"
        "s_add_u32 m0, s63, 0\n\t"
        "s_nop 0\n\t"
        "buffer_load_dwordx4 v4, s[68:71], s47 offen lds\n\t"
        "s_add_u32 m0, s63, 1024\n\t"
        "s_nop 0\n\t"
        "buffer_load_dwordx4 v5, s[68:71], s47 offen lds\n\t"
        "s_add_u32 m0, s63, 2048\n\t"
        "s_nop 0\n\t"
        "buffer_load_dwordx4 v6, s[68:71], s47 offen lds\n\t"
        "s_add_u32 m0, s63, 3072\n\t"
        "s_nop 0\n\t"
        "buffer_load_dwordx4 v7, s[68:71], s47 offen lds\n\t"
        "s_add_u32 m0, s63, 4096\n\t"
        "s_nop 0\n\t"
        "buffer_load_dwordx4 v8, s[68:71], s47 offen lds\n\t"
        "s_cmp_eq_u32 %[pw], 3\n\t"
        "s_cbranch_scc1 53f\n\t"
        "s_mul_i32 s82, s82, 2592\n\t"
        "s_lshl_b32 s83, %[pw], 10\n\t"
        "s_add_u32 s82, s82, s83\n\t"
        "s_add_u32 s80, s80, s82\n\t"
        "s_addc_u32 s81, s81, 0\n\t"
        "s_add_u32 m0, s83, 157824\n\t"
        "v_lshlrev_b32 v40, 4, v244\n\t"
        "s_cmp_eq_u32 %[pw], 2\n\t"
        "s_cselect_b32 s82, 3, -1\n\t"
        "s_mov_b32 exec_hi, s82\n\t"
        "global_load_lds_dwordx4 v40, s[80:81]\n\t"
        "s_mov_b32 exec_hi, -1\n\t"
        "53:\n\t"
        "s_waitcnt vmcnt(9)\n\t"
        "s_barrier\n\t"
        "s_waitcnt vmcnt(0)\n\t"
        "s_barrier\n\t"
        "s_add_u32 s84, %[pw], 4\n\t"
        "s_and_b32 s85, s84, 3\n\t"
        "s_lshr_b32 s86, s84, 2\n\t"
        "v_and_b32 v240, 31, v244\n\t"
        "v_lshrrev_b32 v241, 5, v244\n\t"
        "s_mul_i32 s87, s86, 18944\n\t"
        "s_mul_i32 s88, s85, 36\n\t"
        "s_add_u32 s87, s87, s88\n\t"
        "s_add_u32 s87, s87, 16384\n\t"
        "v_mul_u32_u24 v242, 296, v240\n\t"
        "v_lshlrev_b32 v243, 4, v241\n\t"
        "v_add3_u32 v228, v242, v243, s87\n\t"
        "v_add_u32 v230, s87, v242\n\t"
        "v_add_u32 v229, 9472, v228\n\t"
        "s_mul_i32 s87, s86, 22528\n\t"
        "s_mul_i32 s88, s85, 80\n\t"
        "s_add_u32 s87, s87, s88\n\t"
        "s_add_u32 s87, s87, 65536\n\t"
        "v_mul_u32_u24 v242, 352, v240\n\t"
        "v_add3_u32 v231, v242, v243, s87\n\t"
        "v_mul_u32_u24 v243, 12, v241\n\t"
        "v_sub_u32 v232, v231, v243\n\t"
        "v_add_u32 v232, 64, v232\n\t"
        "v_lshlrev_b32 v233, 4, v244\n\t"
        "v_lshlrev_b32 v234, 2, v244\n\t"
        "v_bfe_u32 v240, v244, 2, 2\n\t"
        "v_and_b32 v241, 3, v244\n\t"
        "v_lshrrev_b32 v242, 4, v244\n\t"
        "v_lshl_add_u32 v240, v242, 2, v240\n\t"
        "v_lshlrev_b32 v241, 3, v241\n\t"
        "v_mul_u32_u24 v243, 352, v240\n\t"
        "v_add3_u32 v235, v243, v241, s87\n\t"
        "s_mul_i32 s87, s86, 18944\n\t"
        "s_mul_i32 s88, s85, 36\n\t"
        "s_add_u32 s87, s87, s88\n\t"
        "s_and_b32 s88, s85, 1\n\t"
        "s_lshl_b32 s88, s88, 2\n\t"
        "s_sub_u32 s87, s87, s88\n\t"
        "s_add_u32 s87, s87, 16528\n\t"
        "v_mul_u32_u24 v243, 296, v240\n\t"
        "v_add3_u32 v236, v243, v241, s87\n\t"
        "s_lshr_b32 s88, s88, 1\n\t"
        "s_sub_u32 s88, 2, s88\n\t"
        "v_and_b32 v243, 15, v244\n\t"
        "v_add_u32 v243, s88, v243\n\t"
        "v_lshlrev_b32 v241, 6, v243\n\t"
        "v_lshl_add_u32 v237, v242, 4, v241\n\t"
        "v_lshlrev_b32 v238, 4, v243\n\t"
        "v_bfe_u32 v240, v244, 3, 2\n\t"
        "v_and_b32 v241, 3, v244\n\t"
        "v_lshl_add_u32 v240, v240, 2, v241\n\t"
        "v_bfe_u32 v241, v244, 2, 1\n\t"
        "v_mul_u32_u24 v241, 9, v241\n\t"
        "v_add_u32 v241, v241, v240\n\t"
        "v_lshrrev_b32 v242, 5, v244\n\t"
        "v_mul_u32_u24 v242, 288, v242\n\t"
        "v_lshl_add_u32 v241, v241, 1, v242\n\t"
        "s_mul_i32 s87, s85, 648\n\t"
        "s_add_u32 s87, s87, 157824\n\t"
        "v_add_u32 v245, s87, v241\n\t"
        "v_mov_b32 v246, 0\n\t"
        "v_mov_b32 v247, 0\n\t"
        "v_mov_b32 v248, 0\n\t"
        "v_mov_b32 v249, 0\n\t"
        "v_mov_b32 v250, 0\n\t"
        "v_mov_b32 v251, 0\n\t"
        "v_mov_b32 v252, 0\n\t"
        "v_mov_b32 v253, 0\n\t"
        "v_mov_b32 v176, 0\n\t"
        "v_mov_b32 v177, 0\n\t"
        "v_mov_b32 v178, 0\n\t"
        "v_mov_b32 v179, 0\n\t"
        "v_mov_b32 v180, 0\n\t"
        "v_mov_b32 v181, 0\n\t"
        "v_mov_b32 v182, 0\n\t"
        "v_mov_b32 v183, 0\n\t"
        "v_mov_b32 v192, 0\n\t"
        "v_mov_b32 v193, 0\n\t"
        "v_mov_b32 v189, 0\n\t"
        "v_mov_b32 v190, 0\n\t"
        "v_mov_b32 v191, 0\n\t"
        "s_branch 52f\n\t"
        "51:\n\t"
        "s_add_u32 m0, s44, 0\n\t"
        "s_nop 0\n\t"
        "buffer_load_dwordx4 v0, s[64:67], s46 offen lds\n\t"
        "s_add_u32 m0, s44, 1024\n\t"
        "s_nop 0\n\t"
        "buffer_load_dwordx4 v1, s[64:67], s46 offen lds\n\t"
        "s_add_u32 m0, s44, 2048\n\t"
        "s_nop 0\n\t"
        "buffer_load_dwordx4 v2, s[64:67], s46 offen lds\n\t"
        "s_add_u32 m0, s44, 3072\n\t"
        "s_nop 0\n\t"
        "buffer_load_dwordx4 v3, s[64:67], s46 offen lds\n\t"
        "s_add_u32 s48, s48, 1\n\t"
        "s_cmp_eq_u32 s48, 4\n\t"
        "s_cselect_b32 s48, 0, s48\n\t"
        "s_lshl_b32 s46, s48, 7\n\t"
        "s_add_u32 m0, s63, 0\n\t"
        "s_nop 0\n\t"
        "buffer_load_dwordx4 v4, s[68:71], s47 offen lds\n\t"
        "s_add_u32 m0, s63, 1024\n\t"
        "s_nop 0\n\t"
        "buffer_load_dwordx4 v5, s[68:71], s47 offen lds\n\t"
        "s_add_u32 m0, s63, 2048\n\t"
        "s_nop 0\n\t"
        "buffer_load_dwordx4 v6, s[68:71], s47 offen lds\n\t"
        "s_add_u32 m0, s63, 3072\n\t"
        "s_nop 0\n\t"
        "buffer_load_dwordx4 v7, s[68:71], s47 offen lds\n\t"
        "s_add_u32 s62, s62, 1\n\t"
        "s_cmp_eq_u32 s62, 4\n\t"
        "s_cselect_b32 s62, 0, s62\n\t"
        "s_mul_i32 s47, s62, s45\n\t"
        "s_add_u32 m0, s44, 16384\n\t"
        "s_nop 0\n\t"
        "buffer_load_dwordx4 v0, s[64:67], s46 offen lds\n\t"
        "s_add_u32 m0, s44, 17408\n\t"
        "s_nop 0\n\t"
        "buffer_load_dwordx4 v1, s[64:67], s46 offen lds\n\t"
        "s_add_u32 m0, s44, 18432\n\t"
        "s_nop 0\n\t"
        "buffer_load_dwordx4 v2, s[64:67], s46 offen lds\n\t"
        "s_add_u32 m0, s44, 19456\n\t"
        "s_nop 0\n\t"
        "buffer_load_dwordx4 v3, s[64:67], s46 offen lds\n\t"
        "s_add_u32 s48, s48, 1\n\t"
        "s_cmp_eq_u32 s48, 4\n\t"
        "s_cselect_b32 s48, 0, s48\n\t"
        "s_lshl_b32 s46, s48, 7\n\t"
        "s_add_u32 m0, s63, 18464\n\t"
        "s_nop 0\n\t"
        "buffer_load_dwordx4 v4, s[68:71], s47 offen lds\n\t"
        "s_add_u32 m0, s63, 19488\n\t"
        "s_nop 0\n\t"
        "buffer_load_dwordx4 v5, s[68:71], s47 offen lds\n\t"
        "s_add_u32 m0, s63, 20512\n\t"
        "s_nop 0\n\t"
        "buffer_load_dwordx4 v6, s[68:71], s47 offen lds\n\t"
        "s_add_u32 m0, s63, 21536\n\t"
        "s_nop 0\n\t"
        "buffer_load_dwordx4 v7, s[68:71], s47 offen lds\n\t"
        "s_add_u32 s62, s62, 1\n\t"
        "s_cmp_eq_u32 s62, 4\n\t"
        "s_cselect_b32 s62, 0, s62\n\t"
        "s_mul_i32 s47, s62, s45\n\t"
        "s_setprio 0\n\t"
        "s_waitcnt vmcnt(8)\n\t"
        "s_barrier\n\t"
        "s_add_u32 m0, s44, 32768\n\t"
        "s_nop 0\n\t"
        "buffer_load_dwordx4 v0, s[64:67], s46 offen lds\n\t"
        "s_add_u32 m0, s44, 33792\n\t"
        "s_nop 0\n\t"
        "buffer_load_dwordx4 v1, s[64:67], s46 offen lds\n\t"
        "s_add_u32 m0, s44, 34816\n\t"
        "s_nop 0\n\t"
        "buffer_load_dwordx4 v2, s[64:67], s46 offen lds\n\t"
        "s_add_u32 m0, s44, 35840\n\t"
        "s_nop 0\n\t"
        "buffer_load_dwordx4 v3, s[64:67], s46 offen lds\n\t"
        "s_add_u32 s48, s48, 1\n\t"
        "s_cmp_eq_u32 s48, 4\n\t"
        "s_cselect_b32 s48, 0, s48\n\t"
        "s_lshl_b32 s46, s48, 7\n\t"
        "s_add_u32 m0, s63, 36928\n\t"
        "s_nop 0\n\t"
        "buffer_load_dwordx4 v4, s[68:71], s47 offen lds\n\t"
        "s_add_u32 m0, s63, 37952\n\t"
        "s_nop 0\n\t"
        "buffer_load_dwordx4 v5, s[68:71], s47 offen lds\n\t"
        "s_add_u32 m0, s63, 38976\n\t"
        "s_nop 0\n\t"
        "buffer_load_dwordx4 v6, s[68:71], s47 offen lds\n\t"
        "s_add_u32 m0, s63, 40000\n\t"
        "s_nop 0\n\t"
        "buffer_load_dwordx4 v7, s[68:71], s47 offen lds\n\t"
        "s_add_u32 s62, s62, 1\n\t"
        "s_cmp_eq_u32 s62, 4\n\t"
        "s_cselect_b32 s62, 0, s62\n\t"
        "s_mul_i32 s47, s62, s45\n\t"
        "s_waitcnt vmcnt(8)\n\t"
        "s_barrier\n\t"
        "s_add_u32 m0, s44, 49152\n\t"
        "s_nop 0\n\t"
        "buffer_load_dwordx4 v0, s[64:67], s46 offen lds\n\t"
        "s_add_u32 m0, s44, 50176\n\t"
        "s_nop 0\n\t"
        "buffer_load_dwordx4 v1, s[64:67], s46 offen lds\n\t"
        "s_add_u32 m0, s44, 51200\n\t"
        "s_nop 0\n\t"
        "buffer_load_dwordx4 v2, s[64:67], s46 offen lds\n\t"
        "s_add_u32 m0, s44, 52224\n\t"
        "s_nop 0\n\t"
        "buffer_load_dwordx4 v3, s[64:67], s46 offen lds\n\t"
        "s_mov_b32 s48, s40\n\t"
        "s_lshl_b32 s46, s48, 7\n\t"
        "s_add_u32 m0, s63, 55392\n\t"
        "s_nop 0\n\t"
        "buffer_load_dwordx4 v4, s[68:71], s47 offen lds\n\t"
        "s_add_u32 m0, s63, 56416\n\t"
        "s_nop 0\n\t"
        "buffer_load_dwordx4 v5, s[68:71], s47 offen lds\n\t"
        "s_add_u32 m0, s63, 57440\n\t"
        "s_nop 0\n\t"
        "buffer_load_dwordx4 v6, s[68:71], s47 offen lds\n\t"
        "s_add_u32 m0, s63, 58464\n\t"
        "s_nop 0\n\t"
        "buffer_load_dwordx4 v7, s[68:71], s47 offen lds\n\t"
        "s_mov_b32 s62, s40\n\t"
        "s_mul_i32 s47, s62, s45\n\t"
        "s_waitcnt vmcnt(8)\n\t"
        "s_barrier\n\t"
        "s_add_u32 m0, s44, 0\n\t"
        "s_nop 0\n\t"
        "buffer_load_dwordx4 v0, s[64:67], s46 offen lds\n\t"
        "s_add_u32 m0, s44, 1024\n\t"
        "s_nop 0\n\t"
        "buffer_load_dwordx4 v1, s[64:67], s46 offen lds\n\t"
        "s_add_u32 m0, s44, 2048\n\t"
        "s_nop 0\n\t"
        "buffer_load_dwordx4 v2, s[64:67], s46 offen lds\n\t"
        "s_add_u32 m0, s44, 3072\n\t"
        "s_nop 0\n\t"
        "buffer_load_dwordx4 v3, s[64:67], s46 offen lds\n\t"
        "s_add_u32 s48, s48, 1\n\t"
        "s_cmp_eq_u32 s48, 9\n\t"
        "s_cselect_b32 s48, 4, s48\n\t"
        "s_lshl_b32 s46, s48, 7\n\t"
        "s_add_u32 m0, s63, 0\n\t"
        "s_nop 0\n\t"
        "buffer_load_dwordx4 v4, s[68:71], s47 offen lds\n\t"
        "s_add_u32 m0, s63, 1024\n\t"
        "s_nop 0\n\t"
        "buffer_load_dwordx4 v5, s[68:71], s47 offen lds\n\t"
        "s_add_u32 m0, s63, 2048\n\t"
        "s_nop 0\n\t"
        "buffer_load_dwordx4 v6, s[68:71], s47 offen lds\n\t"
        "s_add_u32 m0, s63, 3072\n\t"
        "s_nop 0\n\t"
        "buffer_load_dwordx4 v7, s[68:71], s47 offen lds\n\t"
        "s_add_u32 s62, s62, 1\n\t"
        "s_cmp_eq_u32 s62, 9\n\t"
        "s_cselect_b32 s62, 4, s62\n\t"
        "s_mul_i32 s47, s62, s45\n\t"
        "s_waitcnt vmcnt(8)\n\t"
        "s_barrier\n\t"
        "s_add_u32 m0, s44, 16384\n\t"
        "s_nop 0\n\t"
        "buffer_load_dwordx4 v0, s[64:67], s46 offen lds\n\t"
        "s_add_u32 m0, s44, 17408\n\t"
        "s_nop 0\n\t"
        "buffer_load_dwordx4 v1, s[64:67], s46 offen lds\n\t"
        "s_add_u32 m0, s44, 18432\n\t"
        "s_nop 0\n\t"
        "buffer_load_dwordx4 v2, s[64:67], s46 offen lds\n\t"
        "s_add_u32 m0, s44, 19456\n\t"
        "s_nop 0\n\t"
        "buffer_load_dwordx4 v3, s[64:67], s46 offen lds\n\t"
        "s_add_u32 s48, s48, 1\n\t"
        "s_cmp_eq_u32 s48, 9\n\t"
        "s_cselect_b32 s48, 4, s48\n\t"
        "s_lshl_b32 s46, s48, 7\n\t"
        "s_add_u32 m0, s63, 18464\n\t"
        "s_nop 0\n\t"
        "buffer_load_dwordx4 v4, s[68:71], s47 offen lds\n\t"
        "s_add_u32 m0, s63, 19488\n\t"
        "s_nop 0\n\t"
        "buffer_load_dwordx4 v5, s[68:71], s47 offen lds\n\t"
        "s_add_u32 m0, s63, 20512\n\t"
        "s_nop 0\n\t"
        "buffer_load_dwordx4 v6, s[68:71], s47 offen lds\n\t"
        "s_add_u32 m0, s63, 21536\n\t"
        "s_nop 0\n\t"
        "buffer_load_dwordx4 v7, s[68:71], s47 offen lds\n\t"
        "s_add_u32 s62, s62, 1\n\t"
        "s_cmp_eq_u32 s62, 9\n\t"
        "s_cselect_b32 s62, 4, s62\n\t"
        "s_mul_i32 s47, s62, s45\n\t"
        "s_waitcnt vmcnt(8)\n\t"
        "s_barrier\n\t"
        "s_add_u32 m0, s44, 32768\n\t"
        "s_nop 0\n\t"
        "buffer_load_dwordx4 v0, s[64:67], s46 offen lds\n\t"
        "s_add_u32 m0, s44, 33792\n\t"
        "s_nop 0\n\t"
        "buffer_load_dwordx4 v1, s[64:67], s46 offen lds\n\t"
        "s_add_u32 m0, s44, 34816\n\t"
        "s_nop 0\n\t"
        "buffer_load_dwordx4 v2, s[64:67], s46 offen lds\n\t"
        "s_add_u32 m0, s44, 35840\n\t"
        "s_nop 0\n\t"
        "buffer_load_dwordx4 v3, s[64:67], s46 offen lds\n\t"
        "s_add_u32 s48, s48, 1\n\t"
        "s_cmp_eq_u32 s48, 9\n\t"
        "s_cselect_b32 s48, 4, s48\n\t"
        "s_lshl_b32 s46, s48, 7\n\t"
        "s_add_u32 m0, s63, 36928\n\t"
        "s_nop 0\n\t"
        "buffer_load_dwordx4 v4, s[68:71], s47 offen lds\n\t"
        "s_add_u32 m0, s63, 37952\n\t"
        "s_nop 0\n\t"
        "buffer_load_dwordx4 v5, s[68:71], s47 offen lds\n\t"
        "s_add_u32 m0, s63, 38976\n\t"
        "s_nop 0\n\t"
        "buffer_load_dwordx4 v6, s[68:71], s47 offen lds\n\t"
        "s_add_u32 m0, s63, 40000\n\t"
        "s_nop 0\n\t"
        "buffer_load_dwordx4 v7, s[68:71], s47 offen lds\n\t"
        "s_add_u32 s62, s62, 1\n\t"
        "s_cmp_eq_u32 s62, 9\n\t"
        "s_cselect_b32 s62, 4, s62\n\t"
        "s_mul_i32 s47, s62, s45\n\t"
        "s_waitcnt vmcnt(8)\n\t"
        "s_barrier\n\t"
        "s_add_u32 m0, s44, 49152\n\t"
        "s_nop 0\n\t"
        "buffer_load_dwordx4 v0, s[64:67], s46 offen lds\n\t"
        "s_add_u32 m0, s44, 50176\n\t"
        "s_nop 0\n\t"
        "buffer_load_dwordx4 v1, s[64:67], s46 offen lds\n\t"
        "s_add_u32 m0, s44, 51200\n\t"
        "s_nop 0\n\t"
        "buffer_load_dwordx4 v2, s[64:67], s46 offen lds\n\t"
        "s_add_u32 m0, s44, 52224\n\t"
        "s_nop 0\n\t"
        "buffer_load_dwordx4 v3, s[64:67], s46 offen lds\n\t"
        "s_add_u32 s48, s48, 1\n\t"
        "s_cmp_eq_u32 s48, 9\n\t"
        "s_cselect_b32 s48, 4, s48\n\t"
        "s_lshl_b32 s46, s48, 7\n\t"
        "s_add_u32 m0, s63, 55392\n\t"
        "s_nop 0\n\t"
        "buffer_load_dwordx4 v4, s[68:71], s47 offen lds\n\t"
        "s_add_u32 m0, s63, 56416\n\t"
        "s_nop 0\n\t"
        "buffer_load_dwordx4 v5, s[68:71], s47 offen lds\n\t"
        "s_add_u32 m0, s63, 57440\n\t"
        "s_nop 0\n\t"
        "buffer_load_dwordx4 v6, s[68:71], s47 offen lds\n\t"
        "s_add_u32 m0, s63, 58464\n\t"
        "s_nop 0\n\t"
        "buffer_load_dwordx4 v7, s[68:71], s47 offen lds\n\t"
        "s_add_u32 s62, s62, 1\n\t"
        "s_cmp_eq_u32 s62, 9\n\t"
        "s_cselect_b32 s62, 4, s62\n\t"
        "s_mul_i32 s47, s62, s45\n\t"
        "s_waitcnt vmcnt(8)\n\t"
        "s_barrier\n\t"
        "s_add_u32 m0, s44, 0\n\t"
        "s_nop 0\n\t"
        "buffer_load_dwordx4 v0, s[64:67], s46 offen lds\n\t"
        "s_add_u32 m0, s44, 1024\n\t"
        "s_nop 0\n\t"
        "buffer_load_dwordx4 v1, s[64:67], s46 offen lds\n\t"
        "s_add_u32 m0, s44, 2048\n\t"
        "s_nop 0\n\t"
        "buffer_load_dwordx4 v2, s[64:67], s46 offen lds\n\t"
        "s_add_u32 m0, s44, 3072\n\t"
        "s_nop 0\n\t"
        "buffer_load_dwordx4 v3, s[64:67], s46 offen lds\n\t"
        "s_add_u32 m0, s63, 0\n\t"
        "s_nop 0\n\t"
        "buffer_load_dwordx4 v4, s[68:71], s47 offen lds\n\t"
        "s_add_u32 m0, s63, 1024\n\t"
        "s_nop 0\n\t"
        "buffer_load_dwordx4 v5, s[68:71], s47 offen lds\n\t"
        "s_add_u32 m0, s63, 2048\n\t"
        "s_nop 0\n\t"
        "buffer_load_dwordx4 v6, s[68:71], s47 offen lds\n\t"
        "s_add_u32 m0, s63, 3072\n\t"
        "s_nop 0\n\t"
        "buffer_load_dwordx4 v7, s[68:71], s47 offen lds\n\t"
        "s_cmp_eq_u32 %[pw], 3\n\t"
        "s_cbranch_scc1 54f\n\t"
        "s_mul_i32 s82, s82, 2592\n\t"
        "s_lshl_b32 s83, %[pw], 10\n\t"
        "s_add_u32 s82, s82, s83\n\t"
        "s_add_u32 s80, s80, s82\n\t"
        "s_addc_u32 s81, s81, 0\n\t"
        "s_add_u32 m0, s83, 157824\n\t"
        "v_lshlrev_b32 v40, 4, v244\n\t"
        "s_cmp_eq_u32 %[pw], 2\n\t"
        "s_cselect_b32 s82, 3, -1\n\t"
        "s_mov_b32 exec_hi, s82\n\t"
        "global_load_lds_dwordx4 v40, s[80:81]\n\t"
        "s_mov_b32 exec_hi, -1\n\t"
        "54:\n\t"
        "s_waitcnt vmcnt(8)\n\t"
        "s_barrier\n\t"
        "s_waitcnt vmcnt(0)\n\t"
        "s_barrier\n\t"
        "s_add_u32 s84, %[pw], 4\n\t"
        "s_and_b32 s85, s84, 3\n\t"
        "s_lshr_b32 s86, s84, 2\n\t"
        "v_and_b32 v240, 31, v244\n\t"
        "v_lshrrev_b32 v241, 5, v244\n\t"
        "s_mul_i32 s87, s86, 18944\n\t"
        "s_mul_i32 s88, s85, 36\n\t"
        "s_add_u32 s87, s87, s88\n\t"
        "s_add_u32 s87, s87, 16384\n\t"
        "v_mul_u32_u24 v242, 296, v240\n\t"
        "v_lshlrev_b32 v243, 4, v241\n\t"
        "v_add3_u32 v228, v242, v243, s87\n\t"
        "v_add_u32 v230, s87, v242\n\t"
        "v_add_u32 v229, 9472, v228\n\t"
        "s_mul_i32 s87, s86, 22528\n\t"
        "s_mul_i32 s88, s85, 80\n\t"
        "s_add_u32 s87, s87, s88\n\t"
        "s_add_u32 s87, s87, 65536\n\t"
        "v_mul_u32_u24 v242, 352, v240\n\t"
        "v_add3_u32 v231, v242, v243, s87\n\t"
        "v_mul_u32_u24 v243, 12, v241\n\t"
        "v_sub_u32 v232, v231, v243\n\t"
        "v_add_u32 v232, 64, v232\n\t"
        "v_lshlrev_b32 v233, 4, v244\n\t"
        "v_lshlrev_b32 v234, 2, v244\n\t"
        "v_bfe_u32 v240, v244, 2, 2\n\t"
        "v_and_b32 v241, 3, v244\n\t"
        "v_lshrrev_b32 v242, 4, v244\n\t"
        "v_lshl_add_u32 v240, v242, 2, v240\n\t"
        "v_lshlrev_b32 v241, 3, v241\n\t"
        "v_mul_u32_u24 v243, 352, v240\n\t"
        "v_add3_u32 v235, v243, v241, s87\n\t"
        "s_mul_i32 s87, s86, 18944\n\t"
        "s_mul_i32 s88, s85, 36\n\t"
        "s_add_u32 s87, s87, s88\n\t"
        "s_and_b32 s88, s85, 1\n\t"
        "s_lshl_b32 s88, s88, 2\n\t"
        "s_sub_u32 s87, s87, s88\n\t"
        "s_add_u32 s87, s87, 16528\n\t"
        "v_mul_u32_u24 v243, 296, v240\n\t"
        "v_add3_u32 v236, v243, v241, s87\n\t"
        "s_lshr_b32 s88, s88, 1\n\t"
        "s_sub_u32 s88, 2, s88\n\t"
        "v_and_b32 v243, 15, v244\n\t"
        "v_add_u32 v243, s88, v243\n\t"
        "v_lshlrev_b32 v241, 6, v243\n\t"
        "v_lshl_add_u32 v237, v242, 4, v241\n\t"
        "v_lshlrev_b32 v238, 4, v243\n\t"
        "v_bfe_u32 v240, v244, 3, 2\n\t"
        "v_and_b32 v241, 3, v244\n\t"
        "v_lshl_add_u32 v240, v240, 2, v241\n\t"
        "v_bfe_u32 v241, v244, 2, 1\n\t"
        "v_mul_u32_u24 v241, 9, v241\n\t"
        "v_add_u32 v241, v241, v240\n\t"
        "v_lshrrev_b32 v242, 5, v244\n\t"
        "v_mul_u32_u24 v242, 288, v242\n\t"
        "v_lshl_add_u32 v241, v241, 1, v242\n\t"
        "s_mul_i32 s87, s85, 648\n\t"
        "s_add_u32 s87, s87, 157824\n\t"
        "v_add_u32 v245, s87, v241\n\t"
        "v_mov_b32 v246, 0\n\t"
        "v_mov_b32 v247, 0\n\t"
        "v_mov_b32 v248, 0\n\t"
        "v_mov_b32 v249, 0\n\t"
        "v_mov_b32 v250, 0\n\t"
        "v_mov_b32 v251, 0\n\t"
        "v_mov_b32 v252, 0\n\t"
        "v_mov_b32 v253, 0\n\t"
        "v_mov_b32 v176, 0\n\t"
        "v_mov_b32 v177, 0\n\t"
        "v_mov_b32 v178, 0\n\t"
        "v_mov_b32 v179, 0\n\t"
        "v_mov_b32 v180, 0\n\t"
        "v_mov_b32 v181, 0\n\t"
        "v_mov_b32 v182, 0\n\t"
        "v_mov_b32 v183, 0\n\t"
        "v_mov_b32 v192, 0\n\t"
        "v_mov_b32 v193, 0\n\t"
        "v_mov_b32 v189, 0\n\t"
        "v_mov_b32 v190, 0\n\t"
        "v_mov_b32 v191, 0\n\t"
        "52:\n\t"
        "s_mov_b32 exec_lo, 0x0011ffff\n\t"
        "s_mov_b32 exec_hi, 0x0011ffff\n\t"
        "ds_read_u16 v176, v245 offset:0\n\t"
        "ds_read_u16 v177, v245 offset:36\n\t"
        "ds_read_u16 v178, v245 offset:72\n\t"
        "ds_read_u16 v179, v245 offset:108\n\t"
        "ds_read_u16 v180, v245 offset:144\n\t"
        "ds_read_u16 v181, v245 offset:180\n\t"
        "ds_read_u16 v182, v245 offset:216\n\t"
        "ds_read_u16 v183, v245 offset:252\n\t"
        "s_mov_b32 exec_hi, 0\n\t"
        "ds_read_u16 v192, v245 offset:576\n\t"
        "ds_read_u16 v193, v245 offset:612\n\t"
        "s_mov_b64 exec, -1\n\t"
        "s_waitcnt lgkmcnt(0)\n\t"
        "v_lshl_or_b32 v184, v177, 16, v176\n\t"
        "v_lshl_or_b32 v185, v179, 16, v178\n\t"
        "v_lshl_or_b32 v186, v181, 16, v180\n\t"
        "v_lshl_or_b32 v187, v183, 16, v182\n\t"
        "v_lshl_or_b32 v188, v193, 16, v192\n\t"
            : "=&{v[184:187]}"(PA0), "=&{v[188:191]}"(PA1), "=&{v[228:231]}"(EP0), "=&{v[232:235]}"(EP1), "=&{v[236:239]}"(EP2), "=&{v[246:249]}"(EZ0), "=&{v[250:253]}"(EZ1), "=&{v245}"(PHA),
              "=&{v[160:175]}"(WO), "=&{v[216:217]}"(V6)
            : "{v244}"(lane), [karg] "s"(__builtin_amdgcn_kernarg_segment_ptr()), [b] "s"(b), [pw] "s"(w - 4)
            : "v0", "v1", "v2", "v3", "v4", "v5", "v6", "v7", "v8", "v9", "v10", "v11", "v12", "v13", "v14", "v15", "v16", "v17", "v18", "v19", "v20", "v21", "v22", "v23", "v24", "v25", "v26", "v27", "v28", "v29", "v30", "v31", "v32", "v33", "v34", "v35", "v36", "v37", "v38", "v39", "v40", "v41", "v42", "v43", "v44", "v45", "v46", "v240", "v241", "v242", "v243", "v176", "v177", "v178", "v179", "v180", "v181", "v182", "v183", "v192", "v193", "s40", "s41", "s42", "s43", "s44", "s45", "s46", "s47", "s48", "s49", "s50", "s51", "s52", "s53", "s54", "s55", "s56", "s57", "s58", "s59", "s60", "s61", "s62", "s63", "s64", "s65", "s66", "s67", "s68", "s69", "s70", "s71", "s72", "s73", "s74", "s75", "s76", "s77", "s78", "s79", "s80", "s81", "s82", "s83", "s84", "s85", "s86", "s87", "s88", "scc", "vcc", "m0", "memory");

    }
    const int xcd = b & 7, idx = b >> 3, jj = idx >> 2;
    const int rb = (xcd >> 1) * 4 + (idx & 3);
    const bool isQ = jj >= 4; const int j = 4 * (xcd & 1) + (jj & 3);
    const int cb = __builtin_amdgcn_readfirstlane(isQ ? 8 + j : j), isq = __builtin_amdgcn_readfirstlane(cb >> 3);
    const int rg = w & 3, half = w >> 2;
    const int hphi = 4 * j + rg;
    const int ver = __builtin_amdgcn_readfirstlane(cb >= 8 && cb <= 10 ? cb - 7 : 0);
    u32x4 EA4 = {0, 0, 0, 0}; int blk = 0;
    if (ver && w < 4) {
        const int win[3] = {4, 5, 0}, tt = 1 * jj + 2 * (idx & 3);
        int bl[5];
        #pragma unroll
        for (int t = 0; t < 5; ++t) {
            const int p = min((w + 4 * t) >> 1, 8);
            int c = p < win[0] ? (tt + p) % win[0] : (win[2] == 0 || p < win[0] + win[1]) ? win[0] + (tt + p - win[0]) % (win[1] > 0 ? win[1] : 1) : win[0] + win[1] + (tt + p - win[0] - win[1]) % (win[2] > 0 ? win[2] : 1);
            bl[t] = 2 * c + (w & 1);
        }
        const int rot = __builtin_amdgcn_readfirstlane(bl[0]);
        blk = __builtin_amdgcn_readfirstlane(bl[1] | (bl[2] << 8) | (bl[3] << 16) | (bl[4] << 24));
        asm volatile(
        "s_load_dwordx4 s[40:43], %[karg], 0x28\n\t"
        "s_load_dwordx2 s[44:45], %[karg], 0x40\n\t"
        "v_lshrrev_b32 v0, 1, v244\n\t"
        "v_and_b32 v1, 1, v244\n\t"
        "v_and_b32 v2, 0x13, v0\n\t"
        "v_bfe_u32 v3, v0, 2, 1\n\t"
        "v_lshl_or_b32 v2, v3, 3, v2\n\t"
        "v_bfe_u32 v3, v0, 3, 1\n\t"
        "v_lshl_or_b32 v2, v3, 2, v2\n\t"
        "v_mul_u32_u24 v4, 36, v2\n\t"
        "s_mov_b32 s46, %[rot]\n\t"
        "s_cmp_eq_u32 %[cb], 10\n\t"
        "s_cbranch_scc1 42f\n\t"
        "v_lshl_add_u32 v4, v1, 4, v4\n\t"
        "s_lshl_b32 s48, %[w], 10\n\t"
        "s_add_u32 s48, s48, 139392\n\t"
        "s_cmp_eq_u32 %[cb], 9\n\t"
        "s_waitcnt lgkmcnt(0)\n\t"
        "s_cselect_b32 s40, s42, s40\n\t"
        "s_cselect_b32 s41, s43, s41\n\t"
        "v_mov_b32 v221, s41\n\t"
        "v_add_co_u32 v220, vcc, s40, v4\n\t"
        "v_addc_co_u32 v221, vcc, 0, v221, vcc\n\t"
        "s_mul_i32 s47, s46, 1152\n\t"
        "s_add_u32 m0, s48, 0\n\t"
        "v_add_co_u32 v10, vcc, s47, v220\n\t"
        "v_addc_co_u32 v11, vcc, 0, v221, vcc\n\t"
        "global_load_lds_dwordx4 v[10:11], off\n\t"
        "s_branch 43f\n\t"
        "42:\n\t"
        "v_add_u32 v4, 32, v4\n\t"
        "s_lshl_b32 s48, %[w], 8\n\t"
        "s_add_u32 s48, s48, 139392\n\t"
        "s_add_u32 s49, s48, 8192\n\t"
        "v_cmp_eq_u32 vcc, 1, v1\n\t"
        "s_mov_b32 s50, 0x55555555\n\t"
        "s_mov_b32 s51, s50\n\t"
        "v_lshlrev_b32 v2, 1, v2\n\t"
        "s_waitcnt lgkmcnt(0)\n\t"
        "v_mov_b32 v220, s40\n\t"
        "v_mov_b32 v221, s41\n\t"
        "v_mov_b32 v8, s42\n\t"
        "v_mov_b32 v9, s43\n\t"
        "v_cndmask_b32 v220, v220, v8, vcc\n\t"
        "v_cndmask_b32 v221, v221, v9, vcc\n\t"
        "v_add_co_u32 v220, vcc, v220, v4\n\t"
        "v_addc_co_u32 v221, vcc, 0, v221, vcc\n\t"
        "v_mov_b32 v223, s45\n\t"
        "v_add_co_u32 v222, vcc, s44, v2\n\t"
        "v_addc_co_u32 v223, vcc, 0, v223, vcc\n\t"
        "s_mul_i32 s47, s46, 1152\n\t"
        "s_add_u32 m0, s48, 0\n\t"
        "v_add_co_u32 v10, vcc, s47, v220\n\t"
        "v_addc_co_u32 v11, vcc, 0, v221, vcc\n\t"
        "global_load_lds_dword v[10:11], off\n\t"
        "s_mul_i32 s47, s46, 64\n\t"
        "s_add_u32 m0, s49, 0\n\t"
        "s_mov_b64 exec, s[50:51]\n\t"
        "v_add_co_u32 v10, vcc, s47, v222\n\t"
        "v_addc_co_u32 v11, vcc, 0, v223, vcc\n\t"
        "global_load_lds_ushort v[10:11], off\n\t"
        "s_mov_b64 exec, -1\n\t"
        "43:\n\t"
            : "=&{v[220:223]}"(EA4)
            : "{v244}"(lane), [karg] "s"(__builtin_amdgcn_kernarg_segment_ptr()), [cb] "s"(cb), [w] "s"(w), [rot] "s"(rot)
            : "v0", "v1", "v2", "v3", "v4", "v5", "v6", "v7", "v8", "v9", "v10", "v11", "s40", "s41", "s42", "s43", "s44", "s45", "s46", "s47", "s48", "s49", "s50", "s51", "scc", "vcc", "m0", "memory");
    }
    const int un = hphi * 64 + rb * 4 + 2 * half;
    u64 pqb = uni64(ws + OFF_PQ + (size_t)un * 2304), qmb = uni64(ws + OFF_QM + (size_t)un * 1024), qtb = uni64(ws + OFF_QT + (size_t)un * 128);
    u64 kvp = uni64(ws + OFF_KVP + (size_t)(hphi * 16 + rb) * 2736);
    asm volatile("" : "+s"(pqb), "+s"(qmb), "+s"(qtb), "+s"(kvp));
    const u64 wsb = uni64(ws);
    const u64 wom = uni64v(w == 0 ? ~0ull : w == 1 ? 0xfull : 0ull);
    if (w < 4) {
        const int c16 = lane & 15, g = lane >> 4;
        const int rh = w >> 1, ch = w & 1;
        u32x4 V1, V2, V3, V4, V5;
        const int q = (lane >> 2) & 3, pp = lane & 3, pos = 16 * (g >> 1) + 4 * (g & 1) + q, wpos = 16 * (g >> 1) + 8 * (g & 1) + 2 * q;
        const int swz = (g ^ ((c16 >> 1) & 7)) << 4;
        V1[0] = (64 * rh + 32 * ch + c16) * 128 + swz;
        V1[1] = (64 * rh + 32 * (ch ^ 1) + c16) * 128 + swz;
        V1[2] = K1_W + wpos * 144 + (K1_WSEG + 16) * ch + 8 * pp;
        V1[3] = K1_W + wpos * 144 + (pp < 2 ? 128 + 8 * pp : K1_WSEG + 8 * (pp - 2));
        u32 e10 = K1_E + pos * 8 + (pp == 1 ? 8192 : 0), e8 = K1_E + pos * 32 + 8 * pp;
        asm volatile("" : "+v"(e10), "+v"(e8));
        V2[0] = cb == 10 ? e10 : e8;
        V2[2] = 0;
        {
            const int kt = rb * 4 + 2 * rh + (1 - ch);
            const int kh = kt >> 5, m = kt & 31, m1 = (m - 1) & 31;
            const u32 st1 = KV_ST0 + (m1 >> 2) * KV_STS, z = m == 0;
            u32 om0 = kh * 1024, om1 = st1 + kh * 4096 + (m1 & 3) * 1024, ot0 = 2048 + kh * 128, ot1 = st1 + 17920 + kh * 512 + (m1 & 3) * 128;
            asm volatile("" : "+s"(om0), "+s"(om1), "+s"(ot0), "+s"(ot1));
            const u32 omain = z ? om0 : om1, otail = z ? ot0 : ot1;
            const u32 ov = KV_ST0 + (m >> 2) * KV_STS + 8192 + kh * 4864 + (m & 3) * 1216;
            u32 a8 = OFF_KV + omain + ((g >> 1) * 32 + c16) * 16 + (g & 1) * 8;
            u32 a9 = OFF_KV + ov + (g >> 1) * 304 + swap23(c16) * 16 + (g & 1) * 8;
            u32 at = OFF_KV + otail + c16 * 4, ag = OFF_G + (rb * 128 + 64 * rh + 32 * (1 - ch) + c16) * 4;
            u32 xv = OFF_KV + ov + (c16 >> 3) * 304 + 256 + (c16 & 7) * 2;
            asm volatile("" : "+v"(a8), "+v"(a9), "+v"(at), "+v"(ag), "+v"(xv));
            u32 a10 = g == 0 ? at : ag;
            u32 xa = cb == 8 ? a8 : cb == 9 ? a9 : a10;
            V2[1] = xa; V2[3] = xv;
        }
        const float cq = isQ ? 1.4426950408889634f * 0.23570226039551584f : 1.f;
        {
            const u32 t1 = K1_T1 + (64 * rh + c16) * K1_T1P + g * 8;
            V5[0] = t1 + ch * (32 * K1_T1P + 160);
            V5[1] = t1 + ch * (160 - 32 * K1_T1P);
            V5[2] = __float_as_uint(g >= 2 ? cq : 1.f);
            V5[3] = t1 + ch * 32 * K1_T1P + 128;
        }
        const u32 sc0 = __builtin_amdgcn_readfirstlane(__float_as_uint(ch ? cq : 1.f));
        const int br = b >> 7, wi = b & 127;
        #pragma unroll
        for (int r = 0; r < 2; ++r) {
            int t = r == 0 ? tid : (tid < 68 ? 256 + tid : 256);
            int unit = t < 288 ? wi : 128 + (wi >> 3), r2 = t < 288 ? t : (wi & 7) * 36 + (t - 288);
            int ct = (unit * 57) >> 10, ks = unit - 18 * ct;
            int b5, gg, cc;
            if (r2 < 256) { b5 = r2 >> 6; gg = (r2 >> 4) & 3; cc = r2 & 15; } else { b5 = 4; gg = (r2 - 256) >> 3; cc = (r2 - 256) & 7; }
            int oo = 4 * ks + gg, hq = (oo * 57) >> 9, tt = oo - 9 * hq;
            int n = 72 * ct + (b5 < 4 ? 32 * (b5 >> 1) + 8 * (cc >> 2) + 4 * (b5 & 1) + (cc & 3) : 64 + cc);
            int r0 = tt < 8 ? (4 * hq + (tt >> 1)) * 18 + 8 * (tt & 1) : 4 * hq * 18 + 16, st = tt < 8 ? 2 : 18;
            #pragma unroll
            for (int m = 0; m < 4; ++m) { u32 v = (r0 + m * st) * 1152 + n * 2; if (r == 0) V3[m] = v; else V4[m] = v; }
            V6[r] = OFF_WT + (ct * 36 + 18 * br + ks) * 4608 + r2 * 16;
        }
        const u64 wob = uni64(br ? woB : woA);
        asm volatile(
        "s_cmp_eq_u32 %[ver], 1\n\t"
        "s_cbranch_scc1 61f\n\t"
        "s_cmp_eq_u32 %[ver], 2\n\t"
        "s_cbranch_scc1 62f\n\t"
        "s_cmp_eq_u32 %[ver], 3\n\t"
        "s_cbranch_scc1 63f\n\t"
        "v_xor_b32 v218, 64, v196\n\t"
        "v_xor_b32 v219, 64, v197\n\t"
        "s_mov_b32 s44, %[sc0]\n\t"
        "s_mov_b32 s45, %[sc0]\n\t"
        "s_and_b32 s46, %[w], 3\n\t"
        "s_lshr_b32 s47, %[w], 2\n\t"
        "v_and_b32 v240, 31, v244\n\t"
        "v_lshrrev_b32 v241, 5, v244\n\t"
        "s_mul_i32 s48, s47, 18944\n\t"
        "s_mul_i32 s49, s46, 36\n\t"
        "s_add_u32 s48, s48, s49\n\t"
        "s_add_u32 s48, s48, 16384\n\t"
        "v_mul_u32_u24 v242, 296, v240\n\t"
        "v_lshlrev_b32 v243, 4, v241\n\t"
        "v_add3_u32 v228, v242, v243, s48\n\t"
        "v_add_u32 v230, s48, v242\n\t"
        "v_add_u32 v229, 9472, v228\n\t"
        "s_mul_i32 s48, s47, 22528\n\t"
        "s_mul_i32 s49, s46, 80\n\t"
        "s_add_u32 s48, s48, s49\n\t"
        "s_add_u32 s48, s48, 65536\n\t"
        "v_mul_u32_u24 v242, 352, v240\n\t"
        "v_add3_u32 v231, v242, v243, s48\n\t"
        "v_mul_u32_u24 v243, 12, v241\n\t"
        "v_sub_u32 v232, v231, v243\n\t"
        "v_add_u32 v232, 64, v232\n\t"
        "v_lshlrev_b32 v233, 4, v244\n\t"
        "v_lshlrev_b32 v234, 2, v244\n\t"
        "v_bfe_u32 v240, v244, 2, 2\n\t"
        "v_and_b32 v241, 3, v244\n\t"
        "v_lshrrev_b32 v242, 4, v244\n\t"
        "v_lshl_add_u32 v240, v242, 2, v240\n\t"
        "v_lshlrev_b32 v241, 3, v241\n\t"
        "v_mul_u32_u24 v243, 352, v240\n\t"
        "v_add3_u32 v235, v243, v241, s48\n\t"
        "s_mul_i32 s48, s47, 18944\n\t"
        "s_mul_i32 s49, s46, 36\n\t"
        "s_add_u32 s48, s48, s49\n\t"
        "s_and_b32 s49, s46, 1\n\t"
        "s_lshl_b32 s49, s49, 2\n\t"
        "s_sub_u32 s48, s48, s49\n\t"
        "s_add_u32 s48, s48, 16528\n\t"
        "v_mul_u32_u24 v243, 296, v240\n\t"
        "v_add3_u32 v236, v243, v241, s48\n\t"
        "s_lshr_b32 s49, s49, 1\n\t"
        "s_sub_u32 s49, 2, s49\n\t"
        "v_and_b32 v243, 15, v244\n\t"
        "v_add_u32 v243, s49, v243\n\t"
        "v_lshlrev_b32 v241, 6, v243\n\t"
        "v_lshl_add_u32 v237, v242, 4, v241\n\t"
        "v_lshlrev_b32 v238, 4, v243\n\t"
        "v_bfe_u32 v240, v244, 3, 2\n\t"
        "v_and_b32 v241, 3, v244\n\t"
        "v_lshl_add_u32 v240, v240, 2, v241\n\t"
        "v_bfe_u32 v241, v244, 2, 1\n\t"
        "v_mul_u32_u24 v241, 9, v241\n\t"
        "v_add_u32 v241, v241, v240\n\t"
        "v_lshrrev_b32 v242, 5, v244\n\t"
        "v_mul_u32_u24 v242, 288, v242\n\t"
        "v_lshl_add_u32 v241, v241, 1, v242\n\t"
        "s_mul_i32 s48, s46, 648\n\t"
        "s_add_u32 s48, s48, 157824\n\t"
        "v_add_u32 v245, s48, v241\n\t"
        "v_mov_b32 v246, 0\n\t"
        "v_mov_b32 v247, 0\n\t"
        "v_mov_b32 v248, 0\n\t"
        "v_mov_b32 v249, 0\n\t"
        "v_mov_b32 v250, 0\n\t"
        "v_mov_b32 v251, 0\n\t"
        "v_mov_b32 v252, 0\n\t"
        "v_mov_b32 v253, 0\n\t"
        "v_mov_b32 v176, 0\n\t"
        "v_mov_b32 v177, 0\n\t"
        "v_mov_b32 v178, 0\n\t"
        "v_mov_b32 v179, 0\n\t"
        "v_mov_b32 v180, 0\n\t"
        "v_mov_b32 v181, 0\n\t"
        "v_mov_b32 v182, 0\n\t"
        "v_mov_b32 v183, 0\n\t"
        "v_mov_b32 v192, 0\n\t"
        "v_mov_b32 v193, 0\n\t"
        "v_mov_b32 v189, 0\n\t"
        "v_mov_b32 v190, 0\n\t"
        "v_mov_b32 v191, 0\n\t"
        "s_barrier\n\t"
        "ds_read_b128 v[80:83], v196 offset:0\n\t"
        "ds_read_b64_tr_b16 v[96:97], v198 offset:0\n\t"
        "ds_read_b64_tr_b16 v[98:99], v198 offset:144\n\t"
        "ds_read_b128 v[84:87], v196 offset:2048\n\t"
        "ds_read_b128 v[88:91], v197 offset:0\n\t"
        "ds_read_b128 v[92:95], v197 offset:2048\n\t"
        "ds_read_b64_tr_b16 v[100:101], v198 offset:32\n\t"
        "ds_read_b64_tr_b16 v[102:103], v198 offset:176\n\t"
        "ds_read_b64_tr_b16 v[104:105], v198 offset:64\n\t"
        "ds_read_b64_tr_b16 v[106:107], v198 offset:208\n\t"
        "ds_read_b64_tr_b16 v[108:109], v198 offset:96\n\t"
        "ds_read_b64_tr_b16 v[110:111], v198 offset:240\n\t"
        "ds_read_b64_tr_b16 v[112:113], v199 offset:0\n\t"
        "ds_read_b64_tr_b16 v[114:115], v199 offset:144\n\t"
        "s_waitcnt lgkmcnt(0)\n\t"
        "v_mfma_f32_16x16x32_bf16 v[0:3], v[96:99], v[80:83], 0\n\t"
        "ds_read_b128 v[120:123], v218 offset:0\n\t"
        "v_mfma_f32_16x16x32_bf16 v[16:19], v[96:99], v[84:87], 0\n\t"
        "ds_read_b64_tr_b16 v[136:137], v198 offset:4608\n\t"
        "v_mfma_f32_16x16x32_bf16 v[32:35], v[96:99], v[88:91], 0\n\t"
        "ds_read_b64_tr_b16 v[138:139], v198 offset:4752\n\t"
        "v_mfma_f32_16x16x32_bf16 v[48:51], v[96:99], v[92:95], 0\n\t"
        "ds_read_b128 v[124:127], v218 offset:2048\n\t"
        "v_mfma_f32_16x16x32_bf16 v[4:7], v[100:103], v[80:83], 0\n\t"
        "v_mfma_f32_16x16x32_bf16 v[20:23], v[100:103], v[84:87], 0\n\t"
        "ds_read_b128 v[128:131], v219 offset:0\n\t"
        "v_mfma_f32_16x16x32_bf16 v[36:39], v[100:103], v[88:91], 0\n\t"
        "ds_read_b128 v[132:135], v219 offset:2048\n\t"
        "v_mfma_f32_16x16x32_bf16 v[52:55], v[100:103], v[92:95], 0\n\t"
        "ds_read_b64_tr_b16 v[140:141], v198 offset:4640\n\t"
        "v_mfma_f32_16x16x32_bf16 v[8:11], v[104:107], v[80:83], 0\n\t"
        "v_mfma_f32_16x16x32_bf16 v[24:27], v[104:107], v[84:87], 0\n\t"
        "ds_read_b64_tr_b16 v[142:143], v198 offset:4784\n\t"
        "v_mfma_f32_16x16x32_bf16 v[40:43], v[104:107], v[88:91], 0\n\t"
        "ds_read_b64_tr_b16 v[144:145], v198 offset:4672\n\t"
        "v_mfma_f32_16x16x32_bf16 v[56:59], v[104:107], v[92:95], 0\n\t"
        "ds_read_b64_tr_b16 v[146:147], v198 offset:4816\n\t"
        "v_mfma_f32_16x16x32_bf16 v[12:15], v[108:111], v[80:83], 0\n\t"
        "ds_read_b64_tr_b16 v[148:149], v198 offset:4704\n\t"
        "v_mfma_f32_16x16x32_bf16 v[28:31], v[108:111], v[84:87], 0\n\t"
        "v_mfma_f32_16x16x32_bf16 v[44:47], v[108:111], v[88:91], 0\n\t"
        "ds_read_b64_tr_b16 v[150:151], v198 offset:4848\n\t"
        "v_mfma_f32_16x16x32_bf16 v[60:63], v[108:111], v[92:95], 0\n\t"
        "ds_read_b64_tr_b16 v[152:153], v199 offset:4608\n\t"
        "v_mfma_f32_16x16x32_bf16 v[64:67], v[112:115], v[80:83], 0\n\t"
        "ds_read_b64_tr_b16 v[154:155], v199 offset:4752\n\t"
        "v_mfma_f32_16x16x32_bf16 v[68:71], v[112:115], v[84:87], 0\n\t"
        "s_waitcnt lgkmcnt(0)\n\t"
        "s_barrier\n\t"
        "v_mfma_f32_16x16x32_bf16 v[0:3], v[136:139], v[120:123], v[0:3]\n\t"
        "ds_read_b128 v[80:83], v196 offset:16384\n\t"
        "v_mfma_f32_16x16x32_bf16 v[16:19], v[136:139], v[124:127], v[16:19]\n\t"
        "ds_read_b64_tr_b16 v[96:97], v198 offset:18464\n\t"
        "v_mfma_f32_16x16x32_bf16 v[32:35], v[136:139], v[128:131], v[32:35]\n\t"
        "ds_read_b64_tr_b16 v[98:99], v198 offset:18608\n\t"
        "v_mfma_f32_16x16x32_bf16 v[48:51], v[136:139], v[132:135], v[48:51]\n\t"
        "ds_read_b128 v[84:87], v196 offset:18432\n\t"
        "v_mfma_f32_16x16x32_bf16 v[4:7], v[140:143], v[120:123], v[4:7]\n\t"
        "v_mfma_f32_16x16x32_bf16 v[20:23], v[140:143], v[124:127], v[20:23]\n\t"
        "ds_read_b128 v[88:91], v197 offset:16384\n\t"
        "v_mfma_f32_16x16x32_bf16 v[36:39], v[140:143], v[128:131], v[36:39]\n\t"
        "ds_read_b128 v[92:95], v197 offset:18432\n\t"
        "v_mfma_f32_16x16x32_bf16 v[52:55], v[140:143], v[132:135], v[52:55]\n\t"
        "ds_read_b64_tr_b16 v[100:101], v198 offset:18496\n\t"
        "v_mfma_f32_16x16x32_bf16 v[8:11], v[144:147], v[120:123], v[8:11]\n\t"
        "v_mfma_f32_16x16x32_bf16 v[24:27], v[144:147], v[124:127], v[24:27]\n\t"
        "ds_read_b64_tr_b16 v[102:103], v198 offset:18640\n\t"
        "v_mfma_f32_16x16x32_bf16 v[40:43], v[144:147], v[128:131], v[40:43]\n\t"
        "ds_read_b64_tr_b16 v[104:105], v198 offset:18528\n\t"
        "v_mfma_f32_16x16x32_bf16 v[56:59], v[144:147], v[132:135], v[56:59]\n\t"
        "ds_read_b64_tr_b16 v[106:107], v198 offset:18672\n\t"
        "v_mfma_f32_16x16x32_bf16 v[12:15], v[148:151], v[120:123], v[12:15]\n\t"
        "ds_read_b64_tr_b16 v[108:109], v198 offset:18560\n\t"
        "v_mfma_f32_16x16x32_bf16 v[28:31], v[148:151], v[124:127], v[28:31]\n\t"
        "v_mfma_f32_16x16x32_bf16 v[44:47], v[148:151], v[128:131], v[44:47]\n\t"
        "ds_read_b64_tr_b16 v[110:111], v198 offset:18704\n\t"
        "v_mfma_f32_16x16x32_bf16 v[60:63], v[148:151], v[132:135], v[60:63]\n\t"
        "ds_read_b64_tr_b16 v[112:113], v199 offset:18464\n\t"
        "v_mfma_f32_16x16x32_bf16 v[64:67], v[152:155], v[120:123], v[64:67]\n\t"
        "ds_read_b64_tr_b16 v[114:115], v199 offset:18608\n\t"
        "v_mfma_f32_16x16x32_bf16 v[68:71], v[152:155], v[124:127], v[68:71]\n\t"
        "s_waitcnt lgkmcnt(0)\n\t"
        "v_mfma_f32_16x16x32_bf16 v[0:3], v[96:99], v[80:83], v[0:3]\n\t"
        "ds_read_b128 v[120:123], v218 offset:16384\n\t"
        "v_mfma_f32_16x16x32_bf16 v[16:19], v[96:99], v[84:87], v[16:19]\n\t"
        "ds_read_b64_tr_b16 v[136:137], v198 offset:23072\n\t"
        "v_mfma_f32_16x16x32_bf16 v[32:35], v[96:99], v[88:91], v[32:35]\n\t"
        "ds_read_b64_tr_b16 v[138:139], v198 offset:23216\n\t"
        "v_mfma_f32_16x16x32_bf16 v[48:51], v[96:99], v[92:95], v[48:51]\n\t"
        "ds_read_b128 v[124:127], v218 offset:18432\n\t"
        "v_mfma_f32_16x16x32_bf16 v[4:7], v[100:103], v[80:83], v[4:7]\n\t"
        "v_mfma_f32_16x16x32_bf16 v[20:23], v[100:103], v[84:87], v[20:23]\n\t"
        "ds_read_b128 v[128:131], v219 offset:16384\n\t"
        "v_mfma_f32_16x16x32_bf16 v[36:39], v[100:103], v[88:91], v[36:39]\n\t"
        "ds_read_b128 v[132:135], v219 offset:18432\n\t"
        "v_mfma_f32_16x16x32_bf16 v[52:55], v[100:103], v[92:95], v[52:55]\n\t"
        "ds_read_b64_tr_b16 v[140:141], v198 offset:23104\n\t"
        "v_mfma_f32_16x16x32_bf16 v[8:11], v[104:107], v[80:83], v[8:11]\n\t"
        "v_mfma_f32_16x16x32_bf16 v[24:27], v[104:107], v[84:87], v[24:27]\n\t"
        "ds_read_b64_tr_b16 v[142:143], v198 offset:23248\n\t"
        "v_mfma_f32_16x16x32_bf16 v[40:43], v[104:107], v[88:91], v[40:43]\n\t"
        "ds_read_b64_tr_b16 v[144:145], v198 offset:23136\n\t"
        "v_mfma_f32_16x16x32_bf16 v[56:59], v[104:107], v[92:95], v[56:59]\n\t"
        "ds_read_b64_tr_b16 v[146:147], v198 offset:23280\n\t"
        "v_mfma_f32_16x16x32_bf16 v[12:15], v[108:111], v[80:83], v[12:15]\n\t"
        "ds_read_b64_tr_b16 v[148:149], v198 offset:23168\n\t"
        "v_mfma_f32_16x16x32_bf16 v[28:31], v[108:111], v[84:87], v[28:31]\n\t"
        "v_mfma_f32_16x16x32_bf16 v[44:47], v[108:111], v[88:91], v[44:47]\n\t"
        "ds_read_b64_tr_b16 v[150:151], v198 offset:23312\n\t"
        "v_mfma_f32_16x16x32_bf16 v[60:63], v[108:111], v[92:95], v[60:63]\n\t"
        "ds_read_b64_tr_b16 v[152:153], v199 offset:23072\n\t"
        "v_mfma_f32_16x16x32_bf16 v[64:67], v[112:115], v[80:83], v[64:67]\n\t"
        "ds_read_b64_tr_b16 v[154:155], v199 offset:23216\n\t"
        "v_mfma_f32_16x16x32_bf16 v[68:71], v[112:115], v[84:87], v[68:71]\n\t"
        "s_waitcnt lgkmcnt(0)\n\t"
        "s_barrier\n\t"
        "v_mfma_f32_16x16x32_bf16 v[0:3], v[136:139], v[120:123], v[0:3]\n\t"
        "ds_read_b128 v[80:83], v196 offset:32768\n\t"
        "v_mfma_f32_16x16x32_bf16 v[16:19], v[136:139], v[124:127], v[16:19]\n\t"
        "ds_read_b64_tr_b16 v[96:97], v198 offset:36928\n\t"
        "v_mfma_f32_16x16x32_bf16 v[32:35], v[136:139], v[128:131], v[32:35]\n\t"
        "ds_read_b64_tr_b16 v[98:99], v198 offset:37072\n\t"
        "v_mfma_f32_16x16x32_bf16 v[48:51], v[136:139], v[132:135], v[48:51]\n\t"
        "ds_read_b128 v[84:87], v196 offset:34816\n\t"
        "v_mfma_f32_16x16x32_bf16 v[4:7], v[140:143], v[120:123], v[4:7]\n\t"
        "v_mfma_f32_16x16x32_bf16 v[20:23], v[140:143], v[124:127], v[20:23]\n\t"
        "ds_read_b128 v[88:91], v197 offset:32768\n\t"
        "v_mfma_f32_16x16x32_bf16 v[36:39], v[140:143], v[128:131], v[36:39]\n\t"
        "ds_read_b128 v[92:95], v197 offset:34816\n\t"
        "v_mfma_f32_16x16x32_bf16 v[52:55], v[140:143], v[132:135], v[52:55]\n\t"
        "ds_read_b64_tr_b16 v[100:101], v198 offset:36960\n\t"
        "v_mfma_f32_16x16x32_bf16 v[8:11], v[144:147], v[120:123], v[8:11]\n\t"
        "v_mfma_f32_16x16x32_bf16 v[24:27], v[144:147], v[124:127], v[24:27]\n\t"
        "ds_read_b64_tr_b16 v[102:103], v198 offset:37104\n\t"
        "v_mfma_f32_16x16x32_bf16 v[40:43], v[144:147], v[128:131], v[40:43]\n\t"
        "ds_read_b64_tr_b16 v[104:105], v198 offset:36992\n\t"
        "v_mfma_f32_16x16x32_bf16 v[56:59], v[144:147], v[132:135], v[56:59]\n\t"
        "ds_read_b64_tr_b16 v[106:107], v198 offset:37136\n\t"
        "v_mfma_f32_16x16x32_bf16 v[12:15], v[148:151], v[120:123], v[12:15]\n\t"
        "ds_read_b64_tr_b16 v[108:109], v198 offset:37024\n\t"
        "v_mfma_f32_16x16x32_bf16 v[28:31], v[148:151], v[124:127], v[28:31]\n\t"
        "v_mfma_f32_16x16x32_bf16 v[44:47], v[148:151], v[128:131], v[44:47]\n\t"
        "ds_read_b64_tr_b16 v[110:111], v198 offset:37168\n\t"
        "v_mfma_f32_16x16x32_bf16 v[60:63], v[148:151], v[132:135], v[60:63]\n\t"
        "ds_read_b64_tr_b16 v[112:113], v199 offset:36928\n\t"
        "v_mfma_f32_16x16x32_bf16 v[64:67], v[152:155], v[120:123], v[64:67]\n\t"
        "ds_read_b64_tr_b16 v[114:115], v199 offset:37072\n\t"
        "v_mfma_f32_16x16x32_bf16 v[68:71], v[152:155], v[124:127], v[68:71]\n\t"
        "s_waitcnt lgkmcnt(0)\n\t"
        "v_mfma_f32_16x16x32_bf16 v[0:3], v[96:99], v[80:83], v[0:3]\n\t"
        "ds_read_b128 v[120:123], v218 offset:32768\n\t"
        "v_mfma_f32_16x16x32_bf16 v[16:19], v[96:99], v[84:87], v[16:19]\n\t"
        "ds_read_b64_tr_b16 v[136:137], v198 offset:41536\n\t"
        "v_mfma_f32_16x16x32_bf16 v[32:35], v[96:99], v[88:91], v[32:35]\n\t"
        "ds_read_b64_tr_b16 v[138:139], v198 offset:41680\n\t"
        "v_mfma_f32_16x16x32_bf16 v[48:51], v[96:99], v[92:95], v[48:51]\n\t"
        "ds_read_b128 v[124:127], v218 offset:34816\n\t"
        "v_mfma_f32_16x16x32_bf16 v[4:7], v[100:103], v[80:83], v[4:7]\n\t"
        "v_mfma_f32_16x16x32_bf16 v[20:23], v[100:103], v[84:87], v[20:23]\n\t"
        "ds_read_b128 v[128:131], v219 offset:32768\n\t"
        "v_mfma_f32_16x16x32_bf16 v[36:39], v[100:103], v[88:91], v[36:39]\n\t"
        "ds_read_b128 v[132:135], v219 offset:34816\n\t"
        "v_mfma_f32_16x16x32_bf16 v[52:55], v[100:103], v[92:95], v[52:55]\n\t"
        "ds_read_b64_tr_b16 v[140:141], v198 offset:41568\n\t"
        "v_mfma_f32_16x16x32_bf16 v[8:11], v[104:107], v[80:83], v[8:11]\n\t"
        "v_mfma_f32_16x16x32_bf16 v[24:27], v[104:107], v[84:87], v[24:27]\n\t"
        "ds_read_b64_tr_b16 v[142:143], v198 offset:41712\n\t"
        "v_mfma_f32_16x16x32_bf16 v[40:43], v[104:107], v[88:91], v[40:43]\n\t"
        "ds_read_b64_tr_b16 v[144:145], v198 offset:41600\n\t"
        "v_mfma_f32_16x16x32_bf16 v[56:59], v[104:107], v[92:95], v[56:59]\n\t"
        "ds_read_b64_tr_b16 v[146:147], v198 offset:41744\n\t"
        "v_mfma_f32_16x16x32_bf16 v[12:15], v[108:111], v[80:83], v[12:15]\n\t"
        "ds_read_b64_tr_b16 v[148:149], v198 offset:41632\n\t"
        "v_mfma_f32_16x16x32_bf16 v[28:31], v[108:111], v[84:87], v[28:31]\n\t"
        "v_mfma_f32_16x16x32_bf16 v[44:47], v[108:111], v[88:91], v[44:47]\n\t"
        "ds_read_b64_tr_b16 v[150:151], v198 offset:41776\n\t"
        "v_mfma_f32_16x16x32_bf16 v[60:63], v[108:111], v[92:95], v[60:63]\n\t"
        "ds_read_b64_tr_b16 v[152:153], v199 offset:41536\n\t"
        "v_mfma_f32_16x16x32_bf16 v[64:67], v[112:115], v[80:83], v[64:67]\n\t"
        "ds_read_b64_tr_b16 v[154:155], v199 offset:41680\n\t"
        "v_mfma_f32_16x16x32_bf16 v[68:71], v[112:115], v[84:87], v[68:71]\n\t"
        "s_waitcnt lgkmcnt(0)\n\t"
        "s_barrier\n\t"
        "v_mfma_f32_16x16x32_bf16 v[0:3], v[136:139], v[120:123], v[0:3]\n\t"
        "ds_read_b128 v[80:83], v196 offset:49152\n\t"
        "v_mfma_f32_16x16x32_bf16 v[16:19], v[136:139], v[124:127], v[16:19]\n\t"
        "ds_read_b64_tr_b16 v[96:97], v198 offset:55392\n\t"
        "v_mfma_f32_16x16x32_bf16 v[32:35], v[136:139], v[128:131], v[32:35]\n\t"
        "ds_read_b64_tr_b16 v[98:99], v198 offset:55536\n\t"
        "v_mfma_f32_16x16x32_bf16 v[48:51], v[136:139], v[132:135], v[48:51]\n\t"
        "ds_read_b128 v[84:87], v196 offset:51200\n\t"
        "v_mfma_f32_16x16x32_bf16 v[4:7], v[140:143], v[120:123], v[4:7]\n\t"
        "v_mfma_f32_16x16x32_bf16 v[20:23], v[140:143], v[124:127], v[20:23]\n\t"
        "ds_read_b128 v[88:91], v197 offset:49152\n\t"
        "v_mfma_f32_16x16x32_bf16 v[36:39], v[140:143], v[128:131], v[36:39]\n\t"
        "ds_read_b128 v[92:95], v197 offset:51200\n\t"
        "v_mfma_f32_16x16x32_bf16 v[52:55], v[140:143], v[132:135], v[52:55]\n\t"
        "ds_read_b64_tr_b16 v[100:101], v198 offset:55424\n\t"
        "v_mfma_f32_16x16x32_bf16 v[8:11], v[144:147], v[120:123], v[8:11]\n\t"
        "v_mfma_f32_16x16x32_bf16 v[24:27], v[144:147], v[124:127], v[24:27]\n\t"
        "ds_read_b64_tr_b16 v[102:103], v198 offset:55568\n\t"
        "v_mfma_f32_16x16x32_bf16 v[40:43], v[144:147], v[128:131], v[40:43]\n\t"
        "ds_read_b64_tr_b16 v[104:105], v198 offset:55456\n\t"
        "v_mfma_f32_16x16x32_bf16 v[56:59], v[144:147], v[132:135], v[56:59]\n\t"
        "ds_read_b64_tr_b16 v[106:107], v198 offset:55600\n\t"
        "v_mfma_f32_16x16x32_bf16 v[12:15], v[148:151], v[120:123], v[12:15]\n\t"
        "ds_read_b64_tr_b16 v[108:109], v198 offset:55488\n\t"
        "v_mfma_f32_16x16x32_bf16 v[28:31], v[148:151], v[124:127], v[28:31]\n\t"
        "v_mfma_f32_16x16x32_bf16 v[44:47], v[148:151], v[128:131], v[44:47]\n\t"
        "ds_read_b64_tr_b16 v[110:111], v198 offset:55632\n\t"
        "v_mfma_f32_16x16x32_bf16 v[60:63], v[148:151], v[132:135], v[60:63]\n\t"
        "ds_read_b64_tr_b16 v[112:113], v199 offset:55392\n\t"
        "v_mfma_f32_16x16x32_bf16 v[64:67], v[152:155], v[120:123], v[64:67]\n\t"
        "ds_read_b64_tr_b16 v[114:115], v199 offset:55536\n\t"
        "v_mfma_f32_16x16x32_bf16 v[68:71], v[152:155], v[124:127], v[68:71]\n\t"
        "s_waitcnt lgkmcnt(0)\n\t"
        "v_mfma_f32_16x16x32_bf16 v[0:3], v[96:99], v[80:83], v[0:3]\n\t"
        "ds_read_b128 v[120:123], v218 offset:49152\n\t"
        "v_mfma_f32_16x16x32_bf16 v[16:19], v[96:99], v[84:87], v[16:19]\n\t"
        "ds_read_b64_tr_b16 v[136:137], v198 offset:60000\n\t"
        "v_mfma_f32_16x16x32_bf16 v[32:35], v[96:99], v[88:91], v[32:35]\n\t"
        "ds_read_b64_tr_b16 v[138:139], v198 offset:60144\n\t"
        "v_mfma_f32_16x16x32_bf16 v[48:51], v[96:99], v[92:95], v[48:51]\n\t"
        "ds_read_b128 v[124:127], v218 offset:51200\n\t"
        "v_mfma_f32_16x16x32_bf16 v[4:7], v[100:103], v[80:83], v[4:7]\n\t"
        "v_mfma_f32_16x16x32_bf16 v[20:23], v[100:103], v[84:87], v[20:23]\n\t"
        "ds_read_b128 v[128:131], v219 offset:49152\n\t"
        "v_mfma_f32_16x16x32_bf16 v[36:39], v[100:103], v[88:91], v[36:39]\n\t"
        "ds_read_b128 v[132:135], v219 offset:51200\n\t"
        "v_mfma_f32_16x16x32_bf16 v[52:55], v[100:103], v[92:95], v[52:55]\n\t"
        "ds_read_b64_tr_b16 v[140:141], v198 offset:60032\n\t"
        "v_mfma_f32_16x16x32_bf16 v[8:11], v[104:107], v[80:83], v[8:11]\n\t"
        "v_mfma_f32_16x16x32_bf16 v[24:27], v[104:107], v[84:87], v[24:27]\n\t"
        "ds_read_b64_tr_b16 v[142:143], v198 offset:60176\n\t"
        "v_mfma_f32_16x16x32_bf16 v[40:43], v[104:107], v[88:91], v[40:43]\n\t"
        "ds_read_b64_tr_b16 v[144:145], v198 offset:60064\n\t"
        "v_mfma_f32_16x16x32_bf16 v[56:59], v[104:107], v[92:95], v[56:59]\n\t"
        "ds_read_b64_tr_b16 v[146:147], v198 offset:60208\n\t"
        "v_mfma_f32_16x16x32_bf16 v[12:15], v[108:111], v[80:83], v[12:15]\n\t"
        "ds_read_b64_tr_b16 v[148:149], v198 offset:60096\n\t"
        "v_mfma_f32_16x16x32_bf16 v[28:31], v[108:111], v[84:87], v[28:31]\n\t"
        "v_mfma_f32_16x16x32_bf16 v[44:47], v[108:111], v[88:91], v[44:47]\n\t"
        "ds_read_b64_tr_b16 v[150:151], v198 offset:60240\n\t"
        "v_mfma_f32_16x16x32_bf16 v[60:63], v[108:111], v[92:95], v[60:63]\n\t"
        "ds_read_b64_tr_b16 v[152:153], v199 offset:60000\n\t"
        "v_mfma_f32_16x16x32_bf16 v[64:67], v[112:115], v[80:83], v[64:67]\n\t"
        "ds_read_b64_tr_b16 v[154:155], v199 offset:60144\n\t"
        "v_mfma_f32_16x16x32_bf16 v[68:71], v[112:115], v[84:87], v[68:71]\n\t"
        "s_waitcnt lgkmcnt(0)\n\t"
        "s_barrier\n\t"
        "v_mfma_f32_16x16x32_bf16 v[0:3], v[136:139], v[120:123], v[0:3]\n\t"
        "ds_read_b128 v[80:83], v196 offset:0\n\t"
        "v_mfma_f32_16x16x32_bf16 v[16:19], v[136:139], v[124:127], v[16:19]\n\t"
        "ds_read_b64_tr_b16 v[96:97], v198 offset:0\n\t"
        "v_mfma_f32_16x16x32_bf16 v[32:35], v[136:139], v[128:131], v[32:35]\n\t"
        "ds_read_b64_tr_b16 v[98:99], v198 offset:144\n\t"
        "v_mfma_f32_16x16x32_bf16 v[48:51], v[136:139], v[132:135], v[48:51]\n\t"
        "ds_read_b128 v[84:87], v196 offset:2048\n\t"
        "v_mfma_f32_16x16x32_bf16 v[4:7], v[140:143], v[120:123], v[4:7]\n\t"
        "v_mfma_f32_16x16x32_bf16 v[20:23], v[140:143], v[124:127], v[20:23]\n\t"
        "ds_read_b128 v[88:91], v197 offset:0\n\t"
        "v_mfma_f32_16x16x32_bf16 v[36:39], v[140:143], v[128:131], v[36:39]\n\t"
        "ds_read_b128 v[92:95], v197 offset:2048\n\t"
        "v_mfma_f32_16x16x32_bf16 v[52:55], v[140:143], v[132:135], v[52:55]\n\t"
        "ds_read_b64_tr_b16 v[100:101], v198 offset:32\n\t"
        "v_mfma_f32_16x16x32_bf16 v[8:11], v[144:147], v[120:123], v[8:11]\n\t"
        "v_mfma_f32_16x16x32_bf16 v[24:27], v[144:147], v[124:127], v[24:27]\n\t"
        "ds_read_b64_tr_b16 v[102:103], v198 offset:176\n\t"
        "v_mfma_f32_16x16x32_bf16 v[40:43], v[144:147], v[128:131], v[40:43]\n\t"
        "ds_read_b64_tr_b16 v[104:105], v198 offset:64\n\t"
        "v_mfma_f32_16x16x32_bf16 v[56:59], v[144:147], v[132:135], v[56:59]\n\t"
        "ds_read_b64_tr_b16 v[106:107], v198 offset:208\n\t"
        "v_mfma_f32_16x16x32_bf16 v[12:15], v[148:151], v[120:123], v[12:15]\n\t"
        "ds_read_b64_tr_b16 v[108:109], v198 offset:96\n\t"
        "v_mfma_f32_16x16x32_bf16 v[28:31], v[148:151], v[124:127], v[28:31]\n\t"
        "v_mfma_f32_16x16x32_bf16 v[44:47], v[148:151], v[128:131], v[44:47]\n\t"
        "ds_read_b64_tr_b16 v[110:111], v198 offset:240\n\t"
        "v_mfma_f32_16x16x32_bf16 v[60:63], v[148:151], v[132:135], v[60:63]\n\t"
        "ds_read_b64_tr_b16 v[112:113], v199 offset:0\n\t"
        "v_mfma_f32_16x16x32_bf16 v[64:67], v[152:155], v[120:123], v[64:67]\n\t"
        "ds_read_b64_tr_b16 v[114:115], v199 offset:144\n\t"
        "v_mfma_f32_16x16x32_bf16 v[68:71], v[152:155], v[124:127], v[68:71]\n\t"
        "s_waitcnt lgkmcnt(0)\n\t"
        "v_mfma_f32_16x16x32_bf16 v[0:3], v[96:99], v[80:83], v[0:3]\n\t"
        "ds_read_b128 v[120:123], v218 offset:0\n\t"
        "v_mfma_f32_16x16x32_bf16 v[16:19], v[96:99], v[84:87], v[16:19]\n\t"
        "ds_read_b64_tr_b16 v[136:137], v198 offset:4608\n\t"
        "v_mfma_f32_16x16x32_bf16 v[32:35], v[96:99], v[88:91], v[32:35]\n\t"
        "ds_read_b64_tr_b16 v[138:139], v198 offset:4752\n\t"
        "v_mfma_f32_16x16x32_bf16 v[48:51], v[96:99], v[92:95], v[48:51]\n\t"
        "ds_read_b128 v[124:127], v218 offset:2048\n\t"
        "v_mfma_f32_16x16x32_bf16 v[4:7], v[100:103], v[80:83], v[4:7]\n\t"
        "v_mfma_f32_16x16x32_bf16 v[20:23], v[100:103], v[84:87], v[20:23]\n\t"
        "ds_read_b128 v[128:131], v219 offset:0\n\t"
        "v_mfma_f32_16x16x32_bf16 v[36:39], v[100:103], v[88:91], v[36:39]\n\t"
        "ds_read_b128 v[132:135], v219 offset:2048\n\t"
        "v_mfma_f32_16x16x32_bf16 v[52:55], v[100:103], v[92:95], v[52:55]\n\t"
        "ds_read_b64_tr_b16 v[140:141], v198 offset:4640\n\t"
        "v_mfma_f32_16x16x32_bf16 v[8:11], v[104:107], v[80:83], v[8:11]\n\t"
        "v_mfma_f32_16x16x32_bf16 v[24:27], v[104:107], v[84:87], v[24:27]\n\t"
        "ds_read_b64_tr_b16 v[142:143], v198 offset:4784\n\t"
        "v_mfma_f32_16x16x32_bf16 v[40:43], v[104:107], v[88:91], v[40:43]\n\t"
        "ds_read_b64_tr_b16 v[144:145], v198 offset:4672\n\t"
        "v_mfma_f32_16x16x32_bf16 v[56:59], v[104:107], v[92:95], v[56:59]\n\t"
        "ds_read_b64_tr_b16 v[146:147], v198 offset:4816\n\t"
        "v_mfma_f32_16x16x32_bf16 v[12:15], v[108:111], v[80:83], v[12:15]\n\t"
        "ds_read_b64_tr_b16 v[148:149], v198 offset:4704\n\t"
        "v_mfma_f32_16x16x32_bf16 v[28:31], v[108:111], v[84:87], v[28:31]\n\t"
        "v_mfma_f32_16x16x32_bf16 v[44:47], v[108:111], v[88:91], v[44:47]\n\t"
        "ds_read_b64_tr_b16 v[150:151], v198 offset:4848\n\t"
        "v_mfma_f32_16x16x32_bf16 v[60:63], v[108:111], v[92:95], v[60:63]\n\t"
        "ds_read_b64_tr_b16 v[152:153], v199 offset:4608\n\t"
        "v_mfma_f32_16x16x32_bf16 v[64:67], v[112:115], v[80:83], v[64:67]\n\t"
        "ds_read_b64_tr_b16 v[154:155], v199 offset:4752\n\t"
        "v_mfma_f32_16x16x32_bf16 v[68:71], v[112:115], v[84:87], v[68:71]\n\t"
        "s_waitcnt lgkmcnt(0)\n\t"
        "s_barrier\n\t"
        "v_mfma_f32_16x16x32_bf16 v[0:3], v[136:139], v[120:123], v[0:3]\n\t"
        "ds_read_b128 v[80:83], v196 offset:16384\n\t"
        "v_mfma_f32_16x16x32_bf16 v[16:19], v[136:139], v[124:127], v[16:19]\n\t"
        "ds_read_b64_tr_b16 v[96:97], v198 offset:18464\n\t"
        "v_mfma_f32_16x16x32_bf16 v[32:35], v[136:139], v[128:131], v[32:35]\n\t"
        "ds_read_b64_tr_b16 v[98:99], v198 offset:18608\n\t"
        "v_mfma_f32_16x16x32_bf16 v[48:51], v[136:139], v[132:135], v[48:51]\n\t"
        "ds_read_b128 v[84:87], v196 offset:18432\n\t"
        "v_mfma_f32_16x16x32_bf16 v[4:7], v[140:143], v[120:123], v[4:7]\n\t"
        "v_mfma_f32_16x16x32_bf16 v[20:23], v[140:143], v[124:127], v[20:23]\n\t"
        "ds_read_b128 v[88:91], v197 offset:16384\n\t"
        "v_mfma_f32_16x16x32_bf16 v[36:39], v[140:143], v[128:131], v[36:39]\n\t"
        "ds_read_b128 v[92:95], v197 offset:18432\n\t"
        "v_mfma_f32_16x16x32_bf16 v[52:55], v[140:143], v[132:135], v[52:55]\n\t"
        "ds_read_b64_tr_b16 v[100:101], v198 offset:18496\n\t"
        "v_mfma_f32_16x16x32_bf16 v[8:11], v[144:147], v[120:123], v[8:11]\n\t"
        "v_mfma_f32_16x16x32_bf16 v[24:27], v[144:147], v[124:127], v[24:27]\n\t"
        "ds_read_b64_tr_b16 v[102:103], v198 offset:18640\n\t"
        "v_mfma_f32_16x16x32_bf16 v[40:43], v[144:147], v[128:131], v[40:43]\n\t"
        "ds_read_b64_tr_b16 v[104:105], v198 offset:18528\n\t"
        "v_mfma_f32_16x16x32_bf16 v[56:59], v[144:147], v[132:135], v[56:59]\n\t"
        "ds_read_b64_tr_b16 v[106:107], v198 offset:18672\n\t"
        "v_mfma_f32_16x16x32_bf16 v[12:15], v[148:151], v[120:123], v[12:15]\n\t"
        "ds_read_b64_tr_b16 v[108:109], v198 offset:18560\n\t"
        "v_mfma_f32_16x16x32_bf16 v[28:31], v[148:151], v[124:127], v[28:31]\n\t"
        "v_mfma_f32_16x16x32_bf16 v[44:47], v[148:151], v[128:131], v[44:47]\n\t"
        "ds_read_b64_tr_b16 v[110:111], v198 offset:18704\n\t"
        "v_mfma_f32_16x16x32_bf16 v[60:63], v[148:151], v[132:135], v[60:63]\n\t"
        "ds_read_b64_tr_b16 v[112:113], v199 offset:18464\n\t"
        "v_mfma_f32_16x16x32_bf16 v[64:67], v[152:155], v[120:123], v[64:67]\n\t"
        "ds_read_b64_tr_b16 v[114:115], v199 offset:18608\n\t"
        "v_mfma_f32_16x16x32_bf16 v[68:71], v[152:155], v[124:127], v[68:71]\n\t"
        "s_waitcnt lgkmcnt(0)\n\t"
        "v_mfma_f32_16x16x32_bf16 v[0:3], v[96:99], v[80:83], v[0:3]\n\t"
        "ds_read_b128 v[120:123], v218 offset:16384\n\t"
        "v_mfma_f32_16x16x32_bf16 v[16:19], v[96:99], v[84:87], v[16:19]\n\t"
        "ds_read_b64_tr_b16 v[136:137], v198 offset:23072\n\t"
        "v_mfma_f32_16x16x32_bf16 v[32:35], v[96:99], v[88:91], v[32:35]\n\t"
        "ds_read_b64_tr_b16 v[138:139], v198 offset:23216\n\t"
        "v_mfma_f32_16x16x32_bf16 v[48:51], v[96:99], v[92:95], v[48:51]\n\t"
        "ds_read_b128 v[124:127], v218 offset:18432\n\t"
        "v_mfma_f32_16x16x32_bf16 v[4:7], v[100:103], v[80:83], v[4:7]\n\t"
        "v_mfma_f32_16x16x32_bf16 v[20:23], v[100:103], v[84:87], v[20:23]\n\t"
        "ds_read_b128 v[128:131], v219 offset:16384\n\t"
        "v_mfma_f32_16x16x32_bf16 v[36:39], v[100:103], v[88:91], v[36:39]\n\t"
        "ds_read_b128 v[132:135], v219 offset:18432\n\t"
        "v_mfma_f32_16x16x32_bf16 v[52:55], v[100:103], v[92:95], v[52:55]\n\t"
        "ds_read_b64_tr_b16 v[140:141], v198 offset:23104\n\t"
        "v_mfma_f32_16x16x32_bf16 v[8:11], v[104:107], v[80:83], v[8:11]\n\t"
        "v_mfma_f32_16x16x32_bf16 v[24:27], v[104:107], v[84:87], v[24:27]\n\t"
        "ds_read_b64_tr_b16 v[142:143], v198 offset:23248\n\t"
        "v_mfma_f32_16x16x32_bf16 v[40:43], v[104:107], v[88:91], v[40:43]\n\t"
        "ds_read_b64_tr_b16 v[144:145], v198 offset:23136\n\t"
        "v_mfma_f32_16x16x32_bf16 v[56:59], v[104:107], v[92:95], v[56:59]\n\t"
        "ds_read_b64_tr_b16 v[146:147], v198 offset:23280\n\t"
        "v_mfma_f32_16x16x32_bf16 v[12:15], v[108:111], v[80:83], v[12:15]\n\t"
        "ds_read_b64_tr_b16 v[148:149], v198 offset:23168\n\t"
        "v_mfma_f32_16x16x32_bf16 v[28:31], v[108:111], v[84:87], v[28:31]\n\t"
        "v_mfma_f32_16x16x32_bf16 v[44:47], v[108:111], v[88:91], v[44:47]\n\t"
        "ds_read_b64_tr_b16 v[150:151], v198 offset:23312\n\t"
        "v_mfma_f32_16x16x32_bf16 v[60:63], v[108:111], v[92:95], v[60:63]\n\t"
        "ds_read_b64_tr_b16 v[152:153], v199 offset:23072\n\t"
        "v_mfma_f32_16x16x32_bf16 v[64:67], v[112:115], v[80:83], v[64:67]\n\t"
        "ds_read_b64_tr_b16 v[154:155], v199 offset:23216\n\t"
        "v_mfma_f32_16x16x32_bf16 v[68:71], v[112:115], v[84:87], v[68:71]\n\t"
        "s_waitcnt lgkmcnt(0)\n\t"
        "s_barrier\n\t"
        "v_mfma_f32_16x16x32_bf16 v[0:3], v[136:139], v[120:123], v[0:3]\n\t"
        "ds_read_b128 v[80:83], v196 offset:32768\n\t"
        "v_mfma_f32_16x16x32_bf16 v[16:19], v[136:139], v[124:127], v[16:19]\n\t"
        "ds_read_b64_tr_b16 v[96:97], v198 offset:36928\n\t"
        "v_mfma_f32_16x16x32_bf16 v[32:35], v[136:139], v[128:131], v[32:35]\n\t"
        "ds_read_b64_tr_b16 v[98:99], v198 offset:37072\n\t"
        "v_mfma_f32_16x16x32_bf16 v[48:51], v[136:139], v[132:135], v[48:51]\n\t"
        "ds_read_b128 v[84:87], v196 offset:34816\n\t"
        "v_mfma_f32_16x16x32_bf16 v[4:7], v[140:143], v[120:123], v[4:7]\n\t"
        "v_mfma_f32_16x16x32_bf16 v[20:23], v[140:143], v[124:127], v[20:23]\n\t"
        "ds_read_b128 v[88:91], v197 offset:32768\n\t"
        "v_mfma_f32_16x16x32_bf16 v[36:39], v[140:143], v[128:131], v[36:39]\n\t"
        "ds_read_b128 v[92:95], v197 offset:34816\n\t"
        "v_mfma_f32_16x16x32_bf16 v[52:55], v[140:143], v[132:135], v[52:55]\n\t"
        "ds_read_b64_tr_b16 v[100:101], v198 offset:36960\n\t"
        "v_mfma_f32_16x16x32_bf16 v[8:11], v[144:147], v[120:123], v[8:11]\n\t"
        "v_mfma_f32_16x16x32_bf16 v[24:27], v[144:147], v[124:127], v[24:27]\n\t"
        "ds_read_b64_tr_b16 v[102:103], v198 offset:37104\n\t"
        "v_mfma_f32_16x16x32_bf16 v[40:43], v[144:147], v[128:131], v[40:43]\n\t"
        "ds_read_b64_tr_b16 v[104:105], v198 offset:36992\n\t"
        "v_mfma_f32_16x16x32_bf16 v[56:59], v[144:147], v[132:135], v[56:59]\n\t"
        "ds_read_b64_tr_b16 v[106:107], v198 offset:37136\n\t"
        "v_mfma_f32_16x16x32_bf16 v[12:15], v[148:151], v[120:123], v[12:15]\n\t"
        "ds_read_b64_tr_b16 v[108:109], v198 offset:37024\n\t"
        "v_mfma_f32_16x16x32_bf16 v[28:31], v[148:151], v[124:127], v[28:31]\n\t"
        "v_mfma_f32_16x16x32_bf16 v[44:47], v[148:151], v[128:131], v[44:47]\n\t"
        "ds_read_b64_tr_b16 v[110:111], v198 offset:37168\n\t"
        "v_mfma_f32_16x16x32_bf16 v[60:63], v[148:151], v[132:135], v[60:63]\n\t"
        "ds_read_b64_tr_b16 v[112:113], v199 offset:36928\n\t"
        "v_mfma_f32_16x16x32_bf16 v[64:67], v[152:155], v[120:123], v[64:67]\n\t"
        "ds_read_b64_tr_b16 v[114:115], v199 offset:37072\n\t"
        "v_mfma_f32_16x16x32_bf16 v[68:71], v[152:155], v[124:127], v[68:71]\n\t"
        "s_waitcnt lgkmcnt(0)\n\t"
        "v_mfma_f32_16x16x32_bf16 v[0:3], v[96:99], v[80:83], v[0:3]\n\t"
        "ds_read_b128 v[120:123], v218 offset:32768\n\t"
        "v_mfma_f32_16x16x32_bf16 v[16:19], v[96:99], v[84:87], v[16:19]\n\t"
        "ds_read_b64_tr_b16 v[136:137], v198 offset:41536\n\t"
        "v_mfma_f32_16x16x32_bf16 v[32:35], v[96:99], v[88:91], v[32:35]\n\t"
        "ds_read_b64_tr_b16 v[138:139], v198 offset:41680\n\t"
        "v_mfma_f32_16x16x32_bf16 v[48:51], v[96:99], v[92:95], v[48:51]\n\t"
        "ds_read_b128 v[124:127], v218 offset:34816\n\t"
        "v_mfma_f32_16x16x32_bf16 v[4:7], v[100:103], v[80:83], v[4:7]\n\t"
        "v_mfma_f32_16x16x32_bf16 v[20:23], v[100:103], v[84:87], v[20:23]\n\t"
        "ds_read_b128 v[128:131], v219 offset:32768\n\t"
        "v_mfma_f32_16x16x32_bf16 v[36:39], v[100:103], v[88:91], v[36:39]\n\t"
        "ds_read_b128 v[132:135], v219 offset:34816\n\t"
        "v_mfma_f32_16x16x32_bf16 v[52:55], v[100:103], v[92:95], v[52:55]\n\t"
        "ds_read_b64_tr_b16 v[140:141], v198 offset:41568\n\t"
        "v_mfma_f32_16x16x32_bf16 v[8:11], v[104:107], v[80:83], v[8:11]\n\t"
        "v_mfma_f32_16x16x32_bf16 v[24:27], v[104:107], v[84:87], v[24:27]\n\t"
        "ds_read_b64_tr_b16 v[142:143], v198 offset:41712\n\t"
        "v_mfma_f32_16x16x32_bf16 v[40:43], v[104:107], v[88:91], v[40:43]\n\t"
        "ds_read_b64_tr_b16 v[144:145], v198 offset:41600\n\t"
        "v_mfma_f32_16x16x32_bf16 v[56:59], v[104:107], v[92:95], v[56:59]\n\t"
        "ds_read_b64_tr_b16 v[146:147], v198 offset:41744\n\t"
        "v_mfma_f32_16x16x32_bf16 v[12:15], v[108:111], v[80:83], v[12:15]\n\t"
        "ds_read_b64_tr_b16 v[148:149], v198 offset:41632\n\t"
        "v_mfma_f32_16x16x32_bf16 v[28:31], v[108:111], v[84:87], v[28:31]\n\t"
        "v_mfma_f32_16x16x32_bf16 v[44:47], v[108:111], v[88:91], v[44:47]\n\t"
        "ds_read_b64_tr_b16 v[150:151], v198 offset:41776\n\t"
        "v_mfma_f32_16x16x32_bf16 v[60:63], v[108:111], v[92:95], v[60:63]\n\t"
        "ds_read_b64_tr_b16 v[152:153], v199 offset:41536\n\t"
        "v_mfma_f32_16x16x32_bf16 v[64:67], v[112:115], v[80:83], v[64:67]\n\t"
        "ds_read_b64_tr_b16 v[154:155], v199 offset:41680\n\t"
        "v_mfma_f32_16x16x32_bf16 v[68:71], v[112:115], v[84:87], v[68:71]\n\t"
        "s_waitcnt lgkmcnt(0)\n\t"
        "s_barrier\n\t"
        "v_mfma_f32_16x16x32_bf16 v[0:3], v[136:139], v[120:123], v[0:3]\n\t"
        "ds_read_b128 v[80:83], v196 offset:49152\n\t"
        "v_mfma_f32_16x16x32_bf16 v[16:19], v[136:139], v[124:127], v[16:19]\n\t"
        "ds_read_b64_tr_b16 v[96:97], v198 offset:55392\n\t"
        "v_mfma_f32_16x16x32_bf16 v[32:35], v[136:139], v[128:131], v[32:35]\n\t"
        "ds_read_b64_tr_b16 v[98:99], v198 offset:55536\n\t"
        "v_mfma_f32_16x16x32_bf16 v[48:51], v[136:139], v[132:135], v[48:51]\n\t"
        "ds_read_b128 v[84:87], v196 offset:51200\n\t"
        "v_mfma_f32_16x16x32_bf16 v[4:7], v[140:143], v[120:123], v[4:7]\n\t"
        "v_mfma_f32_16x16x32_bf16 v[20:23], v[140:143], v[124:127], v[20:23]\n\t"
        "ds_read_b128 v[88:91], v197 offset:49152\n\t"
        "v_mfma_f32_16x16x32_bf16 v[36:39], v[140:143], v[128:131], v[36:39]\n\t"
        "ds_read_b128 v[92:95], v197 offset:51200\n\t"
        "v_mfma_f32_16x16x32_bf16 v[52:55], v[140:143], v[132:135], v[52:55]\n\t"
        "ds_read_b64_tr_b16 v[100:101], v198 offset:55424\n\t"
        "v_mfma_f32_16x16x32_bf16 v[8:11], v[144:147], v[120:123], v[8:11]\n\t"
        "v_mfma_f32_16x16x32_bf16 v[24:27], v[144:147], v[124:127], v[24:27]\n\t"
        "ds_read_b64_tr_b16 v[102:103], v198 offset:55568\n\t"
        "v_mfma_f32_16x16x32_bf16 v[40:43], v[144:147], v[128:131], v[40:43]\n\t"
        "ds_read_b64_tr_b16 v[104:105], v198 offset:55456\n\t"
        "v_mfma_f32_16x16x32_bf16 v[56:59], v[144:147], v[132:135], v[56:59]\n\t"
        "ds_read_b64_tr_b16 v[106:107], v198 offset:55600\n\t"
        "v_mfma_f32_16x16x32_bf16 v[12:15], v[148:151], v[120:123], v[12:15]\n\t"
        "ds_read_b64_tr_b16 v[108:109], v198 offset:55488\n\t"
        "v_mfma_f32_16x16x32_bf16 v[28:31], v[148:151], v[124:127], v[28:31]\n\t"
        "v_mfma_f32_16x16x32_bf16 v[44:47], v[148:151], v[128:131], v[44:47]\n\t"
        "ds_read_b64_tr_b16 v[110:111], v198 offset:55632\n\t"
        "v_mfma_f32_16x16x32_bf16 v[60:63], v[148:151], v[132:135], v[60:63]\n\t"
        "ds_read_b64_tr_b16 v[112:113], v199 offset:55392\n\t"
        "v_mfma_f32_16x16x32_bf16 v[64:67], v[152:155], v[120:123], v[64:67]\n\t"
        "ds_read_b64_tr_b16 v[114:115], v199 offset:55536\n\t"
        "v_mfma_f32_16x16x32_bf16 v[68:71], v[152:155], v[124:127], v[68:71]\n\t"
        "s_waitcnt lgkmcnt(0)\n\t"
        "v_mfma_f32_16x16x32_bf16 v[0:3], v[96:99], v[80:83], v[0:3]\n\t"
        "ds_read_b128 v[120:123], v218 offset:49152\n\t"
        "ds_read_b64_tr_b16 v[136:137], v198 offset:60000\n\t"
        "v_mfma_f32_16x16x32_bf16 v[16:19], v[96:99], v[84:87], v[16:19]\n\t"
        "ds_read_b64_tr_b16 v[138:139], v198 offset:60144\n\t"
        "v_mfma_f32_16x16x32_bf16 v[32:35], v[96:99], v[88:91], v[32:35]\n\t"
        "ds_read_b128 v[124:127], v218 offset:51200\n\t"
        "v_mfma_f32_16x16x32_bf16 v[48:51], v[96:99], v[92:95], v[48:51]\n\t"
        "ds_read_b128 v[128:131], v219 offset:49152\n\t"
        "v_mfma_f32_16x16x32_bf16 v[4:7], v[100:103], v[80:83], v[4:7]\n\t"
        "ds_read_b128 v[132:135], v219 offset:51200\n\t"
        "ds_read_b64_tr_b16 v[140:141], v198 offset:60032\n\t"
        "v_mfma_f32_16x16x32_bf16 v[20:23], v[100:103], v[84:87], v[20:23]\n\t"
        "ds_read_b64_tr_b16 v[142:143], v198 offset:60176\n\t"
        "v_mfma_f32_16x16x32_bf16 v[36:39], v[100:103], v[88:91], v[36:39]\n\t"
        "ds_read_b64_tr_b16 v[144:145], v198 offset:60064\n\t"
        "v_mfma_f32_16x16x32_bf16 v[52:55], v[100:103], v[92:95], v[52:55]\n\t"
        "ds_read_b64_tr_b16 v[146:147], v198 offset:60208\n\t"
        "v_mfma_f32_16x16x32_bf16 v[8:11], v[104:107], v[80:83], v[8:11]\n\t"
        "ds_read_b64_tr_b16 v[148:149], v198 offset:60096\n\t"
        "v_mfma_f32_16x16x32_bf16 v[24:27], v[104:107], v[84:87], v[24:27]\n\t"
        "ds_read_b64_tr_b16 v[150:151], v198 offset:60240\n\t"
        "ds_read_b64_tr_b16 v[152:153], v199 offset:60000\n\t"
        "v_mfma_f32_16x16x32_bf16 v[40:43], v[104:107], v[88:91], v[40:43]\n\t"
        "ds_read_b64_tr_b16 v[154:155], v199 offset:60144\n\t"
        "v_mfma_f32_16x16x32_bf16 v[56:59], v[104:107], v[92:95], v[56:59]\n\t"
        "global_load_ushort v160, v204, %[wob]\n\t"
        "global_load_ushort v161, v204, %[wob] offset:1152\n\t"
        "v_mfma_f32_16x16x32_bf16 v[12:15], v[108:111], v[80:83], v[12:15]\n\t"
        "global_load_ushort v162, v205, %[wob]\n\t"
        "global_load_ushort v163, v205, %[wob] offset:1152\n\t"
        "v_mfma_f32_16x16x32_bf16 v[28:31], v[108:111], v[84:87], v[28:31]\n\t"
        "global_load_ushort v164, v206, %[wob]\n\t"
        "global_load_ushort v165, v206, %[wob] offset:1152\n\t"
        "global_load_ushort v166, v207, %[wob]\n\t"
        "global_load_ushort v167, v207, %[wob] offset:1152\n\t"
        "v_mfma_f32_16x16x32_bf16 v[44:47], v[108:111], v[88:91], v[44:47]\n\t"
        "s_mov_b64 exec, %[wom]\n\t"
        "global_load_ushort v168, v208, %[wob]\n\t"
        "global_load_ushort v169, v208, %[wob] offset:1152\n\t"
        "s_mov_b64 exec, -1\n\t"
        "v_mfma_f32_16x16x32_bf16 v[60:63], v[108:111], v[92:95], v[60:63]\n\t"
        "s_mov_b64 exec, %[wom]\n\t"
        "global_load_ushort v170, v209, %[wob]\n\t"
        "global_load_ushort v171, v209, %[wob] offset:1152\n\t"
        "s_mov_b64 exec, -1\n\t"
        "v_mfma_f32_16x16x32_bf16 v[64:67], v[112:115], v[80:83], v[64:67]\n\t"
        "s_mov_b64 exec, %[wom]\n\t"
        "global_load_ushort v172, v210, %[wob]\n\t"
        "global_load_ushort v173, v210, %[wob] offset:1152\n\t"
        "s_mov_b64 exec, -1\n\t"
        "v_mfma_f32_16x16x32_bf16 v[68:71], v[112:115], v[84:87], v[68:71]\n\t"
        "s_mov_b64 exec, %[wom]\n\t"
        "global_load_ushort v174, v211, %[wob]\n\t"
        "global_load_ushort v175, v211, %[wob] offset:1152\n\t"
        "s_mov_b64 exec, -1\n\t"
        "s_waitcnt lgkmcnt(0)\n\t"
        "s_barrier\n\t"
        "v_mfma_f32_16x16x32_bf16 v[0:3], v[136:139], v[120:123], v[0:3]\n\t"
        "ds_read_b128 v[80:83], v196 offset:0\n\t"
        "v_mfma_f32_16x16x32_bf16 v[16:19], v[136:139], v[124:127], v[16:19]\n\t"
        "ds_read_b64_tr_b16 v[96:97], v198 offset:0\n\t"
        "v_mfma_f32_16x16x32_bf16 v[32:35], v[136:139], v[128:131], v[32:35]\n\t"
        "ds_read_b64_tr_b16 v[98:99], v198 offset:144\n\t"
        "v_mfma_f32_16x16x32_bf16 v[48:51], v[136:139], v[132:135], v[48:51]\n\t"
        "ds_read_b128 v[84:87], v196 offset:2048\n\t"
        "v_mfma_f32_16x16x32_bf16 v[4:7], v[140:143], v[120:123], v[4:7]\n\t"
        "v_mfma_f32_16x16x32_bf16 v[20:23], v[140:143], v[124:127], v[20:23]\n\t"
        "ds_read_b128 v[88:91], v197 offset:0\n\t"
        "v_mfma_f32_16x16x32_bf16 v[36:39], v[140:143], v[128:131], v[36:39]\n\t"
        "ds_read_b128 v[92:95], v197 offset:2048\n\t"
        "v_mfma_f32_16x16x32_bf16 v[52:55], v[140:143], v[132:135], v[52:55]\n\t"
        "ds_read_b64_tr_b16 v[100:101], v198 offset:32\n\t"
        "v_mfma_f32_16x16x32_bf16 v[8:11], v[144:147], v[120:123], v[8:11]\n\t"
        "v_mfma_f32_16x16x32_bf16 v[24:27], v[144:147], v[124:127], v[24:27]\n\t"
        "ds_read_b64_tr_b16 v[102:103], v198 offset:176\n\t"
        "v_mfma_f32_16x16x32_bf16 v[40:43], v[144:147], v[128:131], v[40:43]\n\t"
        "ds_read_b64_tr_b16 v[104:105], v198 offset:64\n\t"
        "v_mfma_f32_16x16x32_bf16 v[56:59], v[144:147], v[132:135], v[56:59]\n\t"
        "ds_read_b64_tr_b16 v[106:107], v198 offset:208\n\t"
        "v_mfma_f32_16x16x32_bf16 v[12:15], v[148:151], v[120:123], v[12:15]\n\t"
        "ds_read_b64_tr_b16 v[108:109], v198 offset:96\n\t"
        "v_mfma_f32_16x16x32_bf16 v[28:31], v[148:151], v[124:127], v[28:31]\n\t"
        "v_mfma_f32_16x16x32_bf16 v[44:47], v[148:151], v[128:131], v[44:47]\n\t"
        "ds_read_b64_tr_b16 v[110:111], v198 offset:240\n\t"
        "v_mfma_f32_16x16x32_bf16 v[60:63], v[148:151], v[132:135], v[60:63]\n\t"
        "ds_read_b64_tr_b16 v[112:113], v199 offset:0\n\t"
        "v_mfma_f32_16x16x32_bf16 v[64:67], v[152:155], v[120:123], v[64:67]\n\t"
        "ds_read_b64_tr_b16 v[114:115], v199 offset:144\n\t"
        "v_mfma_f32_16x16x32_bf16 v[68:71], v[152:155], v[124:127], v[68:71]\n\t"
        "s_waitcnt lgkmcnt(0)\n\t"
        "v_mfma_f32_16x16x32_bf16 v[0:3], v[96:99], v[80:83], v[0:3]\n\t"
        "ds_read_b128 v[120:123], v218 offset:0\n\t"
        "v_mfma_f32_16x16x32_bf16 v[16:19], v[96:99], v[84:87], v[16:19]\n\t"
        "ds_read_b64_tr_b16 v[136:137], v198 offset:4608\n\t"
        "v_mfma_f32_16x16x32_bf16 v[32:35], v[96:99], v[88:91], v[32:35]\n\t"
        "ds_read_b64_tr_b16 v[138:139], v198 offset:4752\n\t"
        "v_mfma_f32_16x16x32_bf16 v[48:51], v[96:99], v[92:95], v[48:51]\n\t"
        "ds_read_b128 v[124:127], v218 offset:2048\n\t"
        "v_mfma_f32_16x16x32_bf16 v[4:7], v[100:103], v[80:83], v[4:7]\n\t"
        "ds_read_b128 v[128:131], v219 offset:0\n\t"
        "v_mfma_f32_16x16x32_bf16 v[20:23], v[100:103], v[84:87], v[20:23]\n\t"
        "v_mfma_f32_16x16x32_bf16 v[36:39], v[100:103], v[88:91], v[36:39]\n\t"
        "ds_read_b128 v[132:135], v219 offset:2048\n\t"
        "v_mfma_f32_16x16x32_bf16 v[52:55], v[100:103], v[92:95], v[52:55]\n\t"
        "ds_read_b64_tr_b16 v[140:141], v198 offset:4640\n\t"
        "v_mfma_f32_16x16x32_bf16 v[8:11], v[104:107], v[80:83], v[8:11]\n\t"
        "ds_read_b64_tr_b16 v[142:143], v198 offset:4784\n\t"
        "v_mfma_f32_16x16x32_bf16 v[24:27], v[104:107], v[84:87], v[24:27]\n\t"
        "ds_read_b64_tr_b16 v[144:145], v198 offset:4672\n\t"
        "v_mfma_f32_16x16x32_bf16 v[40:43], v[104:107], v[88:91], v[40:43]\n\t"
        "ds_read_b64_tr_b16 v[146:147], v198 offset:4816\n\t"
        "v_mfma_f32_16x16x32_bf16 v[56:59], v[104:107], v[92:95], v[56:59]\n\t"
        "v_mfma_f32_16x16x32_bf16 v[12:15], v[108:111], v[80:83], v[12:15]\n\t"
        "ds_read_b64_tr_b16 v[148:149], v198 offset:4704\n\t"
        "v_mfma_f32_16x16x32_bf16 v[28:31], v[108:111], v[84:87], v[28:31]\n\t"
        "ds_read_b64_tr_b16 v[150:151], v198 offset:4848\n\t"
        "v_mfma_f32_16x16x32_bf16 v[44:47], v[108:111], v[88:91], v[44:47]\n\t"
        "ds_read_b64_tr_b16 v[152:153], v199 offset:4608\n\t"
        "v_mfma_f32_16x16x32_bf16 v[60:63], v[108:111], v[92:95], v[60:63]\n\t"
        "ds_read_b64_tr_b16 v[154:155], v199 offset:4752\n\t"
        "v_mfma_f32_16x16x32_bf16 v[64:67], v[112:115], v[80:83], v[64:67]\n\t"
        "s_mov_b32 exec_lo, 0x0011ffff\n\t"
        "s_mov_b32 exec_hi, 0x0011ffff\n\t"
        "ds_read_u16 v176, v245 offset:0\n\t"
        "ds_read_u16 v177, v245 offset:36\n\t"
        "ds_read_u16 v178, v245 offset:72\n\t"
        "ds_read_u16 v179, v245 offset:108\n\t"
        "ds_read_u16 v180, v245 offset:144\n\t"
        "ds_read_u16 v181, v245 offset:180\n\t"
        "ds_read_u16 v182, v245 offset:216\n\t"
        "ds_read_u16 v183, v245 offset:252\n\t"
        "s_mov_b32 exec_hi, 0\n\t"
        "ds_read_u16 v192, v245 offset:576\n\t"
        "ds_read_u16 v193, v245 offset:612\n\t"
        "s_mov_b64 exec, -1\n\t"
        "v_mfma_f32_16x16x32_bf16 v[68:71], v[112:115], v[84:87], v[68:71]\n\t"
        "s_waitcnt lgkmcnt(0)\n\t"
        "s_cmp_eq_u32 %[isq], 0\n\t"
        "s_cbranch_scc1 65f\n\t"
        "v_mfma_f32_16x16x32_bf16 v[64:67], v[152:155], v[120:123], v[64:67]\n\t"
        "v_lshl_or_b32 v184, v177, 16, v176\n\t"
        "v_lshl_or_b32 v185, v179, 16, v178\n\t"
        "v_mfma_f32_16x16x32_bf16 v[68:71], v[152:155], v[124:127], v[68:71]\n\t"
        "v_lshl_or_b32 v186, v181, 16, v180\n\t"
        "v_lshl_or_b32 v187, v183, 16, v182\n\t"
        "v_mfma_f32_16x16x32_bf16 v[0:3], v[136:139], v[120:123], v[0:3]\n\t"
        "v_lshl_or_b32 v188, v193, 16, v192\n\t"
        "s_nop 0\n\t"
        "v_mfma_f32_16x16x32_bf16 v[16:19], v[136:139], v[124:127], v[16:19]\n\t"
        "v_pk_mul_f32 v[64:65], v[64:65], v[214:215] op_sel_hi:[1,0]\n\t"
        "v_pk_mul_f32 v[66:67], v[66:67], v[214:215] op_sel_hi:[1,0]\n\t"
        "v_cvt_pk_bf16_f32 v80, v64, v65\n\t"
        "v_cvt_pk_bf16_f32 v81, v66, v67\n\t"
        "ds_write_b64 v215, v[80:81] offset:0\n\t"
        "v_mfma_f32_16x16x32_bf16 v[32:35], v[136:139], v[128:131], v[32:35]\n\t"
        "v_pk_mul_f32 v[68:69], v[68:69], v[214:215] op_sel_hi:[1,0]\n\t"
        "v_pk_mul_f32 v[70:71], v[70:71], v[214:215] op_sel_hi:[1,0]\n\t"
        "v_cvt_pk_bf16_f32 v82, v68, v69\n\t"
        "v_cvt_pk_bf16_f32 v83, v70, v71\n\t"
        "ds_write_b64 v215, v[82:83] offset:4736\n\t"
        "v_mfma_f32_16x16x32_bf16 v[48:51], v[136:139], v[132:135], v[48:51]\n\t"
        "v_pk_mul_f32 v[0:1], v[0:1], s[44:45]\n\t"
        "v_pk_mul_f32 v[2:3], v[2:3], s[44:45]\n\t"
        "v_cvt_pk_bf16_f32 v84, v0, v1\n\t"
        "v_cvt_pk_bf16_f32 v85, v2, v3\n\t"
        "ds_write_b64 v212, v[84:85] offset:0\n\t"
        "v_mfma_f32_16x16x32_bf16 v[4:7], v[140:143], v[120:123], v[4:7]\n\t"
        "v_pk_mul_f32 v[16:17], v[16:17], s[44:45]\n\t"
        "v_pk_mul_f32 v[18:19], v[18:19], s[44:45]\n\t"
        "v_cvt_pk_bf16_f32 v86, v16, v17\n\t"
        "v_cvt_pk_bf16_f32 v87, v18, v19\n\t"
        "ds_write_b64 v212, v[86:87] offset:4736\n\t"
        "v_mfma_f32_16x16x32_bf16 v[20:23], v[140:143], v[124:127], v[20:23]\n\t"
        "v_pk_mul_f32 v[32:33], v[32:33], s[44:45]\n\t"
        "v_pk_mul_f32 v[34:35], v[34:35], s[44:45]\n\t"
        "v_cvt_pk_bf16_f32 v88, v32, v33\n\t"
        "v_cvt_pk_bf16_f32 v89, v34, v35\n\t"
        "ds_write_b64 v213, v[88:89] offset:9472\n\t"
        "v_mfma_f32_16x16x32_bf16 v[36:39], v[140:143], v[128:131], v[36:39]\n\t"
        "v_pk_mul_f32 v[48:49], v[48:49], s[44:45]\n\t"
        "v_pk_mul_f32 v[50:51], v[50:51], s[44:45]\n\t"
        "v_cvt_pk_bf16_f32 v90, v48, v49\n\t"
        "v_cvt_pk_bf16_f32 v91, v50, v51\n\t"
        "ds_write_b64 v213, v[90:91] offset:14208\n\t"
        "v_mfma_f32_16x16x32_bf16 v[52:55], v[140:143], v[132:135], v[52:55]\n\t"
        "v_pk_mul_f32 v[4:5], v[4:5], s[44:45]\n\t"
        "v_pk_mul_f32 v[6:7], v[6:7], s[44:45]\n\t"
        "v_cvt_pk_bf16_f32 v92, v4, v5\n\t"
        "v_cvt_pk_bf16_f32 v93, v6, v7\n\t"
        "ds_write_b64 v212, v[92:93] offset:32\n\t"
        "v_mfma_f32_16x16x32_bf16 v[8:11], v[144:147], v[120:123], v[8:11]\n\t"
        "v_pk_mul_f32 v[20:21], v[20:21], s[44:45]\n\t"
        "v_pk_mul_f32 v[22:23], v[22:23], s[44:45]\n\t"
        "v_cvt_pk_bf16_f32 v94, v20, v21\n\t"
        "v_cvt_pk_bf16_f32 v95, v22, v23\n\t"
        "ds_write_b64 v212, v[94:95] offset:4768\n\t"
        "v_mfma_f32_16x16x32_bf16 v[24:27], v[144:147], v[124:127], v[24:27]\n\t"
        "v_pk_mul_f32 v[36:37], v[36:37], s[44:45]\n\t"
        "v_pk_mul_f32 v[38:39], v[38:39], s[44:45]\n\t"
        "v_cvt_pk_bf16_f32 v80, v36, v37\n\t"
        "v_cvt_pk_bf16_f32 v81, v38, v39\n\t"
        "ds_write_b64 v213, v[80:81] offset:9504\n\t"
        "v_mfma_f32_16x16x32_bf16 v[40:43], v[144:147], v[128:131], v[40:43]\n\t"
        "v_pk_mul_f32 v[52:53], v[52:53], s[44:45]\n\t"
        "v_pk_mul_f32 v[54:55], v[54:55], s[44:45]\n\t"
        "v_cvt_pk_bf16_f32 v82, v52, v53\n\t"
        "v_cvt_pk_bf16_f32 v83, v54, v55\n\t"
        "ds_write_b64 v213, v[82:83] offset:14240\n\t"
        "v_mfma_f32_16x16x32_bf16 v[56:59], v[144:147], v[132:135], v[56:59]\n\t"
        "v_pk_mul_f32 v[8:9], v[8:9], s[44:45]\n\t"
        "v_pk_mul_f32 v[10:11], v[10:11], s[44:45]\n\t"
        "v_cvt_pk_bf16_f32 v84, v8, v9\n\t"
        "v_cvt_pk_bf16_f32 v85, v10, v11\n\t"
        "ds_write_b64 v212, v[84:85] offset:64\n\t"
        "v_mfma_f32_16x16x32_bf16 v[12:15], v[148:151], v[120:123], v[12:15]\n\t"
        "v_pk_mul_f32 v[24:25], v[24:25], s[44:45]\n\t"
        "v_pk_mul_f32 v[26:27], v[26:27], s[44:45]\n\t"
        "v_cvt_pk_bf16_f32 v86, v24, v25\n\t"
        "v_cvt_pk_bf16_f32 v87, v26, v27\n\t"
        "ds_write_b64 v212, v[86:87] offset:4800\n\t"
        "v_mfma_f32_16x16x32_bf16 v[28:31], v[148:151], v[124:127], v[28:31]\n\t"
        "v_pk_mul_f32 v[40:41], v[40:41], s[44:45]\n\t"
        "v_pk_mul_f32 v[42:43], v[42:43], s[44:45]\n\t"
        "v_cvt_pk_bf16_f32 v88, v40, v41\n\t"
        "v_cvt_pk_bf16_f32 v89, v42, v43\n\t"
        "ds_write_b64 v213, v[88:89] offset:9536\n\t"
        "v_mfma_f32_16x16x32_bf16 v[44:47], v[148:151], v[128:131], v[44:47]\n\t"
        "v_pk_mul_f32 v[56:57], v[56:57], s[44:45]\n\t"
        "v_pk_mul_f32 v[58:59], v[58:59], s[44:45]\n\t"
        "v_cvt_pk_bf16_f32 v90, v56, v57\n\t"
        "v_cvt_pk_bf16_f32 v91, v58, v59\n\t"
        "ds_write_b64 v213, v[90:91] offset:14272\n\t"
        "v_mfma_f32_16x16x32_bf16 v[60:63], v[148:151], v[132:135], v[60:63]\n\t"
        "v_pk_mul_f32 v[12:13], v[12:13], s[44:45]\n\t"
        "v_pk_mul_f32 v[14:15], v[14:15], s[44:45]\n\t"
        "v_cvt_pk_bf16_f32 v92, v12, v13\n\t"
        "v_cvt_pk_bf16_f32 v93, v14, v15\n\t"
        "ds_write_b64 v212, v[92:93] offset:96\n\t"
        "v_pk_mul_f32 v[28:29], v[28:29], s[44:45]\n\t"
        "v_pk_mul_f32 v[30:31], v[30:31], s[44:45]\n\t"
        "v_cvt_pk_bf16_f32 v94, v28, v29\n\t"
        "v_cvt_pk_bf16_f32 v95, v30, v31\n\t"
        "ds_write_b64 v212, v[94:95] offset:4832\n\t"
        "v_pk_mul_f32 v[44:45], v[44:45], s[44:45]\n\t"
        "v_pk_mul_f32 v[46:47], v[46:47], s[44:45]\n\t"
        "v_cvt_pk_bf16_f32 v80, v44, v45\n\t"
        "v_cvt_pk_bf16_f32 v81, v46, v47\n\t"
        "ds_write_b64 v213, v[80:81] offset:9568\n\t"
        "v_pk_mul_f32 v[60:61], v[60:61], s[44:45]\n\t"
        "v_pk_mul_f32 v[62:63], v[62:63], s[44:45]\n\t"
        "v_cvt_pk_bf16_f32 v82, v60, v61\n\t"
        "v_cvt_pk_bf16_f32 v83, v62, v63\n\t"
        "ds_write_b64 v213, v[82:83] offset:14304\n\t"
        "s_branch 66f\n\t"
        "65:\n\t"
        "v_mfma_f32_16x16x32_bf16 v[64:67], v[152:155], v[120:123], v[64:67]\n\t"
        "v_lshl_or_b32 v184, v177, 16, v176\n\t"
        "v_lshl_or_b32 v185, v179, 16, v178\n\t"
        "v_mfma_f32_16x16x32_bf16 v[68:71], v[152:155], v[124:127], v[68:71]\n\t"
        "v_lshl_or_b32 v186, v181, 16, v180\n\t"
        "v_lshl_or_b32 v187, v183, 16, v182\n\t"
        "v_mfma_f32_16x16x32_bf16 v[0:3], v[136:139], v[120:123], v[0:3]\n\t"
        "v_lshl_or_b32 v188, v193, 16, v192\n\t"
        "s_nop 0\n\t"
        "v_mfma_f32_16x16x32_bf16 v[16:19], v[136:139], v[124:127], v[16:19]\n\t"
        "v_cvt_pk_bf16_f32 v80, v64, v65\n\t"
        "v_cvt_pk_bf16_f32 v81, v66, v67\n\t"
        "ds_write_b64 v215, v[80:81] offset:0\n\t"
        "v_mfma_f32_16x16x32_bf16 v[32:35], v[136:139], v[128:131], v[32:35]\n\t"
        "v_cvt_pk_bf16_f32 v82, v68, v69\n\t"
        "v_cvt_pk_bf16_f32 v83, v70, v71\n\t"
        "ds_write_b64 v215, v[82:83] offset:4736\n\t"
        "v_mfma_f32_16x16x32_bf16 v[48:51], v[136:139], v[132:135], v[48:51]\n\t"
        "v_cvt_pk_bf16_f32 v84, v0, v1\n\t"
        "v_cvt_pk_bf16_f32 v85, v2, v3\n\t"
        "ds_write_b64 v212, v[84:85] offset:0\n\t"
        "v_mfma_f32_16x16x32_bf16 v[4:7], v[140:143], v[120:123], v[4:7]\n\t"
        "v_cvt_pk_bf16_f32 v86, v16, v17\n\t"
        "v_cvt_pk_bf16_f32 v87, v18, v19\n\t"
        "ds_write_b64 v212, v[86:87] offset:4736\n\t"
        "v_mfma_f32_16x16x32_bf16 v[20:23], v[140:143], v[124:127], v[20:23]\n\t"
        "v_cvt_pk_bf16_f32 v88, v32, v33\n\t"
        "v_cvt_pk_bf16_f32 v89, v34, v35\n\t"
        "ds_write_b64 v213, v[88:89] offset:9472\n\t"
        "v_mfma_f32_16x16x32_bf16 v[36:39], v[140:143], v[128:131], v[36:39]\n\t"
        "v_cvt_pk_bf16_f32 v90, v48, v49\n\t"
        "v_cvt_pk_bf16_f32 v91, v50, v51\n\t"
        "ds_write_b64 v213, v[90:91] offset:14208\n\t"
        "v_mfma_f32_16x16x32_bf16 v[52:55], v[140:143], v[132:135], v[52:55]\n\t"
        "v_cvt_pk_bf16_f32 v92, v4, v5\n\t"
        "v_cvt_pk_bf16_f32 v93, v6, v7\n\t"
        "ds_write_b64 v212, v[92:93] offset:32\n\t"
        "v_mfma_f32_16x16x32_bf16 v[8:11], v[144:147], v[120:123], v[8:11]\n\t"
        "v_cvt_pk_bf16_f32 v94, v20, v21\n\t"
        "v_cvt_pk_bf16_f32 v95, v22, v23\n\t"
        "ds_write_b64 v212, v[94:95] offset:4768\n\t"
        "v_mfma_f32_16x16x32_bf16 v[24:27], v[144:147], v[124:127], v[24:27]\n\t"
        "v_cvt_pk_bf16_f32 v80, v36, v37\n\t"
        "v_cvt_pk_bf16_f32 v81, v38, v39\n\t"
        "ds_write_b64 v213, v[80:81] offset:9504\n\t"
        "v_mfma_f32_16x16x32_bf16 v[40:43], v[144:147], v[128:131], v[40:43]\n\t"
        "v_cvt_pk_bf16_f32 v82, v52, v53\n\t"
        "v_cvt_pk_bf16_f32 v83, v54, v55\n\t"
        "ds_write_b64 v213, v[82:83] offset:14240\n\t"
        "v_mfma_f32_16x16x32_bf16 v[56:59], v[144:147], v[132:135], v[56:59]\n\t"
        "v_cvt_pk_bf16_f32 v84, v8, v9\n\t"
        "v_cvt_pk_bf16_f32 v85, v10, v11\n\t"
        "ds_write_b64 v212, v[84:85] offset:64\n\t"
        "v_mfma_f32_16x16x32_bf16 v[12:15], v[148:151], v[120:123], v[12:15]\n\t"
        "v_cvt_pk_bf16_f32 v86, v24, v25\n\t"
        "v_cvt_pk_bf16_f32 v87, v26, v27\n\t"
        "ds_write_b64 v212, v[86:87] offset:4800\n\t"
        "v_mfma_f32_16x16x32_bf16 v[28:31], v[148:151], v[124:127], v[28:31]\n\t"
        "v_cvt_pk_bf16_f32 v88, v40, v41\n\t"
        "v_cvt_pk_bf16_f32 v89, v42, v43\n\t"
        "ds_write_b64 v213, v[88:89] offset:9536\n\t"
        "v_mfma_f32_16x16x32_bf16 v[44:47], v[148:151], v[128:131], v[44:47]\n\t"
        "v_cvt_pk_bf16_f32 v90, v56, v57\n\t"
        "v_cvt_pk_bf16_f32 v91, v58, v59\n\t"
        "ds_write_b64 v213, v[90:91] offset:14272\n\t"
        "v_mfma_f32_16x16x32_bf16 v[60:63], v[148:151], v[132:135], v[60:63]\n\t"
        "v_cvt_pk_bf16_f32 v92, v12, v13\n\t"
        "v_cvt_pk_bf16_f32 v93, v14, v15\n\t"
        "ds_write_b64 v212, v[92:93] offset:96\n\t"
        "v_cvt_pk_bf16_f32 v94, v28, v29\n\t"
        "v_cvt_pk_bf16_f32 v95, v30, v31\n\t"
        "ds_write_b64 v212, v[94:95] offset:4832\n\t"
        "v_cvt_pk_bf16_f32 v80, v44, v45\n\t"
        "v_cvt_pk_bf16_f32 v81, v46, v47\n\t"
        "ds_write_b64 v213, v[80:81] offset:9568\n\t"
        "s_nop 1\n\t"
        "v_cvt_pk_bf16_f32 v82, v60, v61\n\t"
        "v_cvt_pk_bf16_f32 v83, v62, v63\n\t"
        "ds_write_b64 v213, v[82:83] offset:14304\n\t"
        "66:\n\t"
        "s_branch 69f\n\t"
        "61:\n\t"
        "v_xor_b32 v218, 64, v196\n\t"
        "v_xor_b32 v219, 64, v197\n\t"
        "s_mov_b32 s44, %[sc0]\n\t"
        "s_mov_b32 s45, %[sc0]\n\t"
        "s_and_b32 s46, %[w], 3\n\t"
        "s_lshr_b32 s47, %[w], 2\n\t"
        "v_and_b32 v240, 31, v244\n\t"
        "v_lshrrev_b32 v241, 5, v244\n\t"
        "s_mul_i32 s48, s47, 18944\n\t"
        "s_mul_i32 s49, s46, 36\n\t"
        "s_add_u32 s48, s48, s49\n\t"
        "s_add_u32 s48, s48, 16384\n\t"
        "v_mul_u32_u24 v242, 296, v240\n\t"
        "v_lshlrev_b32 v243, 4, v241\n\t"
        "v_add3_u32 v228, v242, v243, s48\n\t"
        "v_add_u32 v230, s48, v242\n\t"
        "v_add_u32 v229, 9472, v228\n\t"
        "s_mul_i32 s48, s47, 22528\n\t"
        "s_mul_i32 s49, s46, 80\n\t"
        "s_add_u32 s48, s48, s49\n\t"
        "s_add_u32 s48, s48, 65536\n\t"
        "v_mul_u32_u24 v242, 352, v240\n\t"
        "v_add3_u32 v231, v242, v243, s48\n\t"
        "v_mul_u32_u24 v243, 12, v241\n\t"
        "v_sub_u32 v232, v231, v243\n\t"
        "v_add_u32 v232, 64, v232\n\t"
        "v_lshlrev_b32 v233, 4, v244\n\t"
        "v_lshlrev_b32 v234, 2, v244\n\t"
        "v_bfe_u32 v240, v244, 2, 2\n\t"
        "v_and_b32 v241, 3, v244\n\t"
        "v_lshrrev_b32 v242, 4, v244\n\t"
        "v_lshl_add_u32 v240, v242, 2, v240\n\t"
        "v_lshlrev_b32 v241, 3, v241\n\t"
        "v_mul_u32_u24 v243, 352, v240\n\t"
        "v_add3_u32 v235, v243, v241, s48\n\t"
        "s_mul_i32 s48, s47, 18944\n\t"
        "s_mul_i32 s49, s46, 36\n\t"
        "s_add_u32 s48, s48, s49\n\t"
        "s_and_b32 s49, s46, 1\n\t"
        "s_lshl_b32 s49, s49, 2\n\t"
        "s_sub_u32 s48, s48, s49\n\t"
        "s_add_u32 s48, s48, 16528\n\t"
        "v_mul_u32_u24 v243, 296, v240\n\t"
        "v_add3_u32 v236, v243, v241, s48\n\t"
        "s_lshr_b32 s49, s49, 1\n\t"
        "s_sub_u32 s49, 2, s49\n\t"
        "v_and_b32 v243, 15, v244\n\t"
        "v_add_u32 v243, s49, v243\n\t"
        "v_lshlrev_b32 v241, 6, v243\n\t"
        "v_lshl_add_u32 v237, v242, 4, v241\n\t"
        "v_lshlrev_b32 v238, 4, v243\n\t"
        "v_bfe_u32 v240, v244, 3, 2\n\t"
        "v_and_b32 v241, 3, v244\n\t"
        "v_lshl_add_u32 v240, v240, 2, v241\n\t"
        "v_bfe_u32 v241, v244, 2, 1\n\t"
        "v_mul_u32_u24 v241, 9, v241\n\t"
        "v_add_u32 v241, v241, v240\n\t"
        "v_lshrrev_b32 v242, 5, v244\n\t"
        "v_mul_u32_u24 v242, 288, v242\n\t"
        "v_lshl_add_u32 v241, v241, 1, v242\n\t"
        "s_mul_i32 s48, s46, 648\n\t"
        "s_add_u32 s48, s48, 157824\n\t"
        "v_add_u32 v245, s48, v241\n\t"
        "v_mov_b32 v246, 0\n\t"
        "v_mov_b32 v247, 0\n\t"
        "v_mov_b32 v248, 0\n\t"
        "v_mov_b32 v249, 0\n\t"
        "v_mov_b32 v250, 0\n\t"
        "v_mov_b32 v251, 0\n\t"
        "v_mov_b32 v252, 0\n\t"
        "v_mov_b32 v253, 0\n\t"
        "v_mov_b32 v176, 0\n\t"
        "v_mov_b32 v177, 0\n\t"
        "v_mov_b32 v178, 0\n\t"
        "v_mov_b32 v179, 0\n\t"
        "v_mov_b32 v180, 0\n\t"
        "v_mov_b32 v181, 0\n\t"
        "v_mov_b32 v182, 0\n\t"
        "v_mov_b32 v183, 0\n\t"
        "v_mov_b32 v192, 0\n\t"
        "v_mov_b32 v193, 0\n\t"
        "v_mov_b32 v189, 0\n\t"
        "v_mov_b32 v190, 0\n\t"
        "v_mov_b32 v191, 0\n\t"
        "s_mov_b32 s52, %[blk]\n\t"
        "s_lshl_b32 s54, %[w], 10\n\t"
        "s_add_u32 s54, s54, 139392\n\t"
        "s_add_u32 s55, s54, 8192\n\t"
        "s_mov_b32 s50, 0x55555555\n\t"
        "s_mov_b32 s51, s50\n\t"
        "s_waitcnt vmcnt(0)\n\t"
        "s_barrier\n\t"
        "ds_read_b128 v[80:83], v196 offset:0\n\t"
        "ds_read_b64_tr_b16 v[96:97], v198 offset:0\n\t"
        "ds_read_b64_tr_b16 v[98:99], v198 offset:144\n\t"
        "ds_read_b128 v[84:87], v196 offset:2048\n\t"
        "ds_read_b128 v[88:91], v197 offset:0\n\t"
        "ds_read_b128 v[92:95], v197 offset:2048\n\t"
        "ds_read_b64_tr_b16 v[100:101], v198 offset:32\n\t"
        "ds_read_b64_tr_b16 v[102:103], v198 offset:176\n\t"
        "ds_read_b64_tr_b16 v[104:105], v198 offset:64\n\t"
        "ds_read_b64_tr_b16 v[106:107], v198 offset:208\n\t"
        "ds_read_b64_tr_b16 v[108:109], v198 offset:96\n\t"
        "ds_read_b64_tr_b16 v[110:111], v198 offset:240\n\t"
        "ds_read_b64_tr_b16 v[112:113], v199 offset:0\n\t"
        "ds_read_b64_tr_b16 v[114:115], v199 offset:144\n\t"
        "ds_read_b64_tr_b16 v[116:117], v200 offset:0\n\t"
        "ds_read_b64_tr_b16 v[118:119], v200 offset:256\n\t"
        "s_waitcnt lgkmcnt(0)\n\t"
        "v_mfma_f32_16x16x32_bf16 v[0:3], v[96:99], v[80:83], 0\n\t"
        "ds_read_b128 v[120:123], v218 offset:0\n\t"
        "v_mfma_f32_16x16x32_bf16 v[16:19], v[96:99], v[84:87], 0\n\t"
        "ds_read_b64_tr_b16 v[136:137], v198 offset:4608\n\t"
        "v_mfma_f32_16x16x32_bf16 v[32:35], v[96:99], v[88:91], 0\n\t"
        "s_and_b32 s53, s52, 0xff\n\t"
        "s_mul_i32 s53, s53, 1152\n\t"
        "s_add_u32 m0, s54, 4096\n\t"
        "v_add_co_u32 v224, vcc, s53, v220\n\t"
        "v_addc_co_u32 v225, vcc, 0, v221, vcc\n\t"
        "global_load_lds_dwordx4 v[224:225], off\n\t"
        "s_lshr_b32 s52, s52, 8\n\t"
        "v_mfma_f32_16x16x32_bf16 v[48:51], v[96:99], v[92:95], 0\n\t"
        "ds_read_b64_tr_b16 v[138:139], v198 offset:4752\n\t"
        "v_mfma_f32_16x16x32_bf16 v[4:7], v[100:103], v[80:83], 0\n\t"
        "ds_read_b128 v[124:127], v218 offset:2048\n\t"
        "v_mfma_f32_16x16x32_bf16 v[20:23], v[100:103], v[84:87], 0\n\t"
        "ds_read_b128 v[128:131], v219 offset:0\n\t"
        "v_mfma_f32_16x16x32_bf16 v[36:39], v[100:103], v[88:91], 0\n\t"
        "v_mfma_f32_16x16x32_bf16 v[52:55], v[100:103], v[92:95], 0\n\t"
        "ds_read_b128 v[132:135], v219 offset:2048\n\t"
        "v_mfma_f32_16x16x32_bf16 v[8:11], v[104:107], v[80:83], 0\n\t"
        "ds_read_b64_tr_b16 v[140:141], v198 offset:4640\n\t"
        "v_mfma_f32_16x16x32_bf16 v[24:27], v[104:107], v[84:87], 0\n\t"
        "ds_read_b64_tr_b16 v[142:143], v198 offset:4784\n\t"
        "v_mfma_f32_16x16x32_bf16 v[40:43], v[104:107], v[88:91], 0\n\t"
        "ds_read_b64_tr_b16 v[144:145], v198 offset:4672\n\t"
        "v_mfma_f32_16x16x32_bf16 v[56:59], v[104:107], v[92:95], 0\n\t"
        "ds_read_b64_tr_b16 v[146:147], v198 offset:4816\n\t"
        "v_mfma_f32_16x16x32_bf16 v[12:15], v[108:111], v[80:83], 0\n\t"
        "ds_read_b64_tr_b16 v[148:149], v198 offset:4704\n\t"
        "v_mfma_f32_16x16x32_bf16 v[28:31], v[108:111], v[84:87], 0\n\t"
        "v_mfma_f32_16x16x32_bf16 v[44:47], v[108:111], v[88:91], 0\n\t"
        "ds_read_b64_tr_b16 v[150:151], v198 offset:4848\n\t"
        "v_mfma_f32_16x16x32_bf16 v[60:63], v[108:111], v[92:95], 0\n\t"
        "ds_read_b64_tr_b16 v[152:153], v199 offset:4608\n\t"
        "v_mfma_f32_16x16x32_bf16 v[64:67], v[112:115], v[80:83], 0\n\t"
        "ds_read_b64_tr_b16 v[154:155], v199 offset:4752\n\t"
        "v_mfma_f32_16x16x32_bf16 v[68:71], v[112:115], v[84:87], 0\n\t"
        "ds_read_b64_tr_b16 v[156:157], v200 offset:1024\n\t"
        "v_mfma_f32_16x16x32_bf16 v[72:75], v[116:119], v[88:91], 0\n\t"
        "ds_read_b64_tr_b16 v[158:159], v200 offset:1280\n\t"
        "v_mfma_f32_16x16x32_bf16 v[76:79], v[116:119], v[92:95], 0\n\t"
        "s_waitcnt lgkmcnt(0)\n\t"
        "s_barrier\n\t"
        "v_mfma_f32_16x16x32_bf16 v[0:3], v[136:139], v[120:123], v[0:3]\n\t"
        "ds_read_b128 v[80:83], v196 offset:16384\n\t"
        "v_mfma_f32_16x16x32_bf16 v[16:19], v[136:139], v[124:127], v[16:19]\n\t"
        "ds_read_b64_tr_b16 v[96:97], v198 offset:18464\n\t"
        "v_mfma_f32_16x16x32_bf16 v[32:35], v[136:139], v[128:131], v[32:35]\n\t"
        "ds_read_b64_tr_b16 v[98:99], v198 offset:18608\n\t"
        "v_mfma_f32_16x16x32_bf16 v[48:51], v[136:139], v[132:135], v[48:51]\n\t"
        "ds_read_b128 v[84:87], v196 offset:18432\n\t"
        "v_mfma_f32_16x16x32_bf16 v[4:7], v[140:143], v[120:123], v[4:7]\n\t"
        "v_mfma_f32_16x16x32_bf16 v[20:23], v[140:143], v[124:127], v[20:23]\n\t"
        "ds_read_b128 v[88:91], v197 offset:16384\n\t"
        "v_mfma_f32_16x16x32_bf16 v[36:39], v[140:143], v[128:131], v[36:39]\n\t"
        "ds_read_b128 v[92:95], v197 offset:18432\n\t"
        "v_mfma_f32_16x16x32_bf16 v[52:55], v[140:143], v[132:135], v[52:55]\n\t"
        "ds_read_b64_tr_b16 v[100:101], v198 offset:18496\n\t"
        "v_mfma_f32_16x16x32_bf16 v[8:11], v[144:147], v[120:123], v[8:11]\n\t"
        "ds_read_b64_tr_b16 v[102:103], v198 offset:18640\n\t"
        "v_mfma_f32_16x16x32_bf16 v[24:27], v[144:147], v[124:127], v[24:27]\n\t"
        "v_mfma_f32_16x16x32_bf16 v[40:43], v[144:147], v[128:131], v[40:43]\n\t"
        "ds_read_b64_tr_b16 v[104:105], v198 offset:18528\n\t"
        "v_mfma_f32_16x16x32_bf16 v[56:59], v[144:147], v[132:135], v[56:59]\n\t"
        "ds_read_b64_tr_b16 v[106:107], v198 offset:18672\n\t"
        "v_mfma_f32_16x16x32_bf16 v[12:15], v[148:151], v[120:123], v[12:15]\n\t"
        "ds_read_b64_tr_b16 v[108:109], v198 offset:18560\n\t"
        "v_mfma_f32_16x16x32_bf16 v[28:31], v[148:151], v[124:127], v[28:31]\n\t"
        "ds_read_b64_tr_b16 v[110:111], v198 offset:18704\n\t"
        "v_mfma_f32_16x16x32_bf16 v[44:47], v[148:151], v[128:131], v[44:47]\n\t"
        "v_mfma_f32_16x16x32_bf16 v[60:63], v[148:151], v[132:135], v[60:63]\n\t"
        "ds_read_b64_tr_b16 v[112:113], v199 offset:18464\n\t"
        "v_mfma_f32_16x16x32_bf16 v[64:67], v[152:155], v[120:123], v[64:67]\n\t"
        "ds_read_b64_tr_b16 v[114:115], v199 offset:18608\n\t"
        "v_mfma_f32_16x16x32_bf16 v[68:71], v[152:155], v[124:127], v[68:71]\n\t"
        "ds_read_b64_tr_b16 v[116:117], v200 offset:2048\n\t"
        "v_mfma_f32_16x16x32_bf16 v[72:75], v[156:159], v[128:131], v[72:75]\n\t"
        "ds_read_b64_tr_b16 v[118:119], v200 offset:2304\n\t"
        "v_mfma_f32_16x16x32_bf16 v[76:79], v[156:159], v[132:135], v[76:79]\n\t"
        "s_waitcnt lgkmcnt(0)\n\t"
        "v_mfma_f32_16x16x32_bf16 v[0:3], v[96:99], v[80:83], v[0:3]\n\t"
        "ds_read_b128 v[120:123], v218 offset:16384\n\t"
        "v_mfma_f32_16x16x32_bf16 v[16:19], v[96:99], v[84:87], v[16:19]\n\t"
        "ds_read_b64_tr_b16 v[136:137], v198 offset:23072\n\t"
        "v_mfma_f32_16x16x32_bf16 v[32:35], v[96:99], v[88:91], v[32:35]\n\t"
        "ds_read_b64_tr_b16 v[138:139], v198 offset:23216\n\t"
        "v_mfma_f32_16x16x32_bf16 v[48:51], v[96:99], v[92:95], v[48:51]\n\t"
        "ds_read_b128 v[124:127], v218 offset:18432\n\t"
        "v_mfma_f32_16x16x32_bf16 v[4:7], v[100:103], v[80:83], v[4:7]\n\t"
        "ds_read_b128 v[128:131], v219 offset:16384\n\t"
        "v_mfma_f32_16x16x32_bf16 v[20:23], v[100:103], v[84:87], v[20:23]\n\t"
        "ds_read_b128 v[132:135], v219 offset:18432\n\t"
        "v_mfma_f32_16x16x32_bf16 v[36:39], v[100:103], v[88:91], v[36:39]\n\t"
        "v_mfma_f32_16x16x32_bf16 v[52:55], v[100:103], v[92:95], v[52:55]\n\t"
        "ds_read_b64_tr_b16 v[140:141], v198 offset:23104\n\t"
        "v_mfma_f32_16x16x32_bf16 v[8:11], v[104:107], v[80:83], v[8:11]\n\t"
        "ds_read_b64_tr_b16 v[142:143], v198 offset:23248\n\t"
        "v_mfma_f32_16x16x32_bf16 v[24:27], v[104:107], v[84:87], v[24:27]\n\t"
        "ds_read_b64_tr_b16 v[144:145], v198 offset:23136\n\t"
        "v_mfma_f32_16x16x32_bf16 v[40:43], v[104:107], v[88:91], v[40:43]\n\t"
        "ds_read_b64_tr_b16 v[146:147], v198 offset:23280\n\t"
        "v_mfma_f32_16x16x32_bf16 v[56:59], v[104:107], v[92:95], v[56:59]\n\t"
        "ds_read_b64_tr_b16 v[148:149], v198 offset:23168\n\t"
        "v_mfma_f32_16x16x32_bf16 v[12:15], v[108:111], v[80:83], v[12:15]\n\t"
        "ds_read_b64_tr_b16 v[150:151], v198 offset:23312\n\t"
        "v_mfma_f32_16x16x32_bf16 v[28:31], v[108:111], v[84:87], v[28:31]\n\t"
        "v_mfma_f32_16x16x32_bf16 v[44:47], v[108:111], v[88:91], v[44:47]\n\t"
        "ds_read_b64_tr_b16 v[152:153], v199 offset:23072\n\t"
        "v_mfma_f32_16x16x32_bf16 v[60:63], v[108:111], v[92:95], v[60:63]\n\t"
        "ds_read_b64_tr_b16 v[154:155], v199 offset:23216\n\t"
        "v_mfma_f32_16x16x32_bf16 v[64:67], v[112:115], v[80:83], v[64:67]\n\t"
        "ds_read_b64_tr_b16 v[156:157], v200 offset:3072\n\t"
        "v_mfma_f32_16x16x32_bf16 v[68:71], v[112:115], v[84:87], v[68:71]\n\t"
        "ds_read_b64_tr_b16 v[158:159], v200 offset:3328\n\t"
        "v_mfma_f32_16x16x32_bf16 v[72:75], v[116:119], v[88:91], v[72:75]\n\t"
        "s_waitcnt vmcnt(0)\n\t"
        "v_mfma_f32_16x16x32_bf16 v[76:79], v[116:119], v[92:95], v[76:79]\n\t"
        "s_waitcnt lgkmcnt(0)\n\t"
        "s_barrier\n\t"
        "v_mfma_f32_16x16x32_bf16 v[0:3], v[136:139], v[120:123], v[0:3]\n\t"
        "ds_read_b128 v[80:83], v196 offset:32768\n\t"
        "v_mfma_f32_16x16x32_bf16 v[16:19], v[136:139], v[124:127], v[16:19]\n\t"
        "ds_read_b64_tr_b16 v[96:97], v198 offset:36928\n\t"
        "v_mfma_f32_16x16x32_bf16 v[32:35], v[136:139], v[128:131], v[32:35]\n\t"
        "ds_read_b64_tr_b16 v[98:99], v198 offset:37072\n\t"
        "v_mfma_f32_16x16x32_bf16 v[48:51], v[136:139], v[132:135], v[48:51]\n\t"
        "ds_read_b128 v[84:87], v196 offset:34816\n\t"
        "v_mfma_f32_16x16x32_bf16 v[4:7], v[140:143], v[120:123], v[4:7]\n\t"
        "v_mfma_f32_16x16x32_bf16 v[20:23], v[140:143], v[124:127], v[20:23]\n\t"
        "ds_read_b128 v[88:91], v197 offset:32768\n\t"
        "v_mfma_f32_16x16x32_bf16 v[36:39], v[140:143], v[128:131], v[36:39]\n\t"
        "ds_read_b128 v[92:95], v197 offset:34816\n\t"
        "v_mfma_f32_16x16x32_bf16 v[52:55], v[140:143], v[132:135], v[52:55]\n\t"
        "ds_read_b64_tr_b16 v[100:101], v198 offset:36960\n\t"
        "v_mfma_f32_16x16x32_bf16 v[8:11], v[144:147], v[120:123], v[8:11]\n\t"
        "ds_read_b64_tr_b16 v[102:103], v198 offset:37104\n\t"
        "v_mfma_f32_16x16x32_bf16 v[24:27], v[144:147], v[124:127], v[24:27]\n\t"
        "v_mfma_f32_16x16x32_bf16 v[40:43], v[144:147], v[128:131], v[40:43]\n\t"
        "ds_read_b64_tr_b16 v[104:105], v198 offset:36992\n\t"
        "v_mfma_f32_16x16x32_bf16 v[56:59], v[144:147], v[132:135], v[56:59]\n\t"
        "ds_read_b64_tr_b16 v[106:107], v198 offset:37136\n\t"
        "v_mfma_f32_16x16x32_bf16 v[12:15], v[148:151], v[120:123], v[12:15]\n\t"
        "ds_read_b64_tr_b16 v[108:109], v198 offset:37024\n\t"
        "v_mfma_f32_16x16x32_bf16 v[28:31], v[148:151], v[124:127], v[28:31]\n\t"
        "ds_read_b64_tr_b16 v[110:111], v198 offset:37168\n\t"
        "v_mfma_f32_16x16x32_bf16 v[44:47], v[148:151], v[128:131], v[44:47]\n\t"
        "v_mfma_f32_16x16x32_bf16 v[60:63], v[148:151], v[132:135], v[60:63]\n\t"
        "ds_read_b64_tr_b16 v[112:113], v199 offset:36928\n\t"
        "v_mfma_f32_16x16x32_bf16 v[64:67], v[152:155], v[120:123], v[64:67]\n\t"
        "ds_read_b64_tr_b16 v[114:115], v199 offset:37072\n\t"
        "v_mfma_f32_16x16x32_bf16 v[68:71], v[152:155], v[124:127], v[68:71]\n\t"
        "ds_read_b64_tr_b16 v[116:117], v200 offset:4096\n\t"
        "v_mfma_f32_16x16x32_bf16 v[72:75], v[156:159], v[128:131], v[72:75]\n\t"
        "ds_read_b64_tr_b16 v[118:119], v200 offset:4352\n\t"
        "v_mfma_f32_16x16x32_bf16 v[76:79], v[156:159], v[132:135], v[76:79]\n\t"
        "s_waitcnt lgkmcnt(0)\n\t"
        "v_mfma_f32_16x16x32_bf16 v[0:3], v[96:99], v[80:83], v[0:3]\n\t"
        "ds_read_b128 v[120:123], v218 offset:32768\n\t"
        "v_mfma_f32_16x16x32_bf16 v[16:19], v[96:99], v[84:87], v[16:19]\n\t"
        "ds_read_b64_tr_b16 v[136:137], v198 offset:41536\n\t"
        "v_mfma_f32_16x16x32_bf16 v[32:35], v[96:99], v[88:91], v[32:35]\n\t"
        "s_and_b32 s53, s52, 0xff\n\t"
        "s_mul_i32 s53, s53, 1152\n\t"
        "s_add_u32 m0, s54, 8192\n\t"
        "v_add_co_u32 v224, vcc, s53, v220\n\t"
        "v_addc_co_u32 v225, vcc, 0, v221, vcc\n\t"
        "global_load_lds_dwordx4 v[224:225], off\n\t"
        "s_lshr_b32 s52, s52, 8\n\t"
        "v_mfma_f32_16x16x32_bf16 v[48:51], v[96:99], v[92:95], v[48:51]\n\t"
        "ds_read_b64_tr_b16 v[138:139], v198 offset:41680\n\t"
        "v_mfma_f32_16x16x32_bf16 v[4:7], v[100:103], v[80:83], v[4:7]\n\t"
        "ds_read_b128 v[124:127], v218 offset:34816\n\t"
        "v_mfma_f32_16x16x32_bf16 v[20:23], v[100:103], v[84:87], v[20:23]\n\t"
        "ds_read_b128 v[128:131], v219 offset:32768\n\t"
        "v_mfma_f32_16x16x32_bf16 v[36:39], v[100:103], v[88:91], v[36:39]\n\t"
        "v_mfma_f32_16x16x32_bf16 v[52:55], v[100:103], v[92:95], v[52:55]\n\t"
        "ds_read_b128 v[132:135], v219 offset:34816\n\t"
        "v_mfma_f32_16x16x32_bf16 v[8:11], v[104:107], v[80:83], v[8:11]\n\t"
        "ds_read_b64_tr_b16 v[140:141], v198 offset:41568\n\t"
        "v_mfma_f32_16x16x32_bf16 v[24:27], v[104:107], v[84:87], v[24:27]\n\t"
        "ds_read_b64_tr_b16 v[142:143], v198 offset:41712\n\t"
        "v_mfma_f32_16x16x32_bf16 v[40:43], v[104:107], v[88:91], v[40:43]\n\t"
        "ds_read_b64_tr_b16 v[144:145], v198 offset:41600\n\t"
        "v_mfma_f32_16x16x32_bf16 v[56:59], v[104:107], v[92:95], v[56:59]\n\t"
        "ds_read_b64_tr_b16 v[146:147], v198 offset:41744\n\t"
        "v_mfma_f32_16x16x32_bf16 v[12:15], v[108:111], v[80:83], v[12:15]\n\t"
        "ds_read_b64_tr_b16 v[148:149], v198 offset:41632\n\t"
        "v_mfma_f32_16x16x32_bf16 v[28:31], v[108:111], v[84:87], v[28:31]\n\t"
        "v_mfma_f32_16x16x32_bf16 v[44:47], v[108:111], v[88:91], v[44:47]\n\t"
        "ds_read_b64_tr_b16 v[150:151], v198 offset:41776\n\t"
        "v_mfma_f32_16x16x32_bf16 v[60:63], v[108:111], v[92:95], v[60:63]\n\t"
        "ds_read_b64_tr_b16 v[152:153], v199 offset:41536\n\t"
        "v_mfma_f32_16x16x32_bf16 v[64:67], v[112:115], v[80:83], v[64:67]\n\t"
        "ds_read_b64_tr_b16 v[154:155], v199 offset:41680\n\t"
        "v_mfma_f32_16x16x32_bf16 v[68:71], v[112:115], v[84:87], v[68:71]\n\t"
        "ds_read_b64_tr_b16 v[156:157], v200 offset:5120\n\t"
        "v_mfma_f32_16x16x32_bf16 v[72:75], v[116:119], v[88:91], v[72:75]\n\t"
        "ds_read_b64_tr_b16 v[158:159], v200 offset:5376\n\t"
        "v_mfma_f32_16x16x32_bf16 v[76:79], v[116:119], v[92:95], v[76:79]\n\t"
        "s_waitcnt lgkmcnt(0)\n\t"
        "s_barrier\n\t"
        "v_mfma_f32_16x16x32_bf16 v[0:3], v[136:139], v[120:123], v[0:3]\n\t"
        "ds_read_b128 v[80:83], v196 offset:49152\n\t"
        "v_mfma_f32_16x16x32_bf16 v[16:19], v[136:139], v[124:127], v[16:19]\n\t"
        "ds_read_b64_tr_b16 v[96:97], v198 offset:55392\n\t"
        "v_mfma_f32_16x16x32_bf16 v[32:35], v[136:139], v[128:131], v[32:35]\n\t"
        "ds_read_b64_tr_b16 v[98:99], v198 offset:55536\n\t"
        "v_mfma_f32_16x16x32_bf16 v[48:51], v[136:139], v[132:135], v[48:51]\n\t"
        "ds_read_b128 v[84:87], v196 offset:51200\n\t"
        "v_mfma_f32_16x16x32_bf16 v[4:7], v[140:143], v[120:123], v[4:7]\n\t"
        "v_mfma_f32_16x16x32_bf16 v[20:23], v[140:143], v[124:127], v[20:23]\n\t"
        "ds_read_b128 v[88:91], v197 offset:49152\n\t"
        "v_mfma_f32_16x16x32_bf16 v[36:39], v[140:143], v[128:131], v[36:39]\n\t"
        "ds_read_b128 v[92:95], v197 offset:51200\n\t"
        "v_mfma_f32_16x16x32_bf16 v[52:55], v[140:143], v[132:135], v[52:55]\n\t"
        "ds_read_b64_tr_b16 v[100:101], v198 offset:55424\n\t"
        "v_mfma_f32_16x16x32_bf16 v[8:11], v[144:147], v[120:123], v[8:11]\n\t"
        "ds_read_b64_tr_b16 v[102:103], v198 offset:55568\n\t"
        "v_mfma_f32_16x16x32_bf16 v[24:27], v[144:147], v[124:127], v[24:27]\n\t"
        "v_mfma_f32_16x16x32_bf16 v[40:43], v[144:147], v[128:131], v[40:43]\n\t"
        "ds_read_b64_tr_b16 v[104:105], v198 offset:55456\n\t"
        "v_mfma_f32_16x16x32_bf16 v[56:59], v[144:147], v[132:135], v[56:59]\n\t"
        "ds_read_b64_tr_b16 v[106:107], v198 offset:55600\n\t"
        "v_mfma_f32_16x16x32_bf16 v[12:15], v[148:151], v[120:123], v[12:15]\n\t"
        "ds_read_b64_tr_b16 v[108:109], v198 offset:55488\n\t"
        "v_mfma_f32_16x16x32_bf16 v[28:31], v[148:151], v[124:127], v[28:31]\n\t"
        "ds_read_b64_tr_b16 v[110:111], v198 offset:55632\n\t"
        "v_mfma_f32_16x16x32_bf16 v[44:47], v[148:151], v[128:131], v[44:47]\n\t"
        "v_mfma_f32_16x16x32_bf16 v[60:63], v[148:151], v[132:135], v[60:63]\n\t"
        "ds_read_b64_tr_b16 v[112:113], v199 offset:55392\n\t"
        "v_mfma_f32_16x16x32_bf16 v[64:67], v[152:155], v[120:123], v[64:67]\n\t"
        "ds_read_b64_tr_b16 v[114:115], v199 offset:55536\n\t"
        "v_mfma_f32_16x16x32_bf16 v[68:71], v[152:155], v[124:127], v[68:71]\n\t"
        "ds_read_b64_tr_b16 v[116:117], v200 offset:6144\n\t"
        "v_mfma_f32_16x16x32_bf16 v[72:75], v[156:159], v[128:131], v[72:75]\n\t"
        "ds_read_b64_tr_b16 v[118:119], v200 offset:6400\n\t"
        "v_mfma_f32_16x16x32_bf16 v[76:79], v[156:159], v[132:135], v[76:79]\n\t"
        "s_waitcnt lgkmcnt(0)\n\t"
        "v_mfma_f32_16x16x32_bf16 v[0:3], v[96:99], v[80:83], v[0:3]\n\t"
        "ds_read_b128 v[120:123], v218 offset:49152\n\t"
        "v_mfma_f32_16x16x32_bf16 v[16:19], v[96:99], v[84:87], v[16:19]\n\t"
        "ds_read_b64_tr_b16 v[136:137], v198 offset:60000\n\t"
        "v_mfma_f32_16x16x32_bf16 v[32:35], v[96:99], v[88:91], v[32:35]\n\t"
        "ds_read_b64_tr_b16 v[138:139], v198 offset:60144\n\t"
        "v_mfma_f32_16x16x32_bf16 v[48:51], v[96:99], v[92:95], v[48:51]\n\t"
        "ds_read_b128 v[124:127], v218 offset:51200\n\t"
        "v_mfma_f32_16x16x32_bf16 v[4:7], v[100:103], v[80:83], v[4:7]\n\t"
        "ds_read_b128 v[128:131], v219 offset:49152\n\t"
        "v_mfma_f32_16x16x32_bf16 v[20:23], v[100:103], v[84:87], v[20:23]\n\t"
        "ds_read_b128 v[132:135], v219 offset:51200\n\t"
        "v_mfma_f32_16x16x32_bf16 v[36:39], v[100:103], v[88:91], v[36:39]\n\t"
        "v_mfma_f32_16x16x32_bf16 v[52:55], v[100:103], v[92:95], v[52:55]\n\t"
        "ds_read_b64_tr_b16 v[140:141], v198 offset:60032\n\t"
        "v_mfma_f32_16x16x32_bf16 v[8:11], v[104:107], v[80:83], v[8:11]\n\t"
        "ds_read_b64_tr_b16 v[142:143], v198 offset:60176\n\t"
        "v_mfma_f32_16x16x32_bf16 v[24:27], v[104:107], v[84:87], v[24:27]\n\t"
        "ds_read_b64_tr_b16 v[144:145], v198 offset:60064\n\t"
        "v_mfma_f32_16x16x32_bf16 v[40:43], v[104:107], v[88:91], v[40:43]\n\t"
        "ds_read_b64_tr_b16 v[146:147], v198 offset:60208\n\t"
        "v_mfma_f32_16x16x32_bf16 v[56:59], v[104:107], v[92:95], v[56:59]\n\t"
        "ds_read_b64_tr_b16 v[148:149], v198 offset:60096\n\t"
        "v_mfma_f32_16x16x32_bf16 v[12:15], v[108:111], v[80:83], v[12:15]\n\t"
        "ds_read_b64_tr_b16 v[150:151], v198 offset:60240\n\t"
        "v_mfma_f32_16x16x32_bf16 v[28:31], v[108:111], v[84:87], v[28:31]\n\t"
        "v_mfma_f32_16x16x32_bf16 v[44:47], v[108:111], v[88:91], v[44:47]\n\t"
        "ds_read_b64_tr_b16 v[152:153], v199 offset:60000\n\t"
        "v_mfma_f32_16x16x32_bf16 v[60:63], v[108:111], v[92:95], v[60:63]\n\t"
        "ds_read_b64_tr_b16 v[154:155], v199 offset:60144\n\t"
        "v_mfma_f32_16x16x32_bf16 v[64:67], v[112:115], v[80:83], v[64:67]\n\t"
        "ds_read_b64_tr_b16 v[156:157], v200 offset:7168\n\t"
        "v_mfma_f32_16x16x32_bf16 v[68:71], v[112:115], v[84:87], v[68:71]\n\t"
        "ds_read_b64_tr_b16 v[158:159], v200 offset:7424\n\t"
        "v_mfma_f32_16x16x32_bf16 v[72:75], v[116:119], v[88:91], v[72:75]\n\t"
        "s_waitcnt vmcnt(0)\n\t"
        "v_mfma_f32_16x16x32_bf16 v[76:79], v[116:119], v[92:95], v[76:79]\n\t"
        "s_waitcnt lgkmcnt(0)\n\t"
        "s_barrier\n\t"
        "v_mfma_f32_16x16x32_bf16 v[0:3], v[136:139], v[120:123], v[0:3]\n\t"
        "ds_read_b128 v[80:83], v196 offset:0\n\t"
        "v_mfma_f32_16x16x32_bf16 v[16:19], v[136:139], v[124:127], v[16:19]\n\t"
        "ds_read_b64_tr_b16 v[96:97], v198 offset:0\n\t"
        "v_mfma_f32_16x16x32_bf16 v[32:35], v[136:139], v[128:131], v[32:35]\n\t"
        "ds_read_b64_tr_b16 v[98:99], v198 offset:144\n\t"
        "v_mfma_f32_16x16x32_bf16 v[48:51], v[136:139], v[132:135], v[48:51]\n\t"
        "ds_read_b128 v[84:87], v196 offset:2048\n\t"
        "v_mfma_f32_16x16x32_bf16 v[4:7], v[140:143], v[120:123], v[4:7]\n\t"
        "v_mfma_f32_16x16x32_bf16 v[20:23], v[140:143], v[124:127], v[20:23]\n\t"
        "ds_read_b128 v[88:91], v197 offset:0\n\t"
        "v_mfma_f32_16x16x32_bf16 v[36:39], v[140:143], v[128:131], v[36:39]\n\t"
        "ds_read_b128 v[92:95], v197 offset:2048\n\t"
        "v_mfma_f32_16x16x32_bf16 v[52:55], v[140:143], v[132:135], v[52:55]\n\t"
        "ds_read_b64_tr_b16 v[100:101], v198 offset:32\n\t"
        "v_mfma_f32_16x16x32_bf16 v[8:11], v[144:147], v[120:123], v[8:11]\n\t"
        "ds_read_b64_tr_b16 v[102:103], v198 offset:176\n\t"
        "v_mfma_f32_16x16x32_bf16 v[24:27], v[144:147], v[124:127], v[24:27]\n\t"
        "v_mfma_f32_16x16x32_bf16 v[40:43], v[144:147], v[128:131], v[40:43]\n\t"
        "ds_read_b64_tr_b16 v[104:105], v198 offset:64\n\t"
        "v_mfma_f32_16x16x32_bf16 v[56:59], v[144:147], v[132:135], v[56:59]\n\t"
        "ds_read_b64_tr_b16 v[106:107], v198 offset:208\n\t"
        "v_mfma_f32_16x16x32_bf16 v[12:15], v[148:151], v[120:123], v[12:15]\n\t"
        "ds_read_b64_tr_b16 v[108:109], v198 offset:96\n\t"
        "v_mfma_f32_16x16x32_bf16 v[28:31], v[148:151], v[124:127], v[28:31]\n\t"
        "ds_read_b64_tr_b16 v[110:111], v198 offset:240\n\t"
        "v_mfma_f32_16x16x32_bf16 v[44:47], v[148:151], v[128:131], v[44:47]\n\t"
        "v_mfma_f32_16x16x32_bf16 v[60:63], v[148:151], v[132:135], v[60:63]\n\t"
        "ds_read_b64_tr_b16 v[112:113], v199 offset:0\n\t"
        "v_mfma_f32_16x16x32_bf16 v[64:67], v[152:155], v[120:123], v[64:67]\n\t"
        "ds_read_b64_tr_b16 v[114:115], v199 offset:144\n\t"
        "v_mfma_f32_16x16x32_bf16 v[68:71], v[152:155], v[124:127], v[68:71]\n\t"
        "ds_read_b64_tr_b16 v[116:117], v200 offset:8192\n\t"
        "v_mfma_f32_16x16x32_bf16 v[72:75], v[156:159], v[128:131], v[72:75]\n\t"
        "ds_read_b64_tr_b16 v[118:119], v200 offset:8448\n\t"
        "v_mfma_f32_16x16x32_bf16 v[76:79], v[156:159], v[132:135], v[76:79]\n\t"
        "s_waitcnt lgkmcnt(0)\n\t"
        "v_mfma_f32_16x16x32_bf16 v[0:3], v[96:99], v[80:83], v[0:3]\n\t"
        "ds_read_b128 v[120:123], v218 offset:0\n\t"
        "v_mfma_f32_16x16x32_bf16 v[16:19], v[96:99], v[84:87], v[16:19]\n\t"
        "ds_read_b64_tr_b16 v[136:137], v198 offset:4608\n\t"
        "v_mfma_f32_16x16x32_bf16 v[32:35], v[96:99], v[88:91], v[32:35]\n\t"
        "s_and_b32 s53, s52, 0xff\n\t"
        "s_mul_i32 s53, s53, 1152\n\t"
        "s_add_u32 m0, s54, 12288\n\t"
        "v_add_co_u32 v224, vcc, s53, v220\n\t"
        "v_addc_co_u32 v225, vcc, 0, v221, vcc\n\t"
        "global_load_lds_dwordx4 v[224:225], off\n\t"
        "s_lshr_b32 s52, s52, 8\n\t"
        "v_mfma_f32_16x16x32_bf16 v[48:51], v[96:99], v[92:95], v[48:51]\n\t"
        "ds_read_b64_tr_b16 v[138:139], v198 offset:4752\n\t"
        "v_mfma_f32_16x16x32_bf16 v[4:7], v[100:103], v[80:83], v[4:7]\n\t"
        "ds_read_b128 v[124:127], v218 offset:2048\n\t"
        "v_mfma_f32_16x16x32_bf16 v[20:23], v[100:103], v[84:87], v[20:23]\n\t"
        "ds_read_b128 v[128:131], v219 offset:0\n\t"
        "v_mfma_f32_16x16x32_bf16 v[36:39], v[100:103], v[88:91], v[36:39]\n\t"
        "v_mfma_f32_16x16x32_bf16 v[52:55], v[100:103], v[92:95], v[52:55]\n\t"
        "ds_read_b128 v[132:135], v219 offset:2048\n\t"
        "v_mfma_f32_16x16x32_bf16 v[8:11], v[104:107], v[80:83], v[8:11]\n\t"
        "ds_read_b64_tr_b16 v[140:141], v198 offset:4640\n\t"
        "v_mfma_f32_16x16x32_bf16 v[24:27], v[104:107], v[84:87], v[24:27]\n\t"
        "ds_read_b64_tr_b16 v[142:143], v198 offset:4784\n\t"
        "v_mfma_f32_16x16x32_bf16 v[40:43], v[104:107], v[88:91], v[40:43]\n\t"
        "ds_read_b64_tr_b16 v[144:145], v198 offset:4672\n\t"
        "v_mfma_f32_16x16x32_bf16 v[56:59], v[104:107], v[92:95], v[56:59]\n\t"
        "ds_read_b64_tr_b16 v[146:147], v198 offset:4816\n\t"
        "v_mfma_f32_16x16x32_bf16 v[12:15], v[108:111], v[80:83], v[12:15]\n\t"
        "ds_read_b64_tr_b16 v[148:149], v198 offset:4704\n\t"
        "v_mfma_f32_16x16x32_bf16 v[28:31], v[108:111], v[84:87], v[28:31]\n\t"
        "v_mfma_f32_16x16x32_bf16 v[44:47], v[108:111], v[88:91], v[44:47]\n\t"
        "ds_read_b64_tr_b16 v[150:151], v198 offset:4848\n\t"
        "v_mfma_f32_16x16x32_bf16 v[60:63], v[108:111], v[92:95], v[60:63]\n\t"
        "ds_read_b64_tr_b16 v[152:153], v199 offset:4608\n\t"
        "v_mfma_f32_16x16x32_bf16 v[64:67], v[112:115], v[80:83], v[64:67]\n\t"
        "ds_read_b64_tr_b16 v[154:155], v199 offset:4752\n\t"
        "v_mfma_f32_16x16x32_bf16 v[68:71], v[112:115], v[84:87], v[68:71]\n\t"
        "ds_read_b64_tr_b16 v[156:157], v200 offset:9216\n\t"
        "v_mfma_f32_16x16x32_bf16 v[72:75], v[116:119], v[88:91], v[72:75]\n\t"
        "ds_read_b64_tr_b16 v[158:159], v200 offset:9472\n\t"
        "v_mfma_f32_16x16x32_bf16 v[76:79], v[116:119], v[92:95], v[76:79]\n\t"
        "s_waitcnt lgkmcnt(0)\n\t"
        "s_barrier\n\t"
        "v_mfma_f32_16x16x32_bf16 v[0:3], v[136:139], v[120:123], v[0:3]\n\t"
        "ds_read_b128 v[80:83], v196 offset:16384\n\t"
        "v_mfma_f32_16x16x32_bf16 v[16:19], v[136:139], v[124:127], v[16:19]\n\t"
        "ds_read_b64_tr_b16 v[96:97], v198 offset:18464\n\t"
        "v_mfma_f32_16x16x32_bf16 v[32:35], v[136:139], v[128:131], v[32:35]\n\t"
        "ds_read_b64_tr_b16 v[98:99], v198 offset:18608\n\t"
        "v_mfma_f32_16x16x32_bf16 v[48:51], v[136:139], v[132:135], v[48:51]\n\t"
        "ds_read_b128 v[84:87], v196 offset:18432\n\t"
        "v_mfma_f32_16x16x32_bf16 v[4:7], v[140:143], v[120:123], v[4:7]\n\t"
        "v_mfma_f32_16x16x32_bf16 v[20:23], v[140:143], v[124:127], v[20:23]\n\t"
        "ds_read_b128 v[88:91], v197 offset:16384\n\t"
        "v_mfma_f32_16x16x32_bf16 v[36:39], v[140:143], v[128:131], v[36:39]\n\t"
        "ds_read_b128 v[92:95], v197 offset:18432\n\t"
        "v_mfma_f32_16x16x32_bf16 v[52:55], v[140:143], v[132:135], v[52:55]\n\t"
        "ds_read_b64_tr_b16 v[100:101], v198 offset:18496\n\t"
        "v_mfma_f32_16x16x32_bf16 v[8:11], v[144:147], v[120:123], v[8:11]\n\t"
        "ds_read_b64_tr_b16 v[102:103], v198 offset:18640\n\t"
        "v_mfma_f32_16x16x32_bf16 v[24:27], v[144:147], v[124:127], v[24:27]\n\t"
        "v_mfma_f32_16x16x32_bf16 v[40:43], v[144:147], v[128:131], v[40:43]\n\t"
        "ds_read_b64_tr_b16 v[104:105], v198 offset:18528\n\t"
        "v_mfma_f32_16x16x32_bf16 v[56:59], v[144:147], v[132:135], v[56:59]\n\t"
        "ds_read_b64_tr_b16 v[106:107], v198 offset:18672\n\t"
        "v_mfma_f32_16x16x32_bf16 v[12:15], v[148:151], v[120:123], v[12:15]\n\t"
        "ds_read_b64_tr_b16 v[108:109], v198 offset:18560\n\t"
        "v_mfma_f32_16x16x32_bf16 v[28:31], v[148:151], v[124:127], v[28:31]\n\t"
        "ds_read_b64_tr_b16 v[110:111], v198 offset:18704\n\t"
        "v_mfma_f32_16x16x32_bf16 v[44:47], v[148:151], v[128:131], v[44:47]\n\t"
        "v_mfma_f32_16x16x32_bf16 v[60:63], v[148:151], v[132:135], v[60:63]\n\t"
        "ds_read_b64_tr_b16 v[112:113], v199 offset:18464\n\t"
        "v_mfma_f32_16x16x32_bf16 v[64:67], v[152:155], v[120:123], v[64:67]\n\t"
        "ds_read_b64_tr_b16 v[114:115], v199 offset:18608\n\t"
        "v_mfma_f32_16x16x32_bf16 v[68:71], v[152:155], v[124:127], v[68:71]\n\t"
        "ds_read_b64_tr_b16 v[116:117], v200 offset:10240\n\t"
        "v_mfma_f32_16x16x32_bf16 v[72:75], v[156:159], v[128:131], v[72:75]\n\t"
        "ds_read_b64_tr_b16 v[118:119], v200 offset:10496\n\t"
        "v_mfma_f32_16x16x32_bf16 v[76:79], v[156:159], v[132:135], v[76:79]\n\t"
        "s_waitcnt lgkmcnt(0)\n\t"
        "v_mfma_f32_16x16x32_bf16 v[0:3], v[96:99], v[80:83], v[0:3]\n\t"
        "ds_read_b128 v[120:123], v218 offset:16384\n\t"
        "v_mfma_f32_16x16x32_bf16 v[16:19], v[96:99], v[84:87], v[16:19]\n\t"
        "ds_read_b64_tr_b16 v[136:137], v198 offset:23072\n\t"
        "v_mfma_f32_16x16x32_bf16 v[32:35], v[96:99], v[88:91], v[32:35]\n\t"
        "ds_read_b64_tr_b16 v[138:139], v198 offset:23216\n\t"
        "v_mfma_f32_16x16x32_bf16 v[48:51], v[96:99], v[92:95], v[48:51]\n\t"
        "ds_read_b128 v[124:127], v218 offset:18432\n\t"
        "v_mfma_f32_16x16x32_bf16 v[4:7], v[100:103], v[80:83], v[4:7]\n\t"
        "ds_read_b128 v[128:131], v219 offset:16384\n\t"
        "v_mfma_f32_16x16x32_bf16 v[20:23], v[100:103], v[84:87], v[20:23]\n\t"
        "ds_read_b128 v[132:135], v219 offset:18432\n\t"
        "v_mfma_f32_16x16x32_bf16 v[36:39], v[100:103], v[88:91], v[36:39]\n\t"
        "v_mfma_f32_16x16x32_bf16 v[52:55], v[100:103], v[92:95], v[52:55]\n\t"
        "ds_read_b64_tr_b16 v[140:141], v198 offset:23104\n\t"
        "v_mfma_f32_16x16x32_bf16 v[8:11], v[104:107], v[80:83], v[8:11]\n\t"
        "ds_read_b64_tr_b16 v[142:143], v198 offset:23248\n\t"
        "v_mfma_f32_16x16x32_bf16 v[24:27], v[104:107], v[84:87], v[24:27]\n\t"
        "ds_read_b64_tr_b16 v[144:145], v198 offset:23136\n\t"
        "v_mfma_f32_16x16x32_bf16 v[40:43], v[104:107], v[88:91], v[40:43]\n\t"
        "ds_read_b64_tr_b16 v[146:147], v198 offset:23280\n\t"
        "v_mfma_f32_16x16x32_bf16 v[56:59], v[104:107], v[92:95], v[56:59]\n\t"
        "ds_read_b64_tr_b16 v[148:149], v198 offset:23168\n\t"
        "v_mfma_f32_16x16x32_bf16 v[12:15], v[108:111], v[80:83], v[12:15]\n\t"
        "ds_read_b64_tr_b16 v[150:151], v198 offset:23312\n\t"
        "v_mfma_f32_16x16x32_bf16 v[28:31], v[108:111], v[84:87], v[28:31]\n\t"
        "v_mfma_f32_16x16x32_bf16 v[44:47], v[108:111], v[88:91], v[44:47]\n\t"
        "ds_read_b64_tr_b16 v[152:153], v199 offset:23072\n\t"
        "v_mfma_f32_16x16x32_bf16 v[60:63], v[108:111], v[92:95], v[60:63]\n\t"
        "ds_read_b64_tr_b16 v[154:155], v199 offset:23216\n\t"
        "v_mfma_f32_16x16x32_bf16 v[64:67], v[112:115], v[80:83], v[64:67]\n\t"
        "ds_read_b64_tr_b16 v[156:157], v200 offset:11264\n\t"
        "v_mfma_f32_16x16x32_bf16 v[68:71], v[112:115], v[84:87], v[68:71]\n\t"
        "ds_read_b64_tr_b16 v[158:159], v200 offset:11520\n\t"
        "v_mfma_f32_16x16x32_bf16 v[72:75], v[116:119], v[88:91], v[72:75]\n\t"
        "s_waitcnt vmcnt(0)\n\t"
        "v_mfma_f32_16x16x32_bf16 v[76:79], v[116:119], v[92:95], v[76:79]\n\t"
        "s_waitcnt lgkmcnt(0)\n\t"
        "s_barrier\n\t"
        "v_mfma_f32_16x16x32_bf16 v[0:3], v[136:139], v[120:123], v[0:3]\n\t"
        "ds_read_b128 v[80:83], v196 offset:32768\n\t"
        "v_mfma_f32_16x16x32_bf16 v[16:19], v[136:139], v[124:127], v[16:19]\n\t"
        "ds_read_b64_tr_b16 v[96:97], v198 offset:36928\n\t"
        "v_mfma_f32_16x16x32_bf16 v[32:35], v[136:139], v[128:131], v[32:35]\n\t"
        "ds_read_b64_tr_b16 v[98:99], v198 offset:37072\n\t"
        "v_mfma_f32_16x16x32_bf16 v[48:51], v[136:139], v[132:135], v[48:51]\n\t"
        "ds_read_b128 v[84:87], v196 offset:34816\n\t"
        "v_mfma_f32_16x16x32_bf16 v[4:7], v[140:143], v[120:123], v[4:7]\n\t"
        "v_mfma_f32_16x16x32_bf16 v[20:23], v[140:143], v[124:127], v[20:23]\n\t"
        "ds_read_b128 v[88:91], v197 offset:32768\n\t"
        "v_mfma_f32_16x16x32_bf16 v[36:39], v[140:143], v[128:131], v[36:39]\n\t"
        "ds_read_b128 v[92:95], v197 offset:34816\n\t"
        "v_mfma_f32_16x16x32_bf16 v[52:55], v[140:143], v[132:135], v[52:55]\n\t"
        "ds_read_b64_tr_b16 v[100:101], v198 offset:36960\n\t"
        "v_mfma_f32_16x16x32_bf16 v[8:11], v[144:147], v[120:123], v[8:11]\n\t"
        "ds_read_b64_tr_b16 v[102:103], v198 offset:37104\n\t"
        "v_mfma_f32_16x16x32_bf16 v[24:27], v[144:147], v[124:127], v[24:27]\n\t"
        "v_mfma_f32_16x16x32_bf16 v[40:43], v[144:147], v[128:131], v[40:43]\n\t"
        "ds_read_b64_tr_b16 v[104:105], v198 offset:36992\n\t"
        "v_mfma_f32_16x16x32_bf16 v[56:59], v[144:147], v[132:135], v[56:59]\n\t"
        "ds_read_b64_tr_b16 v[106:107], v198 offset:37136\n\t"
        "v_mfma_f32_16x16x32_bf16 v[12:15], v[148:151], v[120:123], v[12:15]\n\t"
        "ds_read_b64_tr_b16 v[108:109], v198 offset:37024\n\t"
        "v_mfma_f32_16x16x32_bf16 v[28:31], v[148:151], v[124:127], v[28:31]\n\t"
        "ds_read_b64_tr_b16 v[110:111], v198 offset:37168\n\t"
        "v_mfma_f32_16x16x32_bf16 v[44:47], v[148:151], v[128:131], v[44:47]\n\t"
        "v_mfma_f32_16x16x32_bf16 v[60:63], v[148:151], v[132:135], v[60:63]\n\t"
        "ds_read_b64_tr_b16 v[112:113], v199 offset:36928\n\t"
        "v_mfma_f32_16x16x32_bf16 v[64:67], v[152:155], v[120:123], v[64:67]\n\t"
        "ds_read_b64_tr_b16 v[114:115], v199 offset:37072\n\t"
        "v_mfma_f32_16x16x32_bf16 v[68:71], v[152:155], v[124:127], v[68:71]\n\t"
        "ds_read_b64_tr_b16 v[116:117], v200 offset:12288\n\t"
        "v_mfma_f32_16x16x32_bf16 v[72:75], v[156:159], v[128:131], v[72:75]\n\t"
        "ds_read_b64_tr_b16 v[118:119], v200 offset:12544\n\t"
        "v_mfma_f32_16x16x32_bf16 v[76:79], v[156:159], v[132:135], v[76:79]\n\t"
        "s_waitcnt lgkmcnt(0)\n\t"
        "v_mfma_f32_16x16x32_bf16 v[0:3], v[96:99], v[80:83], v[0:3]\n\t"
        "ds_read_b128 v[120:123], v218 offset:32768\n\t"
        "v_mfma_f32_16x16x32_bf16 v[16:19], v[96:99], v[84:87], v[16:19]\n\t"
        "ds_read_b64_tr_b16 v[136:137], v198 offset:41536\n\t"
        "v_mfma_f32_16x16x32_bf16 v[32:35], v[96:99], v[88:91], v[32:35]\n\t"
        "s_cmp_ge_u32 %[w], 2\n\t"
        "s_cbranch_scc1 45f\n\t"
        "s_and_b32 s53, s52, 0xff\n\t"
        "s_mul_i32 s53, s53, 1152\n\t"
        "s_add_u32 m0, s54, 16384\n\t"
        "v_add_co_u32 v224, vcc, s53, v220\n\t"
        "v_addc_co_u32 v225, vcc, 0, v221, vcc\n\t"
        "global_load_lds_dwordx4 v[224:225], off\n\t"
        "s_lshr_b32 s52, s52, 8\n\t"
        "45:\n\t"
        "v_mfma_f32_16x16x32_bf16 v[48:51], v[96:99], v[92:95], v[48:51]\n\t"
        "ds_read_b64_tr_b16 v[138:139], v198 offset:41680\n\t"
        "v_mfma_f32_16x16x32_bf16 v[4:7], v[100:103], v[80:83], v[4:7]\n\t"
        "ds_read_b128 v[124:127], v218 offset:34816\n\t"
        "v_mfma_f32_16x16x32_bf16 v[20:23], v[100:103], v[84:87], v[20:23]\n\t"
        "ds_read_b128 v[128:131], v219 offset:32768\n\t"
        "v_mfma_f32_16x16x32_bf16 v[36:39], v[100:103], v[88:91], v[36:39]\n\t"
        "v_mfma_f32_16x16x32_bf16 v[52:55], v[100:103], v[92:95], v[52:55]\n\t"
        "ds_read_b128 v[132:135], v219 offset:34816\n\t"
        "v_mfma_f32_16x16x32_bf16 v[8:11], v[104:107], v[80:83], v[8:11]\n\t"
        "ds_read_b64_tr_b16 v[140:141], v198 offset:41568\n\t"
        "v_mfma_f32_16x16x32_bf16 v[24:27], v[104:107], v[84:87], v[24:27]\n\t"
        "ds_read_b64_tr_b16 v[142:143], v198 offset:41712\n\t"
        "v_mfma_f32_16x16x32_bf16 v[40:43], v[104:107], v[88:91], v[40:43]\n\t"
        "ds_read_b64_tr_b16 v[144:145], v198 offset:41600\n\t"
        "v_mfma_f32_16x16x32_bf16 v[56:59], v[104:107], v[92:95], v[56:59]\n\t"
        "ds_read_b64_tr_b16 v[146:147], v198 offset:41744\n\t"
        "v_mfma_f32_16x16x32_bf16 v[12:15], v[108:111], v[80:83], v[12:15]\n\t"
        "ds_read_b64_tr_b16 v[148:149], v198 offset:41632\n\t"
        "v_mfma_f32_16x16x32_bf16 v[28:31], v[108:111], v[84:87], v[28:31]\n\t"
        "v_mfma_f32_16x16x32_bf16 v[44:47], v[108:111], v[88:91], v[44:47]\n\t"
        "ds_read_b64_tr_b16 v[150:151], v198 offset:41776\n\t"
        "v_mfma_f32_16x16x32_bf16 v[60:63], v[108:111], v[92:95], v[60:63]\n\t"
        "ds_read_b64_tr_b16 v[152:153], v199 offset:41536\n\t"
        "v_mfma_f32_16x16x32_bf16 v[64:67], v[112:115], v[80:83], v[64:67]\n\t"
        "ds_read_b64_tr_b16 v[154:155], v199 offset:41680\n\t"
        "v_mfma_f32_16x16x32_bf16 v[68:71], v[112:115], v[84:87], v[68:71]\n\t"
        "ds_read_b64_tr_b16 v[156:157], v200 offset:13312\n\t"
        "v_mfma_f32_16x16x32_bf16 v[72:75], v[116:119], v[88:91], v[72:75]\n\t"
        "ds_read_b64_tr_b16 v[158:159], v200 offset:13568\n\t"
        "v_mfma_f32_16x16x32_bf16 v[76:79], v[116:119], v[92:95], v[76:79]\n\t"
        "s_waitcnt lgkmcnt(0)\n\t"
        "s_barrier\n\t"
        "v_mfma_f32_16x16x32_bf16 v[0:3], v[136:139], v[120:123], v[0:3]\n\t"
        "ds_read_b128 v[80:83], v196 offset:49152\n\t"
        "v_mfma_f32_16x16x32_bf16 v[16:19], v[136:139], v[124:127], v[16:19]\n\t"
        "ds_read_b64_tr_b16 v[96:97], v198 offset:55392\n\t"
        "v_mfma_f32_16x16x32_bf16 v[32:35], v[136:139], v[128:131], v[32:35]\n\t"
        "ds_read_b64_tr_b16 v[98:99], v198 offset:55536\n\t"
        "v_mfma_f32_16x16x32_bf16 v[48:51], v[136:139], v[132:135], v[48:51]\n\t"
        "ds_read_b128 v[84:87], v196 offset:51200\n\t"
        "v_mfma_f32_16x16x32_bf16 v[4:7], v[140:143], v[120:123], v[4:7]\n\t"
        "v_mfma_f32_16x16x32_bf16 v[20:23], v[140:143], v[124:127], v[20:23]\n\t"
        "ds_read_b128 v[88:91], v197 offset:49152\n\t"
        "v_mfma_f32_16x16x32_bf16 v[36:39], v[140:143], v[128:131], v[36:39]\n\t"
        "ds_read_b128 v[92:95], v197 offset:51200\n\t"
        "v_mfma_f32_16x16x32_bf16 v[52:55], v[140:143], v[132:135], v[52:55]\n\t"
        "ds_read_b64_tr_b16 v[100:101], v198 offset:55424\n\t"
        "v_mfma_f32_16x16x32_bf16 v[8:11], v[144:147], v[120:123], v[8:11]\n\t"
        "ds_read_b64_tr_b16 v[102:103], v198 offset:55568\n\t"
        "v_mfma_f32_16x16x32_bf16 v[24:27], v[144:147], v[124:127], v[24:27]\n\t"
        "v_mfma_f32_16x16x32_bf16 v[40:43], v[144:147], v[128:131], v[40:43]\n\t"
        "ds_read_b64_tr_b16 v[104:105], v198 offset:55456\n\t"
        "v_mfma_f32_16x16x32_bf16 v[56:59], v[144:147], v[132:135], v[56:59]\n\t"
        "ds_read_b64_tr_b16 v[106:107], v198 offset:55600\n\t"
        "v_mfma_f32_16x16x32_bf16 v[12:15], v[148:151], v[120:123], v[12:15]\n\t"
        "ds_read_b64_tr_b16 v[108:109], v198 offset:55488\n\t"
        "v_mfma_f32_16x16x32_bf16 v[28:31], v[148:151], v[124:127], v[28:31]\n\t"
        "ds_read_b64_tr_b16 v[110:111], v198 offset:55632\n\t"
        "v_mfma_f32_16x16x32_bf16 v[44:47], v[148:151], v[128:131], v[44:47]\n\t"
        "v_mfma_f32_16x16x32_bf16 v[60:63], v[148:151], v[132:135], v[60:63]\n\t"
        "ds_read_b64_tr_b16 v[112:113], v199 offset:55392\n\t"
        "v_mfma_f32_16x16x32_bf16 v[64:67], v[152:155], v[120:123], v[64:67]\n\t"
        "ds_read_b64_tr_b16 v[114:115], v199 offset:55536\n\t"
        "v_mfma_f32_16x16x32_bf16 v[68:71], v[152:155], v[124:127], v[68:71]\n\t"
        "ds_read_b64_tr_b16 v[116:117], v200 offset:14336\n\t"
        "v_mfma_f32_16x16x32_bf16 v[72:75], v[156:159], v[128:131], v[72:75]\n\t"
        "ds_read_b64_tr_b16 v[118:119], v200 offset:14592\n\t"
        "v_mfma_f32_16x16x32_bf16 v[76:79], v[156:159], v[132:135], v[76:79]\n\t"
        "s_waitcnt lgkmcnt(0)\n\t"
        "v_mfma_f32_16x16x32_bf16 v[0:3], v[96:99], v[80:83], v[0:3]\n\t"
        "s_waitcnt vmcnt(0)\n\t"
        "ds_read_b128 v[120:123], v218 offset:49152\n\t"
        "v_mfma_f32_16x16x32_bf16 v[16:19], v[96:99], v[84:87], v[16:19]\n\t"
        "ds_read_b64_tr_b16 v[136:137], v198 offset:60000\n\t"
        "v_mfma_f32_16x16x32_bf16 v[32:35], v[96:99], v[88:91], v[32:35]\n\t"
        "ds_read_b64_tr_b16 v[138:139], v198 offset:60144\n\t"
        "v_mfma_f32_16x16x32_bf16 v[48:51], v[96:99], v[92:95], v[48:51]\n\t"
        "ds_read_b128 v[124:127], v218 offset:51200\n\t"
        "v_mfma_f32_16x16x32_bf16 v[4:7], v[100:103], v[80:83], v[4:7]\n\t"
        "ds_read_b128 v[128:131], v219 offset:49152\n\t"
        "ds_read_b128 v[132:135], v219 offset:51200\n\t"
        "v_mfma_f32_16x16x32_bf16 v[20:23], v[100:103], v[84:87], v[20:23]\n\t"
        "ds_read_b64_tr_b16 v[140:141], v198 offset:60032\n\t"
        "v_mfma_f32_16x16x32_bf16 v[36:39], v[100:103], v[88:91], v[36:39]\n\t"
        "ds_read_b64_tr_b16 v[142:143], v198 offset:60176\n\t"
        "v_mfma_f32_16x16x32_bf16 v[52:55], v[100:103], v[92:95], v[52:55]\n\t"
        "ds_read_b64_tr_b16 v[144:145], v198 offset:60064\n\t"
        "v_mfma_f32_16x16x32_bf16 v[8:11], v[104:107], v[80:83], v[8:11]\n\t"
        "ds_read_b64_tr_b16 v[146:147], v198 offset:60208\n\t"
        "ds_read_b64_tr_b16 v[148:149], v198 offset:60096\n\t"
        "v_mfma_f32_16x16x32_bf16 v[24:27], v[104:107], v[84:87], v[24:27]\n\t"
        "ds_read_b64_tr_b16 v[150:151], v198 offset:60240\n\t"
        "v_mfma_f32_16x16x32_bf16 v[40:43], v[104:107], v[88:91], v[40:43]\n\t"
        "ds_read_b64_tr_b16 v[152:153], v199 offset:60000\n\t"
        "v_mfma_f32_16x16x32_bf16 v[56:59], v[104:107], v[92:95], v[56:59]\n\t"
        "ds_read_b64_tr_b16 v[154:155], v199 offset:60144\n\t"
        "v_mfma_f32_16x16x32_bf16 v[12:15], v[108:111], v[80:83], v[12:15]\n\t"
        "ds_read_b64_tr_b16 v[156:157], v200 offset:15360\n\t"
        "ds_read_b64_tr_b16 v[158:159], v200 offset:15616\n\t"
        "v_mfma_f32_16x16x32_bf16 v[28:31], v[108:111], v[84:87], v[28:31]\n\t"
        "global_load_ushort v160, v204, %[wob]\n\t"
        "global_load_ushort v161, v204, %[wob] offset:1152\n\t"
        "v_mfma_f32_16x16x32_bf16 v[44:47], v[108:111], v[88:91], v[44:47]\n\t"
        "global_load_ushort v162, v205, %[wob]\n\t"
        "global_load_ushort v163, v205, %[wob] offset:1152\n\t"
        "v_mfma_f32_16x16x32_bf16 v[60:63], v[108:111], v[92:95], v[60:63]\n\t"
        "global_load_ushort v164, v206, %[wob]\n\t"
        "global_load_ushort v165, v206, %[wob] offset:1152\n\t"
        "v_mfma_f32_16x16x32_bf16 v[64:67], v[112:115], v[80:83], v[64:67]\n\t"
        "global_load_ushort v166, v207, %[wob]\n\t"
        "global_load_ushort v167, v207, %[wob] offset:1152\n\t"
        "s_mov_b64 exec, %[wom]\n\t"
        "global_load_ushort v168, v208, %[wob]\n\t"
        "global_load_ushort v169, v208, %[wob] offset:1152\n\t"
        "s_mov_b64 exec, -1\n\t"
        "v_mfma_f32_16x16x32_bf16 v[68:71], v[112:115], v[84:87], v[68:71]\n\t"
        "s_mov_b64 exec, %[wom]\n\t"
        "global_load_ushort v170, v209, %[wob]\n\t"
        "global_load_ushort v171, v209, %[wob] offset:1152\n\t"
        "s_mov_b64 exec, -1\n\t"
        "v_mfma_f32_16x16x32_bf16 v[72:75], v[116:119], v[88:91], v[72:75]\n\t"
        "s_mov_b64 exec, %[wom]\n\t"
        "global_load_ushort v172, v210, %[wob]\n\t"
        "global_load_ushort v173, v210, %[wob] offset:1152\n\t"
        "s_mov_b64 exec, -1\n\t"
        "v_mfma_f32_16x16x32_bf16 v[76:79], v[116:119], v[92:95], v[76:79]\n\t"
        "s_mov_b64 exec, %[wom]\n\t"
        "global_load_ushort v174, v211, %[wob]\n\t"
        "global_load_ushort v175, v211, %[wob] offset:1152\n\t"
        "s_mov_b64 exec, -1\n\t"
        "s_waitcnt lgkmcnt(0)\n\t"
        "s_barrier\n\t"
        "v_mfma_f32_16x16x32_bf16 v[0:3], v[136:139], v[120:123], v[0:3]\n\t"
        "ds_read_b128 v[80:83], v196 offset:0\n\t"
        "v_mfma_f32_16x16x32_bf16 v[16:19], v[136:139], v[124:127], v[16:19]\n\t"
        "ds_read_b64_tr_b16 v[96:97], v198 offset:0\n\t"
        "v_mfma_f32_16x16x32_bf16 v[32:35], v[136:139], v[128:131], v[32:35]\n\t"
        "ds_read_b64_tr_b16 v[98:99], v198 offset:144\n\t"
        "v_mfma_f32_16x16x32_bf16 v[48:51], v[136:139], v[132:135], v[48:51]\n\t"
        "ds_read_b128 v[84:87], v196 offset:2048\n\t"
        "v_mfma_f32_16x16x32_bf16 v[4:7], v[140:143], v[120:123], v[4:7]\n\t"
        "v_mfma_f32_16x16x32_bf16 v[20:23], v[140:143], v[124:127], v[20:23]\n\t"
        "ds_read_b128 v[88:91], v197 offset:0\n\t"
        "v_mfma_f32_16x16x32_bf16 v[36:39], v[140:143], v[128:131], v[36:39]\n\t"
        "ds_read_b128 v[92:95], v197 offset:2048\n\t"
        "v_mfma_f32_16x16x32_bf16 v[52:55], v[140:143], v[132:135], v[52:55]\n\t"
        "ds_read_b64_tr_b16 v[100:101], v198 offset:32\n\t"
        "v_mfma_f32_16x16x32_bf16 v[8:11], v[144:147], v[120:123], v[8:11]\n\t"
        "ds_read_b64_tr_b16 v[102:103], v198 offset:176\n\t"
        "v_mfma_f32_16x16x32_bf16 v[24:27], v[144:147], v[124:127], v[24:27]\n\t"
        "v_mfma_f32_16x16x32_bf16 v[40:43], v[144:147], v[128:131], v[40:43]\n\t"
        "ds_read_b64_tr_b16 v[104:105], v198 offset:64\n\t"
        "v_mfma_f32_16x16x32_bf16 v[56:59], v[144:147], v[132:135], v[56:59]\n\t"
        "ds_read_b64_tr_b16 v[106:107], v198 offset:208\n\t"
        "v_mfma_f32_16x16x32_bf16 v[12:15], v[148:151], v[120:123], v[12:15]\n\t"
        "ds_read_b64_tr_b16 v[108:109], v198 offset:96\n\t"
        "v_mfma_f32_16x16x32_bf16 v[28:31], v[148:151], v[124:127], v[28:31]\n\t"
        "ds_read_b64_tr_b16 v[110:111], v198 offset:240\n\t"
        "v_mfma_f32_16x16x32_bf16 v[44:47], v[148:151], v[128:131], v[44:47]\n\t"
        "v_mfma_f32_16x16x32_bf16 v[60:63], v[148:151], v[132:135], v[60:63]\n\t"
        "ds_read_b64_tr_b16 v[112:113], v199 offset:0\n\t"
        "v_mfma_f32_16x16x32_bf16 v[64:67], v[152:155], v[120:123], v[64:67]\n\t"
        "ds_read_b64_tr_b16 v[114:115], v199 offset:144\n\t"
        "v_mfma_f32_16x16x32_bf16 v[68:71], v[152:155], v[124:127], v[68:71]\n\t"
        "ds_read_b64_tr_b16 v[116:117], v200 offset:16384\n\t"
        "v_mfma_f32_16x16x32_bf16 v[72:75], v[156:159], v[128:131], v[72:75]\n\t"
        "ds_read_b64_tr_b16 v[118:119], v200 offset:16640\n\t"
        "v_mfma_f32_16x16x32_bf16 v[76:79], v[156:159], v[132:135], v[76:79]\n\t"
        "s_waitcnt lgkmcnt(0)\n\t"
        "v_mfma_f32_16x16x32_bf16 v[0:3], v[96:99], v[80:83], v[0:3]\n\t"
        "ds_read_b128 v[120:123], v218 offset:0\n\t"
        "v_mfma_f32_16x16x32_bf16 v[16:19], v[96:99], v[84:87], v[16:19]\n\t"
        "ds_read_b64_tr_b16 v[136:137], v198 offset:4608\n\t"
        "v_mfma_f32_16x16x32_bf16 v[32:35], v[96:99], v[88:91], v[32:35]\n\t"
        "ds_read_b64_tr_b16 v[138:139], v198 offset:4752\n\t"
        "v_mfma_f32_16x16x32_bf16 v[48:51], v[96:99], v[92:95], v[48:51]\n\t"
        "ds_read_b128 v[124:127], v218 offset:2048\n\t"
        "v_mfma_f32_16x16x32_bf16 v[4:7], v[100:103], v[80:83], v[4:7]\n\t"
        "ds_read_b128 v[128:131], v219 offset:0\n\t"
        "v_mfma_f32_16x16x32_bf16 v[20:23], v[100:103], v[84:87], v[20:23]\n\t"
        "ds_read_b128 v[132:135], v219 offset:2048\n\t"
        "v_mfma_f32_16x16x32_bf16 v[36:39], v[100:103], v[88:91], v[36:39]\n\t"
        "v_mfma_f32_16x16x32_bf16 v[52:55], v[100:103], v[92:95], v[52:55]\n\t"
        "ds_read_b64_tr_b16 v[140:141], v198 offset:4640\n\t"
        "v_mfma_f32_16x16x32_bf16 v[8:11], v[104:107], v[80:83], v[8:11]\n\t"
        "ds_read_b64_tr_b16 v[142:143], v198 offset:4784\n\t"
        "v_mfma_f32_16x16x32_bf16 v[24:27], v[104:107], v[84:87], v[24:27]\n\t"
        "ds_read_b64_tr_b16 v[144:145], v198 offset:4672\n\t"
        "v_mfma_f32_16x16x32_bf16 v[40:43], v[104:107], v[88:91], v[40:43]\n\t"
        "ds_read_b64_tr_b16 v[146:147], v198 offset:4816\n\t"
        "v_mfma_f32_16x16x32_bf16 v[56:59], v[104:107], v[92:95], v[56:59]\n\t"
        "ds_read_b64_tr_b16 v[148:149], v198 offset:4704\n\t"
        "v_mfma_f32_16x16x32_bf16 v[12:15], v[108:111], v[80:83], v[12:15]\n\t"
        "ds_read_b64_tr_b16 v[150:151], v198 offset:4848\n\t"
        "v_mfma_f32_16x16x32_bf16 v[28:31], v[108:111], v[84:87], v[28:31]\n\t"
        "v_mfma_f32_16x16x32_bf16 v[44:47], v[108:111], v[88:91], v[44:47]\n\t"
        "ds_read_b64_tr_b16 v[152:153], v199 offset:4608\n\t"
        "v_mfma_f32_16x16x32_bf16 v[60:63], v[108:111], v[92:95], v[60:63]\n\t"
        "ds_read_b64_tr_b16 v[154:155], v199 offset:4752\n\t"
        "v_mfma_f32_16x16x32_bf16 v[64:67], v[112:115], v[80:83], v[64:67]\n\t"
        "ds_read_b64_tr_b16 v[156:157], v200 offset:17408\n\t"
        "v_mfma_f32_16x16x32_bf16 v[68:71], v[112:115], v[84:87], v[68:71]\n\t"
        "ds_read_b64_tr_b16 v[158:159], v200 offset:17664\n\t"
        "v_mfma_f32_16x16x32_bf16 v[72:75], v[116:119], v[88:91], v[72:75]\n\t"
        "s_mov_b32 exec_lo, 0x0011ffff\n\t"
        "s_mov_b32 exec_hi, 0x0011ffff\n\t"
        "ds_read_u16 v176, v245 offset:0\n\t"
        "ds_read_u16 v177, v245 offset:36\n\t"
        "ds_read_u16 v178, v245 offset:72\n\t"
        "ds_read_u16 v179, v245 offset:108\n\t"
        "ds_read_u16 v180, v245 offset:144\n\t"
        "ds_read_u16 v181, v245 offset:180\n\t"
        "ds_read_u16 v182, v245 offset:216\n\t"
        "ds_read_u16 v183, v245 offset:252\n\t"
        "s_mov_b32 exec_hi, 0\n\t"
        "ds_read_u16 v192, v245 offset:576\n\t"
        "ds_read_u16 v193, v245 offset:612\n\t"
        "s_mov_b64 exec, -1\n\t"
        "v_mfma_f32_16x16x32_bf16 v[76:79], v[116:119], v[92:95], v[76:79]\n\t"
        "s_waitcnt lgkmcnt(0)\n\t"
        "v_mfma_f32_16x16x32_bf16 v[72:75], v[156:159], v[128:131], v[72:75]\n\t"
        "v_lshl_or_b32 v184, v177, 16, v176\n\t"
        "v_lshl_or_b32 v185, v179, 16, v178\n\t"
        "v_mfma_f32_16x16x32_bf16 v[76:79], v[156:159], v[132:135], v[76:79]\n\t"
        "v_lshl_or_b32 v186, v181, 16, v180\n\t"
        "v_lshl_or_b32 v187, v183, 16, v182\n\t"
        "v_mfma_f32_16x16x32_bf16 v[64:67], v[152:155], v[120:123], v[64:67]\n\t"
        "v_lshl_or_b32 v188, v193, 16, v192\n\t"
        "s_nop 0\n\t"
        "v_mfma_f32_16x16x32_bf16 v[68:71], v[152:155], v[124:127], v[68:71]\n\t"
        "s_nop 1\n\t"
        "v_mfma_f32_16x16x32_bf16 v[0:3], v[136:139], v[120:123], v[0:3]\n\t"
        "s_nop 1\n\t"
        "v_mfma_f32_16x16x32_bf16 v[16:19], v[136:139], v[124:127], v[16:19]\n\t"
        "v_pk_mul_f32 v[64:65], v[64:65], v[214:215] op_sel_hi:[1,0]\n\t"
        "v_pk_mul_f32 v[66:67], v[66:67], v[214:215] op_sel_hi:[1,0]\n\t"
        "v_cvt_pk_bf16_f32 v80, v64, v65\n\t"
        "v_cvt_pk_bf16_f32 v81, v66, v67\n\t"
        "ds_write_b64 v215, v[80:81] offset:0\n\t"
        "v_mfma_f32_16x16x32_bf16 v[32:35], v[136:139], v[128:131], v[32:35]\n\t"
        "v_pk_mul_f32 v[68:69], v[68:69], v[214:215] op_sel_hi:[1,0]\n\t"
        "v_pk_mul_f32 v[70:71], v[70:71], v[214:215] op_sel_hi:[1,0]\n\t"
        "v_cvt_pk_bf16_f32 v82, v68, v69\n\t"
        "v_cvt_pk_bf16_f32 v83, v70, v71\n\t"
        "ds_write_b64 v215, v[82:83] offset:4736\n\t"
        "v_mfma_f32_16x16x32_bf16 v[48:51], v[136:139], v[132:135], v[48:51]\n\t"
        "v_pk_mul_f32 v[0:1], v[0:1], s[44:45]\n\t"
        "v_pk_mul_f32 v[2:3], v[2:3], s[44:45]\n\t"
        "v_cvt_pk_bf16_f32 v84, v0, v1\n\t"
        "v_cvt_pk_bf16_f32 v85, v2, v3\n\t"
        "ds_write_b64 v212, v[84:85] offset:0\n\t"
        "v_mfma_f32_16x16x32_bf16 v[4:7], v[140:143], v[120:123], v[4:7]\n\t"
        "v_pk_mul_f32 v[16:17], v[16:17], s[44:45]\n\t"
        "v_pk_mul_f32 v[18:19], v[18:19], s[44:45]\n\t"
        "v_cvt_pk_bf16_f32 v86, v16, v17\n\t"
        "v_cvt_pk_bf16_f32 v87, v18, v19\n\t"
        "ds_write_b64 v212, v[86:87] offset:4736\n\t"
        "v_mfma_f32_16x16x32_bf16 v[20:23], v[140:143], v[124:127], v[20:23]\n\t"
        "v_pk_mul_f32 v[32:33], v[32:33], s[44:45]\n\t"
        "v_pk_mul_f32 v[34:35], v[34:35], s[44:45]\n\t"
        "v_cvt_pk_bf16_f32 v88, v32, v33\n\t"
        "v_cvt_pk_bf16_f32 v89, v34, v35\n\t"
        "ds_write_b64 v213, v[88:89] offset:9472\n\t"
        "v_mfma_f32_16x16x32_bf16 v[36:39], v[140:143], v[128:131], v[36:39]\n\t"
        "v_pk_mul_f32 v[48:49], v[48:49], s[44:45]\n\t"
        "v_pk_mul_f32 v[50:51], v[50:51], s[44:45]\n\t"
        "v_cvt_pk_bf16_f32 v90, v48, v49\n\t"
        "v_cvt_pk_bf16_f32 v91, v50, v51\n\t"
        "ds_write_b64 v213, v[90:91] offset:14208\n\t"
        "v_mfma_f32_16x16x32_bf16 v[52:55], v[140:143], v[132:135], v[52:55]\n\t"
        "v_pk_mul_f32 v[4:5], v[4:5], s[44:45]\n\t"
        "v_pk_mul_f32 v[6:7], v[6:7], s[44:45]\n\t"
        "v_cvt_pk_bf16_f32 v92, v4, v5\n\t"
        "v_cvt_pk_bf16_f32 v93, v6, v7\n\t"
        "ds_write_b64 v212, v[92:93] offset:32\n\t"
        "v_mfma_f32_16x16x32_bf16 v[8:11], v[144:147], v[120:123], v[8:11]\n\t"
        "v_pk_mul_f32 v[20:21], v[20:21], s[44:45]\n\t"
        "v_pk_mul_f32 v[22:23], v[22:23], s[44:45]\n\t"
        "v_cvt_pk_bf16_f32 v94, v20, v21\n\t"
        "v_cvt_pk_bf16_f32 v95, v22, v23\n\t"
        "ds_write_b64 v212, v[94:95] offset:4768\n\t"
        "v_mfma_f32_16x16x32_bf16 v[24:27], v[144:147], v[124:127], v[24:27]\n\t"
        "v_pk_mul_f32 v[36:37], v[36:37], s[44:45]\n\t"
        "v_pk_mul_f32 v[38:39], v[38:39], s[44:45]\n\t"
        "v_cvt_pk_bf16_f32 v80, v36, v37\n\t"
        "v_cvt_pk_bf16_f32 v81, v38, v39\n\t"
        "ds_write_b64 v213, v[80:81] offset:9504\n\t"
        "v_mfma_f32_16x16x32_bf16 v[40:43], v[144:147], v[128:131], v[40:43]\n\t"
        "v_pk_mul_f32 v[52:53], v[52:53], s[44:45]\n\t"
        "v_pk_mul_f32 v[54:55], v[54:55], s[44:45]\n\t"
        "v_cvt_pk_bf16_f32 v82, v52, v53\n\t"
        "v_cvt_pk_bf16_f32 v83, v54, v55\n\t"
        "ds_write_b64 v213, v[82:83] offset:14240\n\t"
        "v_mfma_f32_16x16x32_bf16 v[56:59], v[144:147], v[132:135], v[56:59]\n\t"
        "v_pk_mul_f32 v[8:9], v[8:9], s[44:45]\n\t"
        "v_pk_mul_f32 v[10:11], v[10:11], s[44:45]\n\t"
        "v_cvt_pk_bf16_f32 v84, v8, v9\n\t"
        "v_cvt_pk_bf16_f32 v85, v10, v11\n\t"
        "ds_write_b64 v212, v[84:85] offset:64\n\t"
        "v_mfma_f32_16x16x32_bf16 v[12:15], v[148:151], v[120:123], v[12:15]\n\t"
        "v_pk_mul_f32 v[24:25], v[24:25], s[44:45]\n\t"
        "v_pk_mul_f32 v[26:27], v[26:27], s[44:45]\n\t"
        "v_cvt_pk_bf16_f32 v86, v24, v25\n\t"
        "v_cvt_pk_bf16_f32 v87, v26, v27\n\t"
        "ds_write_b64 v212, v[86:87] offset:4800\n\t"
        "v_mfma_f32_16x16x32_bf16 v[28:31], v[148:151], v[124:127], v[28:31]\n\t"
        "v_pk_mul_f32 v[40:41], v[40:41], s[44:45]\n\t"
        "v_pk_mul_f32 v[42:43], v[42:43], s[44:45]\n\t"
        "v_cvt_pk_bf16_f32 v88, v40, v41\n\t"
        "v_cvt_pk_bf16_f32 v89, v42, v43\n\t"
        "ds_write_b64 v213, v[88:89] offset:9536\n\t"
        "v_mfma_f32_16x16x32_bf16 v[44:47], v[148:151], v[128:131], v[44:47]\n\t"
        "v_pk_mul_f32 v[56:57], v[56:57], s[44:45]\n\t"
        "v_pk_mul_f32 v[58:59], v[58:59], s[44:45]\n\t"
        "v_cvt_pk_bf16_f32 v90, v56, v57\n\t"
        "v_cvt_pk_bf16_f32 v91, v58, v59\n\t"
        "ds_write_b64 v213, v[90:91] offset:14272\n\t"
        "v_mfma_f32_16x16x32_bf16 v[60:63], v[148:151], v[132:135], v[60:63]\n\t"
        "v_pk_mul_f32 v[12:13], v[12:13], s[44:45]\n\t"
        "v_pk_mul_f32 v[14:15], v[14:15], s[44:45]\n\t"
        "v_cvt_pk_bf16_f32 v92, v12, v13\n\t"
        "v_cvt_pk_bf16_f32 v93, v14, v15\n\t"
        "ds_write_b64 v212, v[92:93] offset:96\n\t"
        "v_pk_mul_f32 v[28:29], v[28:29], s[44:45]\n\t"
        "v_pk_mul_f32 v[30:31], v[30:31], s[44:45]\n\t"
        "v_cvt_pk_bf16_f32 v94, v28, v29\n\t"
        "v_cvt_pk_bf16_f32 v95, v30, v31\n\t"
        "ds_write_b64 v212, v[94:95] offset:4832\n\t"
        "v_pk_mul_f32 v[44:45], v[44:45], s[44:45]\n\t"
        "v_pk_mul_f32 v[46:47], v[46:47], s[44:45]\n\t"
        "v_cvt_pk_bf16_f32 v80, v44, v45\n\t"
        "v_cvt_pk_bf16_f32 v81, v46, v47\n\t"
        "ds_write_b64 v213, v[80:81] offset:9568\n\t"
        "v_pk_mul_f32 v[60:61], v[60:61], s[44:45]\n\t"
        "v_pk_mul_f32 v[62:63], v[62:63], s[44:45]\n\t"
        "v_cvt_pk_bf16_f32 v82, v60, v61\n\t"
        "v_cvt_pk_bf16_f32 v83, v62, v63\n\t"
        "ds_write_b64 v213, v[82:83] offset:14304\n\t"
        "v_cvt_pk_bf16_f32 v100, v72, v73\n\t"
        "v_cvt_pk_bf16_f32 v101, v74, v75\n\t"
        "v_cvt_pk_bf16_f32 v102, v76, v77\n\t"
        "v_cvt_pk_bf16_f32 v103, v78, v79\n\t"
        "global_store_dwordx2 v201, v[100:101], %[wsb] offset:0 sc1\n\t"
        "global_store_dwordx2 v201, v[102:103], %[wsb] offset:256 sc1\n\t"
        "s_branch 69f\n\t"
        "62:\n\t"
        "v_xor_b32 v218, 64, v196\n\t"
        "v_xor_b32 v219, 64, v197\n\t"
        "s_mov_b32 s44, %[sc0]\n\t"
        "s_mov_b32 s45, %[sc0]\n\t"
        "s_and_b32 s46, %[w], 3\n\t"
        "s_lshr_b32 s47, %[w], 2\n\t"
        "v_and_b32 v240, 31, v244\n\t"
        "v_lshrrev_b32 v241, 5, v244\n\t"
        "s_mul_i32 s48, s47, 18944\n\t"
        "s_mul_i32 s49, s46, 36\n\t"
        "s_add_u32 s48, s48, s49\n\t"
        "s_add_u32 s48, s48, 16384\n\t"
        "v_mul_u32_u24 v242, 296, v240\n\t"
        "v_lshlrev_b32 v243, 4, v241\n\t"
        "v_add3_u32 v228, v242, v243, s48\n\t"
        "v_add_u32 v230, s48, v242\n\t"
        "v_add_u32 v229, 9472, v228\n\t"
        "s_mul_i32 s48, s47, 22528\n\t"
        "s_mul_i32 s49, s46, 80\n\t"
        "s_add_u32 s48, s48, s49\n\t"
        "s_add_u32 s48, s48, 65536\n\t"
        "v_mul_u32_u24 v242, 352, v240\n\t"
        "v_add3_u32 v231, v242, v243, s48\n\t"
        "v_mul_u32_u24 v243, 12, v241\n\t"
        "v_sub_u32 v232, v231, v243\n\t"
        "v_add_u32 v232, 64, v232\n\t"
        "v_lshlrev_b32 v233, 4, v244\n\t"
        "v_lshlrev_b32 v234, 2, v244\n\t"
        "v_bfe_u32 v240, v244, 2, 2\n\t"
        "v_and_b32 v241, 3, v244\n\t"
        "v_lshrrev_b32 v242, 4, v244\n\t"
        "v_lshl_add_u32 v240, v242, 2, v240\n\t"
        "v_lshlrev_b32 v241, 3, v241\n\t"
        "v_mul_u32_u24 v243, 352, v240\n\t"
        "v_add3_u32 v235, v243, v241, s48\n\t"
        "s_mul_i32 s48, s47, 18944\n\t"
        "s_mul_i32 s49, s46, 36\n\t"
        "s_add_u32 s48, s48, s49\n\t"
        "s_and_b32 s49, s46, 1\n\t"
        "s_lshl_b32 s49, s49, 2\n\t"
        "s_sub_u32 s48, s48, s49\n\t"
        "s_add_u32 s48, s48, 16528\n\t"
        "v_mul_u32_u24 v243, 296, v240\n\t"
        "v_add3_u32 v236, v243, v241, s48\n\t"
        "s_lshr_b32 s49, s49, 1\n\t"
        "s_sub_u32 s49, 2, s49\n\t"
        "v_and_b32 v243, 15, v244\n\t"
        "v_add_u32 v243, s49, v243\n\t"
        "v_lshlrev_b32 v241, 6, v243\n\t"
        "v_lshl_add_u32 v237, v242, 4, v241\n\t"
        "v_lshlrev_b32 v238, 4, v243\n\t"
        "v_bfe_u32 v240, v244, 3, 2\n\t"
        "v_and_b32 v241, 3, v244\n\t"
        "v_lshl_add_u32 v240, v240, 2, v241\n\t"
        "v_bfe_u32 v241, v244, 2, 1\n\t"
        "v_mul_u32_u24 v241, 9, v241\n\t"
        "v_add_u32 v241, v241, v240\n\t"
        "v_lshrrev_b32 v242, 5, v244\n\t"
        "v_mul_u32_u24 v242, 288, v242\n\t"
        "v_lshl_add_u32 v241, v241, 1, v242\n\t"
        "s_mul_i32 s48, s46, 648\n\t"
        "s_add_u32 s48, s48, 157824\n\t"
        "v_add_u32 v245, s48, v241\n\t"
        "v_mov_b32 v246, 0\n\t"
        "v_mov_b32 v247, 0\n\t"
        "v_mov_b32 v248, 0\n\t"
        "v_mov_b32 v249, 0\n\t"
        "v_mov_b32 v250, 0\n\t"
        "v_mov_b32 v251, 0\n\t"
        "v_mov_b32 v252, 0\n\t"
        "v_mov_b32 v253, 0\n\t"
        "v_mov_b32 v176, 0\n\t"
        "v_mov_b32 v177, 0\n\t"
        "v_mov_b32 v178, 0\n\t"
        "v_mov_b32 v179, 0\n\t"
        "v_mov_b32 v180, 0\n\t"
        "v_mov_b32 v181, 0\n\t"
        "v_mov_b32 v182, 0\n\t"
        "v_mov_b32 v183, 0\n\t"
        "v_mov_b32 v192, 0\n\t"
        "v_mov_b32 v193, 0\n\t"
        "v_mov_b32 v189, 0\n\t"
        "v_mov_b32 v190, 0\n\t"
        "v_mov_b32 v191, 0\n\t"
        "s_mov_b32 s52, %[blk]\n\t"
        "s_lshl_b32 s54, %[w], 10\n\t"
        "s_add_u32 s54, s54, 139392\n\t"
        "s_add_u32 s55, s54, 8192\n\t"
        "s_mov_b32 s50, 0x55555555\n\t"
        "s_mov_b32 s51, s50\n\t"
        "s_waitcnt vmcnt(0)\n\t"
        "s_barrier\n\t"
        "ds_read_b128 v[80:83], v196 offset:0\n\t"
        "ds_read_b64_tr_b16 v[96:97], v198 offset:0\n\t"
        "ds_read_b64_tr_b16 v[98:99], v198 offset:144\n\t"
        "ds_read_b128 v[84:87], v196 offset:2048\n\t"
        "ds_read_b128 v[88:91], v197 offset:0\n\t"
        "ds_read_b128 v[92:95], v197 offset:2048\n\t"
        "ds_read_b64_tr_b16 v[100:101], v198 offset:32\n\t"
        "ds_read_b64_tr_b16 v[102:103], v198 offset:176\n\t"
        "ds_read_b64_tr_b16 v[104:105], v198 offset:64\n\t"
        "ds_read_b64_tr_b16 v[106:107], v198 offset:208\n\t"
        "ds_read_b64_tr_b16 v[108:109], v198 offset:96\n\t"
        "ds_read_b64_tr_b16 v[110:111], v198 offset:240\n\t"
        "ds_read_b64_tr_b16 v[112:113], v199 offset:0\n\t"
        "ds_read_b64_tr_b16 v[114:115], v199 offset:144\n\t"
        "ds_read_b64_tr_b16 v[116:117], v200 offset:0\n\t"
        "ds_read_b64_tr_b16 v[118:119], v200 offset:256\n\t"
        "s_waitcnt lgkmcnt(0)\n\t"
        "v_mfma_f32_16x16x32_bf16 v[0:3], v[96:99], v[80:83], 0\n\t"
        "ds_read_b128 v[120:123], v218 offset:0\n\t"
        "v_mfma_f32_16x16x32_bf16 v[16:19], v[96:99], v[84:87], 0\n\t"
        "ds_read_b64_tr_b16 v[136:137], v198 offset:4608\n\t"
        "v_mfma_f32_16x16x32_bf16 v[32:35], v[96:99], v[88:91], 0\n\t"
        "s_and_b32 s53, s52, 0xff\n\t"
        "s_mul_i32 s53, s53, 1152\n\t"
        "s_add_u32 m0, s54, 4096\n\t"
        "v_add_co_u32 v224, vcc, s53, v220\n\t"
        "v_addc_co_u32 v225, vcc, 0, v221, vcc\n\t"
        "global_load_lds_dwordx4 v[224:225], off\n\t"
        "s_lshr_b32 s52, s52, 8\n\t"
        "v_mfma_f32_16x16x32_bf16 v[48:51], v[96:99], v[92:95], 0\n\t"
        "ds_read_b64_tr_b16 v[138:139], v198 offset:4752\n\t"
        "v_mfma_f32_16x16x32_bf16 v[4:7], v[100:103], v[80:83], 0\n\t"
        "ds_read_b128 v[124:127], v218 offset:2048\n\t"
        "v_mfma_f32_16x16x32_bf16 v[20:23], v[100:103], v[84:87], 0\n\t"
        "ds_read_b128 v[128:131], v219 offset:0\n\t"
        "v_mfma_f32_16x16x32_bf16 v[36:39], v[100:103], v[88:91], 0\n\t"
        "v_mfma_f32_16x16x32_bf16 v[52:55], v[100:103], v[92:95], 0\n\t"
        "ds_read_b128 v[132:135], v219 offset:2048\n\t"
        "v_mfma_f32_16x16x32_bf16 v[8:11], v[104:107], v[80:83], 0\n\t"
        "ds_read_b64_tr_b16 v[140:141], v198 offset:4640\n\t"
        "v_mfma_f32_16x16x32_bf16 v[24:27], v[104:107], v[84:87], 0\n\t"
        "ds_read_b64_tr_b16 v[142:143], v198 offset:4784\n\t"
        "v_mfma_f32_16x16x32_bf16 v[40:43], v[104:107], v[88:91], 0\n\t"
        "ds_read_b64_tr_b16 v[144:145], v198 offset:4672\n\t"
        "v_mfma_f32_16x16x32_bf16 v[56:59], v[104:107], v[92:95], 0\n\t"
        "ds_read_b64_tr_b16 v[146:147], v198 offset:4816\n\t"
        "v_mfma_f32_16x16x32_bf16 v[12:15], v[108:111], v[80:83], 0\n\t"
        "ds_read_b64_tr_b16 v[148:149], v198 offset:4704\n\t"
        "v_mfma_f32_16x16x32_bf16 v[28:31], v[108:111], v[84:87], 0\n\t"
        "v_mfma_f32_16x16x32_bf16 v[44:47], v[108:111], v[88:91], 0\n\t"
        "ds_read_b64_tr_b16 v[150:151], v198 offset:4848\n\t"
        "v_mfma_f32_16x16x32_bf16 v[60:63], v[108:111], v[92:95], 0\n\t"
        "ds_read_b64_tr_b16 v[152:153], v199 offset:4608\n\t"
        "v_mfma_f32_16x16x32_bf16 v[64:67], v[112:115], v[80:83], 0\n\t"
        "ds_read_b64_tr_b16 v[154:155], v199 offset:4752\n\t"
        "v_mfma_f32_16x16x32_bf16 v[68:71], v[112:115], v[84:87], 0\n\t"
        "ds_read_b64_tr_b16 v[156:157], v200 offset:1024\n\t"
        "v_mfma_f32_16x16x32_bf16 v[72:75], v[88:91], v[116:119], 0\n\t"
        "ds_read_b64_tr_b16 v[158:159], v200 offset:1280\n\t"
        "v_mfma_f32_16x16x32_bf16 v[76:79], v[92:95], v[116:119], 0\n\t"
        "s_waitcnt lgkmcnt(0)\n\t"
        "s_barrier\n\t"
        "v_mfma_f32_16x16x32_bf16 v[0:3], v[136:139], v[120:123], v[0:3]\n\t"
        "ds_read_b128 v[80:83], v196 offset:16384\n\t"
        "v_mfma_f32_16x16x32_bf16 v[16:19], v[136:139], v[124:127], v[16:19]\n\t"
        "ds_read_b64_tr_b16 v[96:97], v198 offset:18464\n\t"
        "v_mfma_f32_16x16x32_bf16 v[32:35], v[136:139], v[128:131], v[32:35]\n\t"
        "ds_read_b64_tr_b16 v[98:99], v198 offset:18608\n\t"
        "v_mfma_f32_16x16x32_bf16 v[48:51], v[136:139], v[132:135], v[48:51]\n\t"
        "ds_read_b128 v[84:87], v196 offset:18432\n\t"
        "v_mfma_f32_16x16x32_bf16 v[4:7], v[140:143], v[120:123], v[4:7]\n\t"
        "v_mfma_f32_16x16x32_bf16 v[20:23], v[140:143], v[124:127], v[20:23]\n\t"
        "ds_read_b128 v[88:91], v197 offset:16384\n\t"
        "v_mfma_f32_16x16x32_bf16 v[36:39], v[140:143], v[128:131], v[36:39]\n\t"
        "ds_read_b128 v[92:95], v197 offset:18432\n\t"
        "v_mfma_f32_16x16x32_bf16 v[52:55], v[140:143], v[132:135], v[52:55]\n\t"
        "ds_read_b64_tr_b16 v[100:101], v198 offset:18496\n\t"
        "v_mfma_f32_16x16x32_bf16 v[8:11], v[144:147], v[120:123], v[8:11]\n\t"
        "ds_read_b64_tr_b16 v[102:103], v198 offset:18640\n\t"
        "v_mfma_f32_16x16x32_bf16 v[24:27], v[144:147], v[124:127], v[24:27]\n\t"
        "v_mfma_f32_16x16x32_bf16 v[40:43], v[144:147], v[128:131], v[40:43]\n\t"
        "ds_read_b64_tr_b16 v[104:105], v198 offset:18528\n\t"
        "v_mfma_f32_16x16x32_bf16 v[56:59], v[144:147], v[132:135], v[56:59]\n\t"
        "ds_read_b64_tr_b16 v[106:107], v198 offset:18672\n\t"
        "v_mfma_f32_16x16x32_bf16 v[12:15], v[148:151], v[120:123], v[12:15]\n\t"
        "ds_read_b64_tr_b16 v[108:109], v198 offset:18560\n\t"
        "v_mfma_f32_16x16x32_bf16 v[28:31], v[148:151], v[124:127], v[28:31]\n\t"
        "ds_read_b64_tr_b16 v[110:111], v198 offset:18704\n\t"
        "v_mfma_f32_16x16x32_bf16 v[44:47], v[148:151], v[128:131], v[44:47]\n\t"
        "v_mfma_f32_16x16x32_bf16 v[60:63], v[148:151], v[132:135], v[60:63]\n\t"
        "ds_read_b64_tr_b16 v[112:113], v199 offset:18464\n\t"
        "v_mfma_f32_16x16x32_bf16 v[64:67], v[152:155], v[120:123], v[64:67]\n\t"
        "ds_read_b64_tr_b16 v[114:115], v199 offset:18608\n\t"
        "v_mfma_f32_16x16x32_bf16 v[68:71], v[152:155], v[124:127], v[68:71]\n\t"
        "ds_read_b64_tr_b16 v[116:117], v200 offset:2048\n\t"
        "v_mfma_f32_16x16x32_bf16 v[72:75], v[128:131], v[156:159], v[72:75]\n\t"
        "ds_read_b64_tr_b16 v[118:119], v200 offset:2304\n\t"
        "v_mfma_f32_16x16x32_bf16 v[76:79], v[132:135], v[156:159], v[76:79]\n\t"
        "s_waitcnt lgkmcnt(0)\n\t"
        "v_mfma_f32_16x16x32_bf16 v[0:3], v[96:99], v[80:83], v[0:3]\n\t"
        "ds_read_b128 v[120:123], v218 offset:16384\n\t"
        "v_mfma_f32_16x16x32_bf16 v[16:19], v[96:99], v[84:87], v[16:19]\n\t"
        "ds_read_b64_tr_b16 v[136:137], v198 offset:23072\n\t"
        "v_mfma_f32_16x16x32_bf16 v[32:35], v[96:99], v[88:91], v[32:35]\n\t"
        "ds_read_b64_tr_b16 v[138:139], v198 offset:23216\n\t"
        "v_mfma_f32_16x16x32_bf16 v[48:51], v[96:99], v[92:95], v[48:51]\n\t"
        "ds_read_b128 v[124:127], v218 offset:18432\n\t"
        "v_mfma_f32_16x16x32_bf16 v[4:7], v[100:103], v[80:83], v[4:7]\n\t"
        "ds_read_b128 v[128:131], v219 offset:16384\n\t"
        "v_mfma_f32_16x16x32_bf16 v[20:23], v[100:103], v[84:87], v[20:23]\n\t"
        "ds_read_b128 v[132:135], v219 offset:18432\n\t"
        "v_mfma_f32_16x16x32_bf16 v[36:39], v[100:103], v[88:91], v[36:39]\n\t"
        "v_mfma_f32_16x16x32_bf16 v[52:55], v[100:103], v[92:95], v[52:55]\n\t"
        "ds_read_b64_tr_b16 v[140:141], v198 offset:23104\n\t"
        "v_mfma_f32_16x16x32_bf16 v[8:11], v[104:107], v[80:83], v[8:11]\n\t"
        "ds_read_b64_tr_b16 v[142:143], v198 offset:23248\n\t"
        "v_mfma_f32_16x16x32_bf16 v[24:27], v[104:107], v[84:87], v[24:27]\n\t"
        "ds_read_b64_tr_b16 v[144:145], v198 offset:23136\n\t"
        "v_mfma_f32_16x16x32_bf16 v[40:43], v[104:107], v[88:91], v[40:43]\n\t"
        "ds_read_b64_tr_b16 v[146:147], v198 offset:23280\n\t"
        "v_mfma_f32_16x16x32_bf16 v[56:59], v[104:107], v[92:95], v[56:59]\n\t"
        "ds_read_b64_tr_b16 v[148:149], v198 offset:23168\n\t"
        "v_mfma_f32_16x16x32_bf16 v[12:15], v[108:111], v[80:83], v[12:15]\n\t"
        "ds_read_b64_tr_b16 v[150:151], v198 offset:23312\n\t"
        "v_mfma_f32_16x16x32_bf16 v[28:31], v[108:111], v[84:87], v[28:31]\n\t"
        "v_mfma_f32_16x16x32_bf16 v[44:47], v[108:111], v[88:91], v[44:47]\n\t"
        "ds_read_b64_tr_b16 v[152:153], v199 offset:23072\n\t"
        "v_mfma_f32_16x16x32_bf16 v[60:63], v[108:111], v[92:95], v[60:63]\n\t"
        "ds_read_b64_tr_b16 v[154:155], v199 offset:23216\n\t"
        "v_mfma_f32_16x16x32_bf16 v[64:67], v[112:115], v[80:83], v[64:67]\n\t"
        "ds_read_b64_tr_b16 v[156:157], v200 offset:3072\n\t"
        "v_mfma_f32_16x16x32_bf16 v[68:71], v[112:115], v[84:87], v[68:71]\n\t"
        "ds_read_b64_tr_b16 v[158:159], v200 offset:3328\n\t"
        "v_mfma_f32_16x16x32_bf16 v[72:75], v[88:91], v[116:119], v[72:75]\n\t"
        "s_waitcnt vmcnt(0)\n\t"
        "v_mfma_f32_16x16x32_bf16 v[76:79], v[92:95], v[116:119], v[76:79]\n\t"
        "s_waitcnt lgkmcnt(0)\n\t"
        "s_barrier\n\t"
        "v_mfma_f32_16x16x32_bf16 v[0:3], v[136:139], v[120:123], v[0:3]\n\t"
        "ds_read_b128 v[80:83], v196 offset:32768\n\t"
        "v_mfma_f32_16x16x32_bf16 v[16:19], v[136:139], v[124:127], v[16:19]\n\t"
        "ds_read_b64_tr_b16 v[96:97], v198 offset:36928\n\t"
        "v_mfma_f32_16x16x32_bf16 v[32:35], v[136:139], v[128:131], v[32:35]\n\t"
        "ds_read_b64_tr_b16 v[98:99], v198 offset:37072\n\t"
        "v_mfma_f32_16x16x32_bf16 v[48:51], v[136:139], v[132:135], v[48:51]\n\t"
        "ds_read_b128 v[84:87], v196 offset:34816\n\t"
        "v_mfma_f32_16x16x32_bf16 v[4:7], v[140:143], v[120:123], v[4:7]\n\t"
        "v_mfma_f32_16x16x32_bf16 v[20:23], v[140:143], v[124:127], v[20:23]\n\t"
        "ds_read_b128 v[88:91], v197 offset:32768\n\t"
        "v_mfma_f32_16x16x32_bf16 v[36:39], v[140:143], v[128:131], v[36:39]\n\t"
        "ds_read_b128 v[92:95], v197 offset:34816\n\t"
        "v_mfma_f32_16x16x32_bf16 v[52:55], v[140:143], v[132:135], v[52:55]\n\t"
        "ds_read_b64_tr_b16 v[100:101], v198 offset:36960\n\t"
        "v_mfma_f32_16x16x32_bf16 v[8:11], v[144:147], v[120:123], v[8:11]\n\t"
        "ds_read_b64_tr_b16 v[102:103], v198 offset:37104\n\t"
        "v_mfma_f32_16x16x32_bf16 v[24:27], v[144:147], v[124:127], v[24:27]\n\t"
        "v_mfma_f32_16x16x32_bf16 v[40:43], v[144:147], v[128:131], v[40:43]\n\t"
        "ds_read_b64_tr_b16 v[104:105], v198 offset:36992\n\t"
        "v_mfma_f32_16x16x32_bf16 v[56:59], v[144:147], v[132:135], v[56:59]\n\t"
        "ds_read_b64_tr_b16 v[106:107], v198 offset:37136\n\t"
        "v_mfma_f32_16x16x32_bf16 v[12:15], v[148:151], v[120:123], v[12:15]\n\t"
        "ds_read_b64_tr_b16 v[108:109], v198 offset:37024\n\t"
        "v_mfma_f32_16x16x32_bf16 v[28:31], v[148:151], v[124:127], v[28:31]\n\t"
        "ds_read_b64_tr_b16 v[110:111], v198 offset:37168\n\t"
        "v_mfma_f32_16x16x32_bf16 v[44:47], v[148:151], v[128:131], v[44:47]\n\t"
        "v_mfma_f32_16x16x32_bf16 v[60:63], v[148:151], v[132:135], v[60:63]\n\t"
        "ds_read_b64_tr_b16 v[112:113], v199 offset:36928\n\t"
        "v_mfma_f32_16x16x32_bf16 v[64:67], v[152:155], v[120:123], v[64:67]\n\t"
        "ds_read_b64_tr_b16 v[114:115], v199 offset:37072\n\t"
        "v_mfma_f32_16x16x32_bf16 v[68:71], v[152:155], v[124:127], v[68:71]\n\t"
        "ds_read_b64_tr_b16 v[116:117], v200 offset:4096\n\t"
        "v_mfma_f32_16x16x32_bf16 v[72:75], v[128:131], v[156:159], v[72:75]\n\t"
        "ds_read_b64_tr_b16 v[118:119], v200 offset:4352\n\t"
        "v_mfma_f32_16x16x32_bf16 v[76:79], v[132:135], v[156:159], v[76:79]\n\t"
        "s_waitcnt lgkmcnt(0)\n\t"
        "v_mfma_f32_16x16x32_bf16 v[0:3], v[96:99], v[80:83], v[0:3]\n\t"
        "ds_read_b128 v[120:123], v218 offset:32768\n\t"
        "v_mfma_f32_16x16x32_bf16 v[16:19], v[96:99], v[84:87], v[16:19]\n\t"
        "ds_read_b64_tr_b16 v[136:137], v198 offset:41536\n\t"
        "v_mfma_f32_16x16x32_bf16 v[32:35], v[96:99], v[88:91], v[32:35]\n\t"
        "s_and_b32 s53, s52, 0xff\n\t"
        "s_mul_i32 s53, s53, 1152\n\t"
        "s_add_u32 m0, s54, 8192\n\t"
        "v_add_co_u32 v224, vcc, s53, v220\n\t"
        "v_addc_co_u32 v225, vcc, 0, v221, vcc\n\t"
        "global_load_lds_dwordx4 v[224:225], off\n\t"
        "s_lshr_b32 s52, s52, 8\n\t"
        "v_mfma_f32_16x16x32_bf16 v[48:51], v[96:99], v[92:95], v[48:51]\n\t"
        "ds_read_b64_tr_b16 v[138:139], v198 offset:41680\n\t"
        "v_mfma_f32_16x16x32_bf16 v[4:7], v[100:103], v[80:83], v[4:7]\n\t"
        "ds_read_b128 v[124:127], v218 offset:34816\n\t"
        "v_mfma_f32_16x16x32_bf16 v[20:23], v[100:103], v[84:87], v[20:23]\n\t"
        "ds_read_b128 v[128:131], v219 offset:32768\n\t"
        "v_mfma_f32_16x16x32_bf16 v[36:39], v[100:103], v[88:91], v[36:39]\n\t"
        "v_mfma_f32_16x16x32_bf16 v[52:55], v[100:103], v[92:95], v[52:55]\n\t"
        "ds_read_b128 v[132:135], v219 offset:34816\n\t"
        "v_mfma_f32_16x16x32_bf16 v[8:11], v[104:107], v[80:83], v[8:11]\n\t"
        "ds_read_b64_tr_b16 v[140:141], v198 offset:41568\n\t"
        "v_mfma_f32_16x16x32_bf16 v[24:27], v[104:107], v[84:87], v[24:27]\n\t"
        "ds_read_b64_tr_b16 v[142:143], v198 offset:41712\n\t"
        "v_mfma_f32_16x16x32_bf16 v[40:43], v[104:107], v[88:91], v[40:43]\n\t"
        "ds_read_b64_tr_b16 v[144:145], v198 offset:41600\n\t"
        "v_mfma_f32_16x16x32_bf16 v[56:59], v[104:107], v[92:95], v[56:59]\n\t"
        "ds_read_b64_tr_b16 v[146:147], v198 offset:41744\n\t"
        "v_mfma_f32_16x16x32_bf16 v[12:15], v[108:111], v[80:83], v[12:15]\n\t"
        "ds_read_b64_tr_b16 v[148:149], v198 offset:41632\n\t"
        "v_mfma_f32_16x16x32_bf16 v[28:31], v[108:111], v[84:87], v[28:31]\n\t"
        "v_mfma_f32_16x16x32_bf16 v[44:47], v[108:111], v[88:91], v[44:47]\n\t"
        "ds_read_b64_tr_b16 v[150:151], v198 offset:41776\n\t"
        "v_mfma_f32_16x16x32_bf16 v[60:63], v[108:111], v[92:95], v[60:63]\n\t"
        "ds_read_b64_tr_b16 v[152:153], v199 offset:41536\n\t"
        "v_mfma_f32_16x16x32_bf16 v[64:67], v[112:115], v[80:83], v[64:67]\n\t"
        "ds_read_b64_tr_b16 v[154:155], v199 offset:41680\n\t"
        "v_mfma_f32_16x16x32_bf16 v[68:71], v[112:115], v[84:87], v[68:71]\n\t"
        "ds_read_b64_tr_b16 v[156:157], v200 offset:5120\n\t"
        "v_mfma_f32_16x16x32_bf16 v[72:75], v[88:91], v[116:119], v[72:75]\n\t"
        "ds_read_b64_tr_b16 v[158:159], v200 offset:5376\n\t"
        "v_mfma_f32_16x16x32_bf16 v[76:79], v[92:95], v[116:119], v[76:79]\n\t"
        "s_waitcnt lgkmcnt(0)\n\t"
        "s_barrier\n\t"
        "v_mfma_f32_16x16x32_bf16 v[0:3], v[136:139], v[120:123], v[0:3]\n\t"
        "ds_read_b128 v[80:83], v196 offset:49152\n\t"
        "v_mfma_f32_16x16x32_bf16 v[16:19], v[136:139], v[124:127], v[16:19]\n\t"
        "ds_read_b64_tr_b16 v[96:97], v198 offset:55392\n\t"
        "v_mfma_f32_16x16x32_bf16 v[32:35], v[136:139], v[128:131], v[32:35]\n\t"
        "ds_read_b64_tr_b16 v[98:99], v198 offset:55536\n\t"
        "v_mfma_f32_16x16x32_bf16 v[48:51], v[136:139], v[132:135], v[48:51]\n\t"
        "ds_read_b128 v[84:87], v196 offset:51200\n\t"
        "v_mfma_f32_16x16x32_bf16 v[4:7], v[140:143], v[120:123], v[4:7]\n\t"
        "v_mfma_f32_16x16x32_bf16 v[20:23], v[140:143], v[124:127], v[20:23]\n\t"
        "ds_read_b128 v[88:91], v197 offset:49152\n\t"
        "v_mfma_f32_16x16x32_bf16 v[36:39], v[140:143], v[128:131], v[36:39]\n\t"
        "ds_read_b128 v[92:95], v197 offset:51200\n\t"
        "v_mfma_f32_16x16x32_bf16 v[52:55], v[140:143], v[132:135], v[52:55]\n\t"
        "ds_read_b64_tr_b16 v[100:101], v198 offset:55424\n\t"
        "v_mfma_f32_16x16x32_bf16 v[8:11], v[144:147], v[120:123], v[8:11]\n\t"
        "ds_read_b64_tr_b16 v[102:103], v198 offset:55568\n\t"
        "v_mfma_f32_16x16x32_bf16 v[24:27], v[144:147], v[124:127], v[24:27]\n\t"
        "v_mfma_f32_16x16x32_bf16 v[40:43], v[144:147], v[128:131], v[40:43]\n\t"
        "ds_read_b64_tr_b16 v[104:105], v198 offset:55456\n\t"
        "v_mfma_f32_16x16x32_bf16 v[56:59], v[144:147], v[132:135], v[56:59]\n\t"
        "ds_read_b64_tr_b16 v[106:107], v198 offset:55600\n\t"
        "v_mfma_f32_16x16x32_bf16 v[12:15], v[148:151], v[120:123], v[12:15]\n\t"
        "ds_read_b64_tr_b16 v[108:109], v198 offset:55488\n\t"
        "v_mfma_f32_16x16x32_bf16 v[28:31], v[148:151], v[124:127], v[28:31]\n\t"
        "ds_read_b64_tr_b16 v[110:111], v198 offset:55632\n\t"
        "v_mfma_f32_16x16x32_bf16 v[44:47], v[148:151], v[128:131], v[44:47]\n\t"
        "v_mfma_f32_16x16x32_bf16 v[60:63], v[148:151], v[132:135], v[60:63]\n\t"
        "ds_read_b64_tr_b16 v[112:113], v199 offset:55392\n\t"
        "v_mfma_f32_16x16x32_bf16 v[64:67], v[152:155], v[120:123], v[64:67]\n\t"
        "ds_read_b64_tr_b16 v[114:115], v199 offset:55536\n\t"
        "v_mfma_f32_16x16x32_bf16 v[68:71], v[152:155], v[124:127], v[68:71]\n\t"
        "ds_read_b64_tr_b16 v[116:117], v200 offset:6144\n\t"
        "v_mfma_f32_16x16x32_bf16 v[72:75], v[128:131], v[156:159], v[72:75]\n\t"
        "ds_read_b64_tr_b16 v[118:119], v200 offset:6400\n\t"
        "v_mfma_f32_16x16x32_bf16 v[76:79], v[132:135], v[156:159], v[76:79]\n\t"
        "s_waitcnt lgkmcnt(0)\n\t"
        "v_mfma_f32_16x16x32_bf16 v[0:3], v[96:99], v[80:83], v[0:3]\n\t"
        "ds_read_b128 v[120:123], v218 offset:49152\n\t"
        "v_mfma_f32_16x16x32_bf16 v[16:19], v[96:99], v[84:87], v[16:19]\n\t"
        "ds_read_b64_tr_b16 v[136:137], v198 offset:60000\n\t"
        "v_mfma_f32_16x16x32_bf16 v[32:35], v[96:99], v[88:91], v[32:35]\n\t"
        "ds_read_b64_tr_b16 v[138:139], v198 offset:60144\n\t"
        "v_mfma_f32_16x16x32_bf16 v[48:51], v[96:99], v[92:95], v[48:51]\n\t"
        "ds_read_b128 v[124:127], v218 offset:51200\n\t"
        "v_mfma_f32_16x16x32_bf16 v[4:7], v[100:103], v[80:83], v[4:7]\n\t"
        "ds_read_b128 v[128:131], v219 offset:49152\n\t"
        "v_mfma_f32_16x16x32_bf16 v[20:23], v[100:103], v[84:87], v[20:23]\n\t"
        "ds_read_b128 v[132:135], v219 offset:51200\n\t"
        "v_mfma_f32_16x16x32_bf16 v[36:39], v[100:103], v[88:91], v[36:39]\n\t"
        "v_mfma_f32_16x16x32_bf16 v[52:55], v[100:103], v[92:95], v[52:55]\n\t"
        "ds_read_b64_tr_b16 v[140:141], v198 offset:60032\n\t"
        "v_mfma_f32_16x16x32_bf16 v[8:11], v[104:107], v[80:83], v[8:11]\n\t"
        "ds_read_b64_tr_b16 v[142:143], v198 offset:60176\n\t"
        "v_mfma_f32_16x16x32_bf16 v[24:27], v[104:107], v[84:87], v[24:27]\n\t"
        "ds_read_b64_tr_b16 v[144:145], v198 offset:60064\n\t"
        "v_mfma_f32_16x16x32_bf16 v[40:43], v[104:107], v[88:91], v[40:43]\n\t"
        "ds_read_b64_tr_b16 v[146:147], v198 offset:60208\n\t"
        "v_mfma_f32_16x16x32_bf16 v[56:59], v[104:107], v[92:95], v[56:59]\n\t"
        "ds_read_b64_tr_b16 v[148:149], v198 offset:60096\n\t"
        "v_mfma_f32_16x16x32_bf16 v[12:15], v[108:111], v[80:83], v[12:15]\n\t"
        "ds_read_b64_tr_b16 v[150:151], v198 offset:60240\n\t"
        "v_mfma_f32_16x16x32_bf16 v[28:31], v[108:111], v[84:87], v[28:31]\n\t"
        "v_mfma_f32_16x16x32_bf16 v[44:47], v[108:111], v[88:91], v[44:47]\n\t"
        "ds_read_b64_tr_b16 v[152:153], v199 offset:60000\n\t"
        "v_mfma_f32_16x16x32_bf16 v[60:63], v[108:111], v[92:95], v[60:63]\n\t"
        "ds_read_b64_tr_b16 v[154:155], v199 offset:60144\n\t"
        "v_mfma_f32_16x16x32_bf16 v[64:67], v[112:115], v[80:83], v[64:67]\n\t"
        "ds_read_b64_tr_b16 v[156:157], v200 offset:7168\n\t"
        "v_mfma_f32_16x16x32_bf16 v[68:71], v[112:115], v[84:87], v[68:71]\n\t"
        "ds_read_b64_tr_b16 v[158:159], v200 offset:7424\n\t"
        "v_mfma_f32_16x16x32_bf16 v[72:75], v[88:91], v[116:119], v[72:75]\n\t"
        "s_waitcnt vmcnt(0)\n\t"
        "v_mfma_f32_16x16x32_bf16 v[76:79], v[92:95], v[116:119], v[76:79]\n\t"
        "s_waitcnt lgkmcnt(0)\n\t"
        "s_barrier\n\t"
        "v_mfma_f32_16x16x32_bf16 v[0:3], v[136:139], v[120:123], v[0:3]\n\t"
        "ds_read_b128 v[80:83], v196 offset:0\n\t"
        "v_mfma_f32_16x16x32_bf16 v[16:19], v[136:139], v[124:127], v[16:19]\n\t"
        "ds_read_b64_tr_b16 v[96:97], v198 offset:0\n\t"
        "v_mfma_f32_16x16x32_bf16 v[32:35], v[136:139], v[128:131], v[32:35]\n\t"
        "ds_read_b64_tr_b16 v[98:99], v198 offset:144\n\t"
        "v_mfma_f32_16x16x32_bf16 v[48:51], v[136:139], v[132:135], v[48:51]\n\t"
        "ds_read_b128 v[84:87], v196 offset:2048\n\t"
        "v_mfma_f32_16x16x32_bf16 v[4:7], v[140:143], v[120:123], v[4:7]\n\t"
        "v_mfma_f32_16x16x32_bf16 v[20:23], v[140:143], v[124:127], v[20:23]\n\t"
        "ds_read_b128 v[88:91], v197 offset:0\n\t"
        "v_mfma_f32_16x16x32_bf16 v[36:39], v[140:143], v[128:131], v[36:39]\n\t"
        "ds_read_b128 v[92:95], v197 offset:2048\n\t"
        "v_mfma_f32_16x16x32_bf16 v[52:55], v[140:143], v[132:135], v[52:55]\n\t"
        "ds_read_b64_tr_b16 v[100:101], v198 offset:32\n\t"
        "v_mfma_f32_16x16x32_bf16 v[8:11], v[144:147], v[120:123], v[8:11]\n\t"
        "ds_read_b64_tr_b16 v[102:103], v198 offset:176\n\t"
        "v_mfma_f32_16x16x32_bf16 v[24:27], v[144:147], v[124:127], v[24:27]\n\t"
        "v_mfma_f32_16x16x32_bf16 v[40:43], v[144:147], v[128:131], v[40:43]\n\t"
        "ds_read_b64_tr_b16 v[104:105], v198 offset:64\n\t"
        "v_mfma_f32_16x16x32_bf16 v[56:59], v[144:147], v[132:135], v[56:59]\n\t"
        "ds_read_b64_tr_b16 v[106:107], v198 offset:208\n\t"
        "v_mfma_f32_16x16x32_bf16 v[12:15], v[148:151], v[120:123], v[12:15]\n\t"
        "ds_read_b64_tr_b16 v[108:109], v198 offset:96\n\t"
        "v_mfma_f32_16x16x32_bf16 v[28:31], v[148:151], v[124:127], v[28:31]\n\t"
        "ds_read_b64_tr_b16 v[110:111], v198 offset:240\n\t"
        "v_mfma_f32_16x16x32_bf16 v[44:47], v[148:151], v[128:131], v[44:47]\n\t"
        "v_mfma_f32_16x16x32_bf16 v[60:63], v[148:151], v[132:135], v[60:63]\n\t"
        "ds_read_b64_tr_b16 v[112:113], v199 offset:0\n\t"
        "v_mfma_f32_16x16x32_bf16 v[64:67], v[152:155], v[120:123], v[64:67]\n\t"
        "ds_read_b64_tr_b16 v[114:115], v199 offset:144\n\t"
        "v_mfma_f32_16x16x32_bf16 v[68:71], v[152:155], v[124:127], v[68:71]\n\t"
        "ds_read_b64_tr_b16 v[116:117], v200 offset:8192\n\t"
        "v_mfma_f32_16x16x32_bf16 v[72:75], v[128:131], v[156:159], v[72:75]\n\t"
        "ds_read_b64_tr_b16 v[118:119], v200 offset:8448\n\t"
        "v_mfma_f32_16x16x32_bf16 v[76:79], v[132:135], v[156:159], v[76:79]\n\t"
        "s_waitcnt lgkmcnt(0)\n\t"
        "v_mfma_f32_16x16x32_bf16 v[0:3], v[96:99], v[80:83], v[0:3]\n\t"
        "ds_read_b128 v[120:123], v218 offset:0\n\t"
        "v_mfma_f32_16x16x32_bf16 v[16:19], v[96:99], v[84:87], v[16:19]\n\t"
        "ds_read_b64_tr_b16 v[136:137], v198 offset:4608\n\t"
        "v_mfma_f32_16x16x32_bf16 v[32:35], v[96:99], v[88:91], v[32:35]\n\t"
        "s_and_b32 s53, s52, 0xff\n\t"
        "s_mul_i32 s53, s53, 1152\n\t"
        "s_add_u32 m0, s54, 12288\n\t"
        "v_add_co_u32 v224, vcc, s53, v220\n\t"
        "v_addc_co_u32 v225, vcc, 0, v221, vcc\n\t"
        "global_load_lds_dwordx4 v[224:225], off\n\t"
        "s_lshr_b32 s52, s52, 8\n\t"
        "v_mfma_f32_16x16x32_bf16 v[48:51], v[96:99], v[92:95], v[48:51]\n\t"
        "ds_read_b64_tr_b16 v[138:139], v198 offset:4752\n\t"
        "v_mfma_f32_16x16x32_bf16 v[4:7], v[100:103], v[80:83], v[4:7]\n\t"
        "ds_read_b128 v[124:127], v218 offset:2048\n\t"
        "v_mfma_f32_16x16x32_bf16 v[20:23], v[100:103], v[84:87], v[20:23]\n\t"
        "ds_read_b128 v[128:131], v219 offset:0\n\t"
        "v_mfma_f32_16x16x32_bf16 v[36:39], v[100:103], v[88:91], v[36:39]\n\t"
        "v_mfma_f32_16x16x32_bf16 v[52:55], v[100:103], v[92:95], v[52:55]\n\t"
        "ds_read_b128 v[132:135], v219 offset:2048\n\t"
        "v_mfma_f32_16x16x32_bf16 v[8:11], v[104:107], v[80:83], v[8:11]\n\t"
        "ds_read_b64_tr_b16 v[140:141], v198 offset:4640\n\t"
        "v_mfma_f32_16x16x32_bf16 v[24:27], v[104:107], v[84:87], v[24:27]\n\t"
        "ds_read_b64_tr_b16 v[142:143], v198 offset:4784\n\t"
        "v_mfma_f32_16x16x32_bf16 v[40:43], v[104:107], v[88:91], v[40:43]\n\t"
        "ds_read_b64_tr_b16 v[144:145], v198 offset:4672\n\t"
        "v_mfma_f32_16x16x32_bf16 v[56:59], v[104:107], v[92:95], v[56:59]\n\t"
        "ds_read_b64_tr_b16 v[146:147], v198 offset:4816\n\t"
        "v_mfma_f32_16x16x32_bf16 v[12:15], v[108:111], v[80:83], v[12:15]\n\t"
        "ds_read_b64_tr_b16 v[148:149], v198 offset:4704\n\t"
        "v_mfma_f32_16x16x32_bf16 v[28:31], v[108:111], v[84:87], v[28:31]\n\t"
        "v_mfma_f32_16x16x32_bf16 v[44:47], v[108:111], v[88:91], v[44:47]\n\t"
        "ds_read_b64_tr_b16 v[150:151], v198 offset:4848\n\t"
        "v_mfma_f32_16x16x32_bf16 v[60:63], v[108:111], v[92:95], v[60:63]\n\t"
        "ds_read_b64_tr_b16 v[152:153], v199 offset:4608\n\t"
        "v_mfma_f32_16x16x32_bf16 v[64:67], v[112:115], v[80:83], v[64:67]\n\t"
        "ds_read_b64_tr_b16 v[154:155], v199 offset:4752\n\t"
        "v_mfma_f32_16x16x32_bf16 v[68:71], v[112:115], v[84:87], v[68:71]\n\t"
        "ds_read_b64_tr_b16 v[156:157], v200 offset:9216\n\t"
        "v_mfma_f32_16x16x32_bf16 v[72:75], v[88:91], v[116:119], v[72:75]\n\t"
        "ds_read_b64_tr_b16 v[158:159], v200 offset:9472\n\t"
        "v_mfma_f32_16x16x32_bf16 v[76:79], v[92:95], v[116:119], v[76:79]\n\t"
        "s_waitcnt lgkmcnt(0)\n\t"
        "s_barrier\n\t"
        "v_mfma_f32_16x16x32_bf16 v[0:3], v[136:139], v[120:123], v[0:3]\n\t"
        "ds_read_b128 v[80:83], v196 offset:16384\n\t"
        "v_mfma_f32_16x16x32_bf16 v[16:19], v[136:139], v[124:127], v[16:19]\n\t"
        "ds_read_b64_tr_b16 v[96:97], v198 offset:18464\n\t"
        "v_mfma_f32_16x16x32_bf16 v[32:35], v[136:139], v[128:131], v[32:35]\n\t"
        "ds_read_b64_tr_b16 v[98:99], v198 offset:18608\n\t"
        "v_mfma_f32_16x16x32_bf16 v[48:51], v[136:139], v[132:135], v[48:51]\n\t"
        "ds_read_b128 v[84:87], v196 offset:18432\n\t"
        "v_mfma_f32_16x16x32_bf16 v[4:7], v[140:143], v[120:123], v[4:7]\n\t"
        "v_mfma_f32_16x16x32_bf16 v[20:23], v[140:143], v[124:127], v[20:23]\n\t"
        "ds_read_b128 v[88:91], v197 offset:16384\n\t"
        "v_mfma_f32_16x16x32_bf16 v[36:39], v[140:143], v[128:131], v[36:39]\n\t"
        "ds_read_b128 v[92:95], v197 offset:18432\n\t"
        "v_mfma_f32_16x16x32_bf16 v[52:55], v[140:143], v[132:135], v[52:55]\n\t"
        "ds_read_b64_tr_b16 v[100:101], v198 offset:18496\n\t"
        "v_mfma_f32_16x16x32_bf16 v[8:11], v[144:147], v[120:123], v[8:11]\n\t"
        "ds_read_b64_tr_b16 v[102:103], v198 offset:18640\n\t"
        "v_mfma_f32_16x16x32_bf16 v[24:27], v[144:147], v[124:127], v[24:27]\n\t"
        "v_mfma_f32_16x16x32_bf16 v[40:43], v[144:147], v[128:131], v[40:43]\n\t"
        "ds_read_b64_tr_b16 v[104:105], v198 offset:18528\n\t"
        "v_mfma_f32_16x16x32_bf16 v[56:59], v[144:147], v[132:135], v[56:59]\n\t"
        "ds_read_b64_tr_b16 v[106:107], v198 offset:18672\n\t"
        "v_mfma_f32_16x16x32_bf16 v[12:15], v[148:151], v[120:123], v[12:15]\n\t"
        "ds_read_b64_tr_b16 v[108:109], v198 offset:18560\n\t"
        "v_mfma_f32_16x16x32_bf16 v[28:31], v[148:151], v[124:127], v[28:31]\n\t"
        "ds_read_b64_tr_b16 v[110:111], v198 offset:18704\n\t"
        "v_mfma_f32_16x16x32_bf16 v[44:47], v[148:151], v[128:131], v[44:47]\n\t"
        "v_mfma_f32_16x16x32_bf16 v[60:63], v[148:151], v[132:135], v[60:63]\n\t"
        "ds_read_b64_tr_b16 v[112:113], v199 offset:18464\n\t"
        "v_mfma_f32_16x16x32_bf16 v[64:67], v[152:155], v[120:123], v[64:67]\n\t"
        "ds_read_b64_tr_b16 v[114:115], v199 offset:18608\n\t"
        "v_mfma_f32_16x16x32_bf16 v[68:71], v[152:155], v[124:127], v[68:71]\n\t"
        "ds_read_b64_tr_b16 v[116:117], v200 offset:10240\n\t"
        "v_mfma_f32_16x16x32_bf16 v[72:75], v[128:131], v[156:159], v[72:75]\n\t"
        "ds_read_b64_tr_b16 v[118:119], v200 offset:10496\n\t"
        "v_mfma_f32_16x16x32_bf16 v[76:79], v[132:135], v[156:159], v[76:79]\n\t"
        "s_waitcnt lgkmcnt(0)\n\t"
        "v_mfma_f32_16x16x32_bf16 v[0:3], v[96:99], v[80:83], v[0:3]\n\t"
        "ds_read_b128 v[120:123], v218 offset:16384\n\t"
        "v_mfma_f32_16x16x32_bf16 v[16:19], v[96:99], v[84:87], v[16:19]\n\t"
        "ds_read_b64_tr_b16 v[136:137], v198 offset:23072\n\t"
        "v_mfma_f32_16x16x32_bf16 v[32:35], v[96:99], v[88:91], v[32:35]\n\t"
        "ds_read_b64_tr_b16 v[138:139], v198 offset:23216\n\t"
        "v_mfma_f32_16x16x32_bf16 v[48:51], v[96:99], v[92:95], v[48:51]\n\t"
        "ds_read_b128 v[124:127], v218 offset:18432\n\t"
        "v_mfma_f32_16x16x32_bf16 v[4:7], v[100:103], v[80:83], v[4:7]\n\t"
        "ds_read_b128 v[128:131], v219 offset:16384\n\t"
        "v_mfma_f32_16x16x32_bf16 v[20:23], v[100:103], v[84:87], v[20:23]\n\t"
        "ds_read_b128 v[132:135], v219 offset:18432\n\t"
        "v_mfma_f32_16x16x32_bf16 v[36:39], v[100:103], v[88:91], v[36:39]\n\t"
        "v_mfma_f32_16x16x32_bf16 v[52:55], v[100:103], v[92:95], v[52:55]\n\t"
        "ds_read_b64_tr_b16 v[140:141], v198 offset:23104\n\t"
        "v_mfma_f32_16x16x32_bf16 v[8:11], v[104:107], v[80:83], v[8:11]\n\t"
        "ds_read_b64_tr_b16 v[142:143], v198 offset:23248\n\t"
        "v_mfma_f32_16x16x32_bf16 v[24:27], v[104:107], v[84:87], v[24:27]\n\t"
        "ds_read_b64_tr_b16 v[144:145], v198 offset:23136\n\t"
        "v_mfma_f32_16x16x32_bf16 v[40:43], v[104:107], v[88:91], v[40:43]\n\t"
        "ds_read_b64_tr_b16 v[146:147], v198 offset:23280\n\t"
        "v_mfma_f32_16x16x32_bf16 v[56:59], v[104:107], v[92:95], v[56:59]\n\t"
        "ds_read_b64_tr_b16 v[148:149], v198 offset:23168\n\t"
        "v_mfma_f32_16x16x32_bf16 v[12:15], v[108:111], v[80:83], v[12:15]\n\t"
        "ds_read_b64_tr_b16 v[150:151], v198 offset:23312\n\t"
        "v_mfma_f32_16x16x32_bf16 v[28:31], v[108:111], v[84:87], v[28:31]\n\t"
        "v_mfma_f32_16x16x32_bf16 v[44:47], v[108:111], v[88:91], v[44:47]\n\t"
        "ds_read_b64_tr_b16 v[152:153], v199 offset:23072\n\t"
        "v_mfma_f32_16x16x32_bf16 v[60:63], v[108:111], v[92:95], v[60:63]\n\t"
        "ds_read_b64_tr_b16 v[154:155], v199 offset:23216\n\t"
        "v_mfma_f32_16x16x32_bf16 v[64:67], v[112:115], v[80:83], v[64:67]\n\t"
        "ds_read_b64_tr_b16 v[156:157], v200 offset:11264\n\t"
        "v_mfma_f32_16x16x32_bf16 v[68:71], v[112:115], v[84:87], v[68:71]\n\t"
        "ds_read_b64_tr_b16 v[158:159], v200 offset:11520\n\t"
        "v_mfma_f32_16x16x32_bf16 v[72:75], v[88:91], v[116:119], v[72:75]\n\t"
        "s_waitcnt vmcnt(0)\n\t"
        "v_mfma_f32_16x16x32_bf16 v[76:79], v[92:95], v[116:119], v[76:79]\n\t"
        "s_waitcnt lgkmcnt(0)\n\t"
        "s_barrier\n\t"
        "v_mfma_f32_16x16x32_bf16 v[0:3], v[136:139], v[120:123], v[0:3]\n\t"
        "ds_read_b128 v[80:83], v196 offset:32768\n\t"
        "v_mfma_f32_16x16x32_bf16 v[16:19], v[136:139], v[124:127], v[16:19]\n\t"
        "ds_read_b64_tr_b16 v[96:97], v198 offset:36928\n\t"
        "v_mfma_f32_16x16x32_bf16 v[32:35], v[136:139], v[128:131], v[32:35]\n\t"
        "ds_read_b64_tr_b16 v[98:99], v198 offset:37072\n\t"
        "v_mfma_f32_16x16x32_bf16 v[48:51], v[136:139], v[132:135], v[48:51]\n\t"
        "ds_read_b128 v[84:87], v196 offset:34816\n\t"
        "v_mfma_f32_16x16x32_bf16 v[4:7], v[140:143], v[120:123], v[4:7]\n\t"
        "v_mfma_f32_16x16x32_bf16 v[20:23], v[140:143], v[124:127], v[20:23]\n\t"
        "ds_read_b128 v[88:91], v197 offset:32768\n\t"
        "v_mfma_f32_16x16x32_bf16 v[36:39], v[140:143], v[128:131], v[36:39]\n\t"
        "ds_read_b128 v[92:95], v197 offset:34816\n\t"
        "v_mfma_f32_16x16x32_bf16 v[52:55], v[140:143], v[132:135], v[52:55]\n\t"
        "ds_read_b64_tr_b16 v[100:101], v198 offset:36960\n\t"
        "v_mfma_f32_16x16x32_bf16 v[8:11], v[144:147], v[120:123], v[8:11]\n\t"
        "ds_read_b64_tr_b16 v[102:103], v198 offset:37104\n\t"
        "v_mfma_f32_16x16x32_bf16 v[24:27], v[144:147], v[124:127], v[24:27]\n\t"
        "v_mfma_f32_16x16x32_bf16 v[40:43], v[144:147], v[128:131], v[40:43]\n\t"
        "ds_read_b64_tr_b16 v[104:105], v198 offset:36992\n\t"
        "v_mfma_f32_16x16x32_bf16 v[56:59], v[144:147], v[132:135], v[56:59]\n\t"
        "ds_read_b64_tr_b16 v[106:107], v198 offset:37136\n\t"
        "v_mfma_f32_16x16x32_bf16 v[12:15], v[148:151], v[120:123], v[12:15]\n\t"
        "ds_read_b64_tr_b16 v[108:109], v198 offset:37024\n\t"
        "v_mfma_f32_16x16x32_bf16 v[28:31], v[148:151], v[124:127], v[28:31]\n\t"
        "ds_read_b64_tr_b16 v[110:111], v198 offset:37168\n\t"
        "v_mfma_f32_16x16x32_bf16 v[44:47], v[148:151], v[128:131], v[44:47]\n\t"
        "v_mfma_f32_16x16x32_bf16 v[60:63], v[148:151], v[132:135], v[60:63]\n\t"
        "ds_read_b64_tr_b16 v[112:113], v199 offset:36928\n\t"
        "v_mfma_f32_16x16x32_bf16 v[64:67], v[152:155], v[120:123], v[64:67]\n\t"
        "ds_read_b64_tr_b16 v[114:115], v199 offset:37072\n\t"
        "v_mfma_f32_16x16x32_bf16 v[68:71], v[152:155], v[124:127], v[68:71]\n\t"
        "ds_read_b64_tr_b16 v[116:117], v200 offset:12288\n\t"
        "v_mfma_f32_16x16x32_bf16 v[72:75], v[128:131], v[156:159], v[72:75]\n\t"
        "ds_read_b64_tr_b16 v[118:119], v200 offset:12544\n\t"
        "v_mfma_f32_16x16x32_bf16 v[76:79], v[132:135], v[156:159], v[76:79]\n\t"
        "s_waitcnt lgkmcnt(0)\n\t"
        "v_mfma_f32_16x16x32_bf16 v[0:3], v[96:99], v[80:83], v[0:3]\n\t"
        "ds_read_b128 v[120:123], v218 offset:32768\n\t"
        "v_mfma_f32_16x16x32_bf16 v[16:19], v[96:99], v[84:87], v[16:19]\n\t"
        "ds_read_b64_tr_b16 v[136:137], v198 offset:41536\n\t"
        "v_mfma_f32_16x16x32_bf16 v[32:35], v[96:99], v[88:91], v[32:35]\n\t"
        "s_cmp_ge_u32 %[w], 2\n\t"
        "s_cbranch_scc1 46f\n\t"
        "s_and_b32 s53, s52, 0xff\n\t"
        "s_mul_i32 s53, s53, 1152\n\t"
        "s_add_u32 m0, s54, 16384\n\t"
        "v_add_co_u32 v224, vcc, s53, v220\n\t"
        "v_addc_co_u32 v225, vcc, 0, v221, vcc\n\t"
        "global_load_lds_dwordx4 v[224:225], off\n\t"
        "s_lshr_b32 s52, s52, 8\n\t"
        "46:\n\t"
        "v_mfma_f32_16x16x32_bf16 v[48:51], v[96:99], v[92:95], v[48:51]\n\t"
        "ds_read_b64_tr_b16 v[138:139], v198 offset:41680\n\t"
        "v_mfma_f32_16x16x32_bf16 v[4:7], v[100:103], v[80:83], v[4:7]\n\t"
        "ds_read_b128 v[124:127], v218 offset:34816\n\t"
        "v_mfma_f32_16x16x32_bf16 v[20:23], v[100:103], v[84:87], v[20:23]\n\t"
        "ds_read_b128 v[128:131], v219 offset:32768\n\t"
        "v_mfma_f32_16x16x32_bf16 v[36:39], v[100:103], v[88:91], v[36:39]\n\t"
        "v_mfma_f32_16x16x32_bf16 v[52:55], v[100:103], v[92:95], v[52:55]\n\t"
        "ds_read_b128 v[132:135], v219 offset:34816\n\t"
        "v_mfma_f32_16x16x32_bf16 v[8:11], v[104:107], v[80:83], v[8:11]\n\t"
        "ds_read_b64_tr_b16 v[140:141], v198 offset:41568\n\t"
        "v_mfma_f32_16x16x32_bf16 v[24:27], v[104:107], v[84:87], v[24:27]\n\t"
        "ds_read_b64_tr_b16 v[142:143], v198 offset:41712\n\t"
        "v_mfma_f32_16x16x32_bf16 v[40:43], v[104:107], v[88:91], v[40:43]\n\t"
        "ds_read_b64_tr_b16 v[144:145], v198 offset:41600\n\t"
        "v_mfma_f32_16x16x32_bf16 v[56:59], v[104:107], v[92:95], v[56:59]\n\t"
        "ds_read_b64_tr_b16 v[146:147], v198 offset:41744\n\t"
        "v_mfma_f32_16x16x32_bf16 v[12:15], v[108:111], v[80:83], v[12:15]\n\t"
        "ds_read_b64_tr_b16 v[148:149], v198 offset:41632\n\t"
        "v_mfma_f32_16x16x32_bf16 v[28:31], v[108:111], v[84:87], v[28:31]\n\t"
        "v_mfma_f32_16x16x32_bf16 v[44:47], v[108:111], v[88:91], v[44:47]\n\t"
        "ds_read_b64_tr_b16 v[150:151], v198 offset:41776\n\t"
        "v_mfma_f32_16x16x32_bf16 v[60:63], v[108:111], v[92:95], v[60:63]\n\t"
        "ds_read_b64_tr_b16 v[152:153], v199 offset:41536\n\t"
        "v_mfma_f32_16x16x32_bf16 v[64:67], v[112:115], v[80:83], v[64:67]\n\t"
        "ds_read_b64_tr_b16 v[154:155], v199 offset:41680\n\t"
        "v_mfma_f32_16x16x32_bf16 v[68:71], v[112:115], v[84:87], v[68:71]\n\t"
        "ds_read_b64_tr_b16 v[156:157], v200 offset:13312\n\t"
        "v_mfma_f32_16x16x32_bf16 v[72:75], v[88:91], v[116:119], v[72:75]\n\t"
        "ds_read_b64_tr_b16 v[158:159], v200 offset:13568\n\t"
        "v_mfma_f32_16x16x32_bf16 v[76:79], v[92:95], v[116:119], v[76:79]\n\t"
        "s_waitcnt lgkmcnt(0)\n\t"
        "s_barrier\n\t"
        "v_mfma_f32_16x16x32_bf16 v[0:3], v[136:139], v[120:123], v[0:3]\n\t"
        "ds_read_b128 v[80:83], v196 offset:49152\n\t"
        "v_mfma_f32_16x16x32_bf16 v[16:19], v[136:139], v[124:127], v[16:19]\n\t"
        "ds_read_b64_tr_b16 v[96:97], v198 offset:55392\n\t"
        "v_mfma_f32_16x16x32_bf16 v[32:35], v[136:139], v[128:131], v[32:35]\n\t"
        "ds_read_b64_tr_b16 v[98:99], v198 offset:55536\n\t"
        "v_mfma_f32_16x16x32_bf16 v[48:51], v[136:139], v[132:135], v[48:51]\n\t"
        "ds_read_b128 v[84:87], v196 offset:51200\n\t"
        "v_mfma_f32_16x16x32_bf16 v[4:7], v[140:143], v[120:123], v[4:7]\n\t"
        "v_mfma_f32_16x16x32_bf16 v[20:23], v[140:143], v[124:127], v[20:23]\n\t"
        "ds_read_b128 v[88:91], v197 offset:49152\n\t"
        "v_mfma_f32_16x16x32_bf16 v[36:39], v[140:143], v[128:131], v[36:39]\n\t"
        "ds_read_b128 v[92:95], v197 offset:51200\n\t"
        "v_mfma_f32_16x16x32_bf16 v[52:55], v[140:143], v[132:135], v[52:55]\n\t"
        "ds_read_b64_tr_b16 v[100:101], v198 offset:55424\n\t"
        "v_mfma_f32_16x16x32_bf16 v[8:11], v[144:147], v[120:123], v[8:11]\n\t"
        "ds_read_b64_tr_b16 v[102:103], v198 offset:55568\n\t"
        "v_mfma_f32_16x16x32_bf16 v[24:27], v[144:147], v[124:127], v[24:27]\n\t"
        "v_mfma_f32_16x16x32_bf16 v[40:43], v[144:147], v[128:131], v[40:43]\n\t"
        "ds_read_b64_tr_b16 v[104:105], v198 offset:55456\n\t"
        "v_mfma_f32_16x16x32_bf16 v[56:59], v[144:147], v[132:135], v[56:59]\n\t"
        "ds_read_b64_tr_b16 v[106:107], v198 offset:55600\n\t"
        "v_mfma_f32_16x16x32_bf16 v[12:15], v[148:151], v[120:123], v[12:15]\n\t"
        "ds_read_b64_tr_b16 v[108:109], v198 offset:55488\n\t"
        "v_mfma_f32_16x16x32_bf16 v[28:31], v[148:151], v[124:127], v[28:31]\n\t"
        "ds_read_b64_tr_b16 v[110:111], v198 offset:55632\n\t"
        "v_mfma_f32_16x16x32_bf16 v[44:47], v[148:151], v[128:131], v[44:47]\n\t"
        "v_mfma_f32_16x16x32_bf16 v[60:63], v[148:151], v[132:135], v[60:63]\n\t"
        "ds_read_b64_tr_b16 v[112:113], v199 offset:55392\n\t"
        "v_mfma_f32_16x16x32_bf16 v[64:67], v[152:155], v[120:123], v[64:67]\n\t"
        "ds_read_b64_tr_b16 v[114:115], v199 offset:55536\n\t"
        "v_mfma_f32_16x16x32_bf16 v[68:71], v[152:155], v[124:127], v[68:71]\n\t"
        "ds_read_b64_tr_b16 v[116:117], v200 offset:14336\n\t"
        "v_mfma_f32_16x16x32_bf16 v[72:75], v[128:131], v[156:159], v[72:75]\n\t"
        "ds_read_b64_tr_b16 v[118:119], v200 offset:14592\n\t"
        "v_mfma_f32_16x16x32_bf16 v[76:79], v[132:135], v[156:159], v[76:79]\n\t"
        "s_waitcnt lgkmcnt(0)\n\t"
        "v_mfma_f32_16x16x32_bf16 v[0:3], v[96:99], v[80:83], v[0:3]\n\t"
        "s_waitcnt vmcnt(0)\n\t"
        "ds_read_b128 v[120:123], v218 offset:49152\n\t"
        "v_mfma_f32_16x16x32_bf16 v[16:19], v[96:99], v[84:87], v[16:19]\n\t"
        "ds_read_b64_tr_b16 v[136:137], v198 offset:60000\n\t"
        "v_mfma_f32_16x16x32_bf16 v[32:35], v[96:99], v[88:91], v[32:35]\n\t"
        "ds_read_b64_tr_b16 v[138:139], v198 offset:60144\n\t"
        "v_mfma_f32_16x16x32_bf16 v[48:51], v[96:99], v[92:95], v[48:51]\n\t"
        "ds_read_b128 v[124:127], v218 offset:51200\n\t"
        "v_mfma_f32_16x16x32_bf16 v[4:7], v[100:103], v[80:83], v[4:7]\n\t"
        "ds_read_b128 v[128:131], v219 offset:49152\n\t"
        "ds_read_b128 v[132:135], v219 offset:51200\n\t"
        "v_mfma_f32_16x16x32_bf16 v[20:23], v[100:103], v[84:87], v[20:23]\n\t"
        "ds_read_b64_tr_b16 v[140:141], v198 offset:60032\n\t"
        "v_mfma_f32_16x16x32_bf16 v[36:39], v[100:103], v[88:91], v[36:39]\n\t"
        "ds_read_b64_tr_b16 v[142:143], v198 offset:60176\n\t"
        "v_mfma_f32_16x16x32_bf16 v[52:55], v[100:103], v[92:95], v[52:55]\n\t"
        "ds_read_b64_tr_b16 v[144:145], v198 offset:60064\n\t"
        "v_mfma_f32_16x16x32_bf16 v[8:11], v[104:107], v[80:83], v[8:11]\n\t"
        "ds_read_b64_tr_b16 v[146:147], v198 offset:60208\n\t"
        "ds_read_b64_tr_b16 v[148:149], v198 offset:60096\n\t"
        "v_mfma_f32_16x16x32_bf16 v[24:27], v[104:107], v[84:87], v[24:27]\n\t"
        "ds_read_b64_tr_b16 v[150:151], v198 offset:60240\n\t"
        "v_mfma_f32_16x16x32_bf16 v[40:43], v[104:107], v[88:91], v[40:43]\n\t"
        "ds_read_b64_tr_b16 v[152:153], v199 offset:60000\n\t"
        "v_mfma_f32_16x16x32_bf16 v[56:59], v[104:107], v[92:95], v[56:59]\n\t"
        "ds_read_b64_tr_b16 v[154:155], v199 offset:60144\n\t"
        "v_mfma_f32_16x16x32_bf16 v[12:15], v[108:111], v[80:83], v[12:15]\n\t"
        "ds_read_b64_tr_b16 v[156:157], v200 offset:15360\n\t"
        "ds_read_b64_tr_b16 v[158:159], v200 offset:15616\n\t"
        "v_mfma_f32_16x16x32_bf16 v[28:31], v[108:111], v[84:87], v[28:31]\n\t"
        "global_load_ushort v160, v204, %[wob]\n\t"
        "global_load_ushort v161, v204, %[wob] offset:1152\n\t"
        "v_mfma_f32_16x16x32_bf16 v[44:47], v[108:111], v[88:91], v[44:47]\n\t"
        "global_load_ushort v162, v205, %[wob]\n\t"
        "global_load_ushort v163, v205, %[wob] offset:1152\n\t"
        "v_mfma_f32_16x16x32_bf16 v[60:63], v[108:111], v[92:95], v[60:63]\n\t"
        "global_load_ushort v164, v206, %[wob]\n\t"
        "global_load_ushort v165, v206, %[wob] offset:1152\n\t"
        "v_mfma_f32_16x16x32_bf16 v[64:67], v[112:115], v[80:83], v[64:67]\n\t"
        "global_load_ushort v166, v207, %[wob]\n\t"
        "global_load_ushort v167, v207, %[wob] offset:1152\n\t"
        "s_mov_b64 exec, %[wom]\n\t"
        "global_load_ushort v168, v208, %[wob]\n\t"
        "global_load_ushort v169, v208, %[wob] offset:1152\n\t"
        "s_mov_b64 exec, -1\n\t"
        "v_mfma_f32_16x16x32_bf16 v[68:71], v[112:115], v[84:87], v[68:71]\n\t"
        "s_mov_b64 exec, %[wom]\n\t"
        "global_load_ushort v170, v209, %[wob]\n\t"
        "global_load_ushort v171, v209, %[wob] offset:1152\n\t"
        "s_mov_b64 exec, -1\n\t"
        "v_mfma_f32_16x16x32_bf16 v[72:75], v[88:91], v[116:119], v[72:75]\n\t"
        "s_mov_b64 exec, %[wom]\n\t"
        "global_load_ushort v172, v210, %[wob]\n\t"
        "global_load_ushort v173, v210, %[wob] offset:1152\n\t"
        "s_mov_b64 exec, -1\n\t"
        "v_mfma_f32_16x16x32_bf16 v[76:79], v[92:95], v[116:119], v[76:79]\n\t"
        "s_mov_b64 exec, %[wom]\n\t"
        "global_load_ushort v174, v211, %[wob]\n\t"
        "global_load_ushort v175, v211, %[wob] offset:1152\n\t"
        "s_mov_b64 exec, -1\n\t"
        "s_waitcnt lgkmcnt(0)\n\t"
        "s_barrier\n\t"
        "v_mfma_f32_16x16x32_bf16 v[0:3], v[136:139], v[120:123], v[0:3]\n\t"
        "ds_read_b128 v[80:83], v196 offset:0\n\t"
        "v_mfma_f32_16x16x32_bf16 v[16:19], v[136:139], v[124:127], v[16:19]\n\t"
        "ds_read_b64_tr_b16 v[96:97], v198 offset:0\n\t"
        "v_mfma_f32_16x16x32_bf16 v[32:35], v[136:139], v[128:131], v[32:35]\n\t"
        "ds_read_b64_tr_b16 v[98:99], v198 offset:144\n\t"
        "v_mfma_f32_16x16x32_bf16 v[48:51], v[136:139], v[132:135], v[48:51]\n\t"
        "ds_read_b128 v[84:87], v196 offset:2048\n\t"
        "v_mfma_f32_16x16x32_bf16 v[4:7], v[140:143], v[120:123], v[4:7]\n\t"
        "v_mfma_f32_16x16x32_bf16 v[20:23], v[140:143], v[124:127], v[20:23]\n\t"
        "ds_read_b128 v[88:91], v197 offset:0\n\t"
        "v_mfma_f32_16x16x32_bf16 v[36:39], v[140:143], v[128:131], v[36:39]\n\t"
        "ds_read_b128 v[92:95], v197 offset:2048\n\t"
        "v_mfma_f32_16x16x32_bf16 v[52:55], v[140:143], v[132:135], v[52:55]\n\t"
        "ds_read_b64_tr_b16 v[100:101], v198 offset:32\n\t"
        "v_mfma_f32_16x16x32_bf16 v[8:11], v[144:147], v[120:123], v[8:11]\n\t"
        "ds_read_b64_tr_b16 v[102:103], v198 offset:176\n\t"
        "v_mfma_f32_16x16x32_bf16 v[24:27], v[144:147], v[124:127], v[24:27]\n\t"
        "v_mfma_f32_16x16x32_bf16 v[40:43], v[144:147], v[128:131], v[40:43]\n\t"
        "ds_read_b64_tr_b16 v[104:105], v198 offset:64\n\t"
        "v_mfma_f32_16x16x32_bf16 v[56:59], v[144:147], v[132:135], v[56:59]\n\t"
        "ds_read_b64_tr_b16 v[106:107], v198 offset:208\n\t"
        "v_mfma_f32_16x16x32_bf16 v[12:15], v[148:151], v[120:123], v[12:15]\n\t"
        "ds_read_b64_tr_b16 v[108:109], v198 offset:96\n\t"
        "v_mfma_f32_16x16x32_bf16 v[28:31], v[148:151], v[124:127], v[28:31]\n\t"
        "ds_read_b64_tr_b16 v[110:111], v198 offset:240\n\t"
        "v_mfma_f32_16x16x32_bf16 v[44:47], v[148:151], v[128:131], v[44:47]\n\t"
        "v_mfma_f32_16x16x32_bf16 v[60:63], v[148:151], v[132:135], v[60:63]\n\t"
        "ds_read_b64_tr_b16 v[112:113], v199 offset:0\n\t"
        "v_mfma_f32_16x16x32_bf16 v[64:67], v[152:155], v[120:123], v[64:67]\n\t"
        "ds_read_b64_tr_b16 v[114:115], v199 offset:144\n\t"
        "v_mfma_f32_16x16x32_bf16 v[68:71], v[152:155], v[124:127], v[68:71]\n\t"
        "ds_read_b64_tr_b16 v[116:117], v200 offset:16384\n\t"
        "v_mfma_f32_16x16x32_bf16 v[72:75], v[128:131], v[156:159], v[72:75]\n\t"
        "ds_read_b64_tr_b16 v[118:119], v200 offset:16640\n\t"
        "v_mfma_f32_16x16x32_bf16 v[76:79], v[132:135], v[156:159], v[76:79]\n\t"
        "s_waitcnt lgkmcnt(0)\n\t"
        "v_mfma_f32_16x16x32_bf16 v[0:3], v[96:99], v[80:83], v[0:3]\n\t"
        "ds_read_b128 v[120:123], v218 offset:0\n\t"
        "v_mfma_f32_16x16x32_bf16 v[16:19], v[96:99], v[84:87], v[16:19]\n\t"
        "ds_read_b64_tr_b16 v[136:137], v198 offset:4608\n\t"
        "v_mfma_f32_16x16x32_bf16 v[32:35], v[96:99], v[88:91], v[32:35]\n\t"
        "ds_read_b64_tr_b16 v[138:139], v198 offset:4752\n\t"
        "v_mfma_f32_16x16x32_bf16 v[48:51], v[96:99], v[92:95], v[48:51]\n\t"
        "ds_read_b128 v[124:127], v218 offset:2048\n\t"
        "v_mfma_f32_16x16x32_bf16 v[4:7], v[100:103], v[80:83], v[4:7]\n\t"
        "ds_read_b128 v[128:131], v219 offset:0\n\t"
        "v_mfma_f32_16x16x32_bf16 v[20:23], v[100:103], v[84:87], v[20:23]\n\t"
        "ds_read_b128 v[132:135], v219 offset:2048\n\t"
        "v_mfma_f32_16x16x32_bf16 v[36:39], v[100:103], v[88:91], v[36:39]\n\t"
        "v_mfma_f32_16x16x32_bf16 v[52:55], v[100:103], v[92:95], v[52:55]\n\t"
        "ds_read_b64_tr_b16 v[140:141], v198 offset:4640\n\t"
        "v_mfma_f32_16x16x32_bf16 v[8:11], v[104:107], v[80:83], v[8:11]\n\t"
        "ds_read_b64_tr_b16 v[142:143], v198 offset:4784\n\t"
        "v_mfma_f32_16x16x32_bf16 v[24:27], v[104:107], v[84:87], v[24:27]\n\t"
        "ds_read_b64_tr_b16 v[144:145], v198 offset:4672\n\t"
        "v_mfma_f32_16x16x32_bf16 v[40:43], v[104:107], v[88:91], v[40:43]\n\t"
        "ds_read_b64_tr_b16 v[146:147], v198 offset:4816\n\t"
        "v_mfma_f32_16x16x32_bf16 v[56:59], v[104:107], v[92:95], v[56:59]\n\t"
        "ds_read_b64_tr_b16 v[148:149], v198 offset:4704\n\t"
        "v_mfma_f32_16x16x32_bf16 v[12:15], v[108:111], v[80:83], v[12:15]\n\t"
        "ds_read_b64_tr_b16 v[150:151], v198 offset:4848\n\t"
        "v_mfma_f32_16x16x32_bf16 v[28:31], v[108:111], v[84:87], v[28:31]\n\t"
        "v_mfma_f32_16x16x32_bf16 v[44:47], v[108:111], v[88:91], v[44:47]\n\t"
        "ds_read_b64_tr_b16 v[152:153], v199 offset:4608\n\t"
        "v_mfma_f32_16x16x32_bf16 v[60:63], v[108:111], v[92:95], v[60:63]\n\t"
        "ds_read_b64_tr_b16 v[154:155], v199 offset:4752\n\t"
        "v_mfma_f32_16x16x32_bf16 v[64:67], v[112:115], v[80:83], v[64:67]\n\t"
        "ds_read_b64_tr_b16 v[156:157], v200 offset:17408\n\t"
        "v_mfma_f32_16x16x32_bf16 v[68:71], v[112:115], v[84:87], v[68:71]\n\t"
        "ds_read_b64_tr_b16 v[158:159], v200 offset:17664\n\t"
        "v_mfma_f32_16x16x32_bf16 v[72:75], v[88:91], v[116:119], v[72:75]\n\t"
        "s_mov_b32 exec_lo, 0x0011ffff\n\t"
        "s_mov_b32 exec_hi, 0x0011ffff\n\t"
        "ds_read_u16 v176, v245 offset:0\n\t"
        "ds_read_u16 v177, v245 offset:36\n\t"
        "ds_read_u16 v178, v245 offset:72\n\t"
        "ds_read_u16 v179, v245 offset:108\n\t"
        "ds_read_u16 v180, v245 offset:144\n\t"
        "ds_read_u16 v181, v245 offset:180\n\t"
        "ds_read_u16 v182, v245 offset:216\n\t"
        "ds_read_u16 v183, v245 offset:252\n\t"
        "s_mov_b32 exec_hi, 0\n\t"
        "ds_read_u16 v192, v245 offset:576\n\t"
        "ds_read_u16 v193, v245 offset:612\n\t"
        "s_mov_b64 exec, -1\n\t"
        "v_mfma_f32_16x16x32_bf16 v[76:79], v[92:95], v[116:119], v[76:79]\n\t"
        "s_waitcnt lgkmcnt(0)\n\t"
        "v_mfma_f32_16x16x32_bf16 v[72:75], v[128:131], v[156:159], v[72:75]\n\t"
        "v_lshl_or_b32 v184, v177, 16, v176\n\t"
        "v_lshl_or_b32 v185, v179, 16, v178\n\t"
        "v_mfma_f32_16x16x32_bf16 v[76:79], v[132:135], v[156:159], v[76:79]\n\t"
        "v_lshl_or_b32 v186, v181, 16, v180\n\t"
        "v_lshl_or_b32 v187, v183, 16, v182\n\t"
        "v_mfma_f32_16x16x32_bf16 v[64:67], v[152:155], v[120:123], v[64:67]\n\t"
        "v_lshl_or_b32 v188, v193, 16, v192\n\t"
        "s_nop 0\n\t"
        "v_mfma_f32_16x16x32_bf16 v[68:71], v[152:155], v[124:127], v[68:71]\n\t"
        "s_nop 1\n\t"
        "v_mfma_f32_16x16x32_bf16 v[0:3], v[136:139], v[120:123], v[0:3]\n\t"
        "s_nop 1\n\t"
        "v_mfma_f32_16x16x32_bf16 v[16:19], v[136:139], v[124:127], v[16:19]\n\t"
        "v_pk_mul_f32 v[64:65], v[64:65], v[214:215] op_sel_hi:[1,0]\n\t"
        "v_pk_mul_f32 v[66:67], v[66:67], v[214:215] op_sel_hi:[1,0]\n\t"
        "v_cvt_pk_bf16_f32 v80, v64, v65\n\t"
        "v_cvt_pk_bf16_f32 v81, v66, v67\n\t"
        "ds_write_b64 v215, v[80:81] offset:0\n\t"
        "v_mfma_f32_16x16x32_bf16 v[32:35], v[136:139], v[128:131], v[32:35]\n\t"
        "v_pk_mul_f32 v[68:69], v[68:69], v[214:215] op_sel_hi:[1,0]\n\t"
        "v_pk_mul_f32 v[70:71], v[70:71], v[214:215] op_sel_hi:[1,0]\n\t"
        "v_cvt_pk_bf16_f32 v82, v68, v69\n\t"
        "v_cvt_pk_bf16_f32 v83, v70, v71\n\t"
        "ds_write_b64 v215, v[82:83] offset:4736\n\t"
        "v_mfma_f32_16x16x32_bf16 v[48:51], v[136:139], v[132:135], v[48:51]\n\t"
        "v_pk_mul_f32 v[0:1], v[0:1], s[44:45]\n\t"
        "v_pk_mul_f32 v[2:3], v[2:3], s[44:45]\n\t"
        "v_cvt_pk_bf16_f32 v84, v0, v1\n\t"
        "v_cvt_pk_bf16_f32 v85, v2, v3\n\t"
        "ds_write_b64 v212, v[84:85] offset:0\n\t"
        "v_mfma_f32_16x16x32_bf16 v[4:7], v[140:143], v[120:123], v[4:7]\n\t"
        "v_pk_mul_f32 v[16:17], v[16:17], s[44:45]\n\t"
        "v_pk_mul_f32 v[18:19], v[18:19], s[44:45]\n\t"
        "v_cvt_pk_bf16_f32 v86, v16, v17\n\t"
        "v_cvt_pk_bf16_f32 v87, v18, v19\n\t"
        "ds_write_b64 v212, v[86:87] offset:4736\n\t"
        "v_mfma_f32_16x16x32_bf16 v[20:23], v[140:143], v[124:127], v[20:23]\n\t"
        "v_pk_mul_f32 v[32:33], v[32:33], s[44:45]\n\t"
        "v_pk_mul_f32 v[34:35], v[34:35], s[44:45]\n\t"
        "v_cvt_pk_bf16_f32 v88, v32, v33\n\t"
        "v_cvt_pk_bf16_f32 v89, v34, v35\n\t"
        "ds_write_b64 v213, v[88:89] offset:9472\n\t"
        "v_mfma_f32_16x16x32_bf16 v[36:39], v[140:143], v[128:131], v[36:39]\n\t"
        "v_pk_mul_f32 v[48:49], v[48:49], s[44:45]\n\t"
        "v_pk_mul_f32 v[50:51], v[50:51], s[44:45]\n\t"
        "v_cvt_pk_bf16_f32 v90, v48, v49\n\t"
        "v_cvt_pk_bf16_f32 v91, v50, v51\n\t"
        "ds_write_b64 v213, v[90:91] offset:14208\n\t"
        "v_mfma_f32_16x16x32_bf16 v[52:55], v[140:143], v[132:135], v[52:55]\n\t"
        "v_pk_mul_f32 v[4:5], v[4:5], s[44:45]\n\t"
        "v_pk_mul_f32 v[6:7], v[6:7], s[44:45]\n\t"
        "v_cvt_pk_bf16_f32 v92, v4, v5\n\t"
        "v_cvt_pk_bf16_f32 v93, v6, v7\n\t"
        "ds_write_b64 v212, v[92:93] offset:32\n\t"
        "v_mfma_f32_16x16x32_bf16 v[8:11], v[144:147], v[120:123], v[8:11]\n\t"
        "v_pk_mul_f32 v[20:21], v[20:21], s[44:45]\n\t"
        "v_pk_mul_f32 v[22:23], v[22:23], s[44:45]\n\t"
        "v_cvt_pk_bf16_f32 v94, v20, v21\n\t"
        "v_cvt_pk_bf16_f32 v95, v22, v23\n\t"
        "ds_write_b64 v212, v[94:95] offset:4768\n\t"
        "v_mfma_f32_16x16x32_bf16 v[24:27], v[144:147], v[124:127], v[24:27]\n\t"
        "v_pk_mul_f32 v[36:37], v[36:37], s[44:45]\n\t"
        "v_pk_mul_f32 v[38:39], v[38:39], s[44:45]\n\t"
        "v_cvt_pk_bf16_f32 v80, v36, v37\n\t"
        "v_cvt_pk_bf16_f32 v81, v38, v39\n\t"
        "ds_write_b64 v213, v[80:81] offset:9504\n\t"
        "v_mfma_f32_16x16x32_bf16 v[40:43], v[144:147], v[128:131], v[40:43]\n\t"
        "v_pk_mul_f32 v[52:53], v[52:53], s[44:45]\n\t"
        "v_pk_mul_f32 v[54:55], v[54:55], s[44:45]\n\t"
        "v_cvt_pk_bf16_f32 v82, v52, v53\n\t"
        "v_cvt_pk_bf16_f32 v83, v54, v55\n\t"
        "ds_write_b64 v213, v[82:83] offset:14240\n\t"
        "v_mfma_f32_16x16x32_bf16 v[56:59], v[144:147], v[132:135], v[56:59]\n\t"
        "v_pk_mul_f32 v[8:9], v[8:9], s[44:45]\n\t"
        "v_pk_mul_f32 v[10:11], v[10:11], s[44:45]\n\t"
        "v_cvt_pk_bf16_f32 v84, v8, v9\n\t"
        "v_cvt_pk_bf16_f32 v85, v10, v11\n\t"
        "ds_write_b64 v212, v[84:85] offset:64\n\t"
        "v_mfma_f32_16x16x32_bf16 v[12:15], v[148:151], v[120:123], v[12:15]\n\t"
        "v_pk_mul_f32 v[24:25], v[24:25], s[44:45]\n\t"
        "v_pk_mul_f32 v[26:27], v[26:27], s[44:45]\n\t"
        "v_cvt_pk_bf16_f32 v86, v24, v25\n\t"
        "v_cvt_pk_bf16_f32 v87, v26, v27\n\t"
        "ds_write_b64 v212, v[86:87] offset:4800\n\t"
        "v_mfma_f32_16x16x32_bf16 v[28:31], v[148:151], v[124:127], v[28:31]\n\t"
        "v_pk_mul_f32 v[40:41], v[40:41], s[44:45]\n\t"
        "v_pk_mul_f32 v[42:43], v[42:43], s[44:45]\n\t"
        "v_cvt_pk_bf16_f32 v88, v40, v41\n\t"
        "v_cvt_pk_bf16_f32 v89, v42, v43\n\t"
        "ds_write_b64 v213, v[88:89] offset:9536\n\t"
        "v_mfma_f32_16x16x32_bf16 v[44:47], v[148:151], v[128:131], v[44:47]\n\t"
        "v_pk_mul_f32 v[56:57], v[56:57], s[44:45]\n\t"
        "v_pk_mul_f32 v[58:59], v[58:59], s[44:45]\n\t"
        "v_cvt_pk_bf16_f32 v90, v56, v57\n\t"
        "v_cvt_pk_bf16_f32 v91, v58, v59\n\t"
        "ds_write_b64 v213, v[90:91] offset:14272\n\t"
        "v_mfma_f32_16x16x32_bf16 v[60:63], v[148:151], v[132:135], v[60:63]\n\t"
        "v_pk_mul_f32 v[12:13], v[12:13], s[44:45]\n\t"
        "v_pk_mul_f32 v[14:15], v[14:15], s[44:45]\n\t"
        "v_cvt_pk_bf16_f32 v92, v12, v13\n\t"
        "v_cvt_pk_bf16_f32 v93, v14, v15\n\t"
        "ds_write_b64 v212, v[92:93] offset:96\n\t"
        "v_pk_mul_f32 v[28:29], v[28:29], s[44:45]\n\t"
        "v_pk_mul_f32 v[30:31], v[30:31], s[44:45]\n\t"
        "v_cvt_pk_bf16_f32 v94, v28, v29\n\t"
        "v_cvt_pk_bf16_f32 v95, v30, v31\n\t"
        "ds_write_b64 v212, v[94:95] offset:4832\n\t"
        "v_pk_mul_f32 v[44:45], v[44:45], s[44:45]\n\t"
        "v_pk_mul_f32 v[46:47], v[46:47], s[44:45]\n\t"
        "v_cvt_pk_bf16_f32 v80, v44, v45\n\t"
        "v_cvt_pk_bf16_f32 v81, v46, v47\n\t"
        "ds_write_b64 v213, v[80:81] offset:9568\n\t"
        "v_pk_mul_f32 v[60:61], v[60:61], s[44:45]\n\t"
        "v_pk_mul_f32 v[62:63], v[62:63], s[44:45]\n\t"
        "v_cvt_pk_bf16_f32 v82, v60, v61\n\t"
        "v_cvt_pk_bf16_f32 v83, v62, v63\n\t"
        "ds_write_b64 v213, v[82:83] offset:14304\n\t"
        "v_cvt_pk_bf16_f32 v100, v72, v73\n\t"
        "v_cvt_pk_bf16_f32 v101, v74, v75\n\t"
        "v_cvt_pk_bf16_f32 v102, v76, v77\n\t"
        "v_cvt_pk_bf16_f32 v103, v78, v79\n\t"
        "global_store_dwordx2 v201, v[100:101], %[wsb] offset:0 sc1\n\t"
        "global_store_dwordx2 v201, v[102:103], %[wsb] offset:608 sc1\n\t"
        "s_branch 69f\n\t"
        "63:\n\t"
        "v_xor_b32 v218, 64, v196\n\t"
        "v_xor_b32 v219, 64, v197\n\t"
        "s_mov_b32 s44, %[sc0]\n\t"
        "s_mov_b32 s45, %[sc0]\n\t"
        "s_and_b32 s46, %[w], 3\n\t"
        "s_lshr_b32 s47, %[w], 2\n\t"
        "v_and_b32 v240, 31, v244\n\t"
        "v_lshrrev_b32 v241, 5, v244\n\t"
        "s_mul_i32 s48, s47, 18944\n\t"
        "s_mul_i32 s49, s46, 36\n\t"
        "s_add_u32 s48, s48, s49\n\t"
        "s_add_u32 s48, s48, 16384\n\t"
        "v_mul_u32_u24 v242, 296, v240\n\t"
        "v_lshlrev_b32 v243, 4, v241\n\t"
        "v_add3_u32 v228, v242, v243, s48\n\t"
        "v_add_u32 v230, s48, v242\n\t"
        "v_add_u32 v229, 9472, v228\n\t"
        "s_mul_i32 s48, s47, 22528\n\t"
        "s_mul_i32 s49, s46, 80\n\t"
        "s_add_u32 s48, s48, s49\n\t"
        "s_add_u32 s48, s48, 65536\n\t"
        "v_mul_u32_u24 v242, 352, v240\n\t"
        "v_add3_u32 v231, v242, v243, s48\n\t"
        "v_mul_u32_u24 v243, 12, v241\n\t"
        "v_sub_u32 v232, v231, v243\n\t"
        "v_add_u32 v232, 64, v232\n\t"
        "v_lshlrev_b32 v233, 4, v244\n\t"
        "v_lshlrev_b32 v234, 2, v244\n\t"
        "v_bfe_u32 v240, v244, 2, 2\n\t"
        "v_and_b32 v241, 3, v244\n\t"
        "v_lshrrev_b32 v242, 4, v244\n\t"
        "v_lshl_add_u32 v240, v242, 2, v240\n\t"
        "v_lshlrev_b32 v241, 3, v241\n\t"
        "v_mul_u32_u24 v243, 352, v240\n\t"
        "v_add3_u32 v235, v243, v241, s48\n\t"
        "s_mul_i32 s48, s47, 18944\n\t"
        "s_mul_i32 s49, s46, 36\n\t"
        "s_add_u32 s48, s48, s49\n\t"
        "s_and_b32 s49, s46, 1\n\t"
        "s_lshl_b32 s49, s49, 2\n\t"
        "s_sub_u32 s48, s48, s49\n\t"
        "s_add_u32 s48, s48, 16528\n\t"
        "v_mul_u32_u24 v243, 296, v240\n\t"
        "v_add3_u32 v236, v243, v241, s48\n\t"
        "s_lshr_b32 s49, s49, 1\n\t"
        "s_sub_u32 s49, 2, s49\n\t"
        "v_and_b32 v243, 15, v244\n\t"
        "v_add_u32 v243, s49, v243\n\t"
        "v_lshlrev_b32 v241, 6, v243\n\t"
        "v_lshl_add_u32 v237, v242, 4, v241\n\t"
        "v_lshlrev_b32 v238, 4, v243\n\t"
        "v_bfe_u32 v240, v244, 3, 2\n\t"
        "v_and_b32 v241, 3, v244\n\t"
        "v_lshl_add_u32 v240, v240, 2, v241\n\t"
        "v_bfe_u32 v241, v244, 2, 1\n\t"
        "v_mul_u32_u24 v241, 9, v241\n\t"
        "v_add_u32 v241, v241, v240\n\t"
        "v_lshrrev_b32 v242, 5, v244\n\t"
        "v_mul_u32_u24 v242, 288, v242\n\t"
        "v_lshl_add_u32 v241, v241, 1, v242\n\t"
        "s_mul_i32 s48, s46, 648\n\t"
        "s_add_u32 s48, s48, 157824\n\t"
        "v_add_u32 v245, s48, v241\n\t"
        "v_mov_b32 v246, 0\n\t"
        "v_mov_b32 v247, 0\n\t"
        "v_mov_b32 v248, 0\n\t"
        "v_mov_b32 v249, 0\n\t"
        "v_mov_b32 v250, 0\n\t"
        "v_mov_b32 v251, 0\n\t"
        "v_mov_b32 v252, 0\n\t"
        "v_mov_b32 v253, 0\n\t"
        "v_mov_b32 v176, 0\n\t"
        "v_mov_b32 v177, 0\n\t"
        "v_mov_b32 v178, 0\n\t"
        "v_mov_b32 v179, 0\n\t"
        "v_mov_b32 v180, 0\n\t"
        "v_mov_b32 v181, 0\n\t"
        "v_mov_b32 v182, 0\n\t"
        "v_mov_b32 v183, 0\n\t"
        "v_mov_b32 v192, 0\n\t"
        "v_mov_b32 v193, 0\n\t"
        "v_mov_b32 v189, 0\n\t"
        "v_mov_b32 v190, 0\n\t"
        "v_mov_b32 v191, 0\n\t"
        "s_mov_b32 s52, %[blk]\n\t"
        "s_lshl_b32 s54, %[w], 8\n\t"
        "s_add_u32 s54, s54, 139392\n\t"
        "s_add_u32 s55, s54, 8192\n\t"
        "s_mov_b32 s50, 0x55555555\n\t"
        "s_mov_b32 s51, s50\n\t"
        "s_waitcnt vmcnt(0)\n\t"
        "s_barrier\n\t"
        "ds_read_b128 v[80:83], v196 offset:0\n\t"
        "ds_read_b64_tr_b16 v[96:97], v198 offset:0\n\t"
        "ds_read_b64_tr_b16 v[98:99], v198 offset:144\n\t"
        "ds_read_b128 v[84:87], v196 offset:2048\n\t"
        "ds_read_b128 v[88:91], v197 offset:0\n\t"
        "ds_read_b128 v[92:95], v197 offset:2048\n\t"
        "ds_read_b64_tr_b16 v[100:101], v198 offset:32\n\t"
        "ds_read_b64_tr_b16 v[102:103], v198 offset:176\n\t"
        "ds_read_b64_tr_b16 v[104:105], v198 offset:64\n\t"
        "ds_read_b64_tr_b16 v[106:107], v198 offset:208\n\t"
        "ds_read_b64_tr_b16 v[108:109], v198 offset:96\n\t"
        "ds_read_b64_tr_b16 v[110:111], v198 offset:240\n\t"
        "ds_read_b64_tr_b16 v[112:113], v199 offset:0\n\t"
        "ds_read_b64_tr_b16 v[114:115], v199 offset:144\n\t"
        "ds_read_b64_tr_b16 v[116:117], v200 offset:0\n\t"
        "ds_read_b64_tr_b16 v[118:119], v200 offset:64\n\t"
        "s_waitcnt lgkmcnt(0)\n\t"
        "v_mfma_f32_16x16x32_bf16 v[0:3], v[96:99], v[80:83], 0\n\t"
        "ds_read_b128 v[120:123], v218 offset:0\n\t"
        "v_mfma_f32_16x16x32_bf16 v[16:19], v[96:99], v[84:87], 0\n\t"
        "ds_read_b64_tr_b16 v[136:137], v198 offset:4608\n\t"
        "v_mfma_f32_16x16x32_bf16 v[32:35], v[96:99], v[88:91], 0\n\t"
        "s_and_b32 s53, s52, 0xff\n\t"
        "s_mul_i32 s53, s53, 1152\n\t"
        "s_add_u32 m0, s54, 1024\n\t"
        "v_add_co_u32 v224, vcc, s53, v220\n\t"
        "v_addc_co_u32 v225, vcc, 0, v221, vcc\n\t"
        "global_load_lds_dword v[224:225], off\n\t"
        "s_and_b32 s53, s52, 0xff\n\t"
        "s_mul_i32 s53, s53, 64\n\t"
        "s_add_u32 m0, s55, 1024\n\t"
        "s_mov_b64 exec, s[50:51]\n\t"
        "v_add_co_u32 v224, vcc, s53, v222\n\t"
        "v_addc_co_u32 v225, vcc, 0, v223, vcc\n\t"
        "global_load_lds_ushort v[224:225], off\n\t"
        "s_mov_b64 exec, -1\n\t"
        "s_lshr_b32 s52, s52, 8\n\t"
        "v_mfma_f32_16x16x32_bf16 v[48:51], v[96:99], v[92:95], 0\n\t"
        "ds_read_b64_tr_b16 v[138:139], v198 offset:4752\n\t"
        "v_mfma_f32_16x16x32_bf16 v[4:7], v[100:103], v[80:83], 0\n\t"
        "ds_read_b128 v[124:127], v218 offset:2048\n\t"
        "v_mfma_f32_16x16x32_bf16 v[20:23], v[100:103], v[84:87], 0\n\t"
        "ds_read_b128 v[128:131], v219 offset:0\n\t"
        "v_mfma_f32_16x16x32_bf16 v[36:39], v[100:103], v[88:91], 0\n\t"
        "v_mfma_f32_16x16x32_bf16 v[52:55], v[100:103], v[92:95], 0\n\t"
        "ds_read_b128 v[132:135], v219 offset:2048\n\t"
        "v_mfma_f32_16x16x32_bf16 v[8:11], v[104:107], v[80:83], 0\n\t"
        "ds_read_b64_tr_b16 v[140:141], v198 offset:4640\n\t"
        "v_mfma_f32_16x16x32_bf16 v[24:27], v[104:107], v[84:87], 0\n\t"
        "ds_read_b64_tr_b16 v[142:143], v198 offset:4784\n\t"
        "v_mfma_f32_16x16x32_bf16 v[40:43], v[104:107], v[88:91], 0\n\t"
        "ds_read_b64_tr_b16 v[144:145], v198 offset:4672\n\t"
        "v_mfma_f32_16x16x32_bf16 v[56:59], v[104:107], v[92:95], 0\n\t"
        "ds_read_b64_tr_b16 v[146:147], v198 offset:4816\n\t"
        "v_mfma_f32_16x16x32_bf16 v[12:15], v[108:111], v[80:83], 0\n\t"
        "ds_read_b64_tr_b16 v[148:149], v198 offset:4704\n\t"
        "v_mfma_f32_16x16x32_bf16 v[28:31], v[108:111], v[84:87], 0\n\t"
        "v_mfma_f32_16x16x32_bf16 v[44:47], v[108:111], v[88:91], 0\n\t"
        "ds_read_b64_tr_b16 v[150:151], v198 offset:4848\n\t"
        "v_mfma_f32_16x16x32_bf16 v[60:63], v[108:111], v[92:95], 0\n\t"
        "ds_read_b64_tr_b16 v[152:153], v199 offset:4608\n\t"
        "v_mfma_f32_16x16x32_bf16 v[64:67], v[112:115], v[80:83], 0\n\t"
        "ds_read_b64_tr_b16 v[154:155], v199 offset:4752\n\t"
        "v_mfma_f32_16x16x32_bf16 v[68:71], v[112:115], v[84:87], 0\n\t"
        "ds_read_b64_tr_b16 v[156:157], v200 offset:256\n\t"
        "v_mfma_f32_16x16x32_bf16 v[72:75], v[116:119], v[88:91], 0\n\t"
        "ds_read_b64_tr_b16 v[158:159], v200 offset:320\n\t"
        "v_mfma_f32_16x16x32_bf16 v[76:79], v[116:119], v[92:95], 0\n\t"
        "s_waitcnt lgkmcnt(0)\n\t"
        "s_barrier\n\t"
        "v_mfma_f32_16x16x32_bf16 v[0:3], v[136:139], v[120:123], v[0:3]\n\t"
        "ds_read_b128 v[80:83], v196 offset:16384\n\t"
        "v_mfma_f32_16x16x32_bf16 v[16:19], v[136:139], v[124:127], v[16:19]\n\t"
        "ds_read_b64_tr_b16 v[96:97], v198 offset:18464\n\t"
        "v_mfma_f32_16x16x32_bf16 v[32:35], v[136:139], v[128:131], v[32:35]\n\t"
        "ds_read_b64_tr_b16 v[98:99], v198 offset:18608\n\t"
        "v_mfma_f32_16x16x32_bf16 v[48:51], v[136:139], v[132:135], v[48:51]\n\t"
        "ds_read_b128 v[84:87], v196 offset:18432\n\t"
        "v_mfma_f32_16x16x32_bf16 v[4:7], v[140:143], v[120:123], v[4:7]\n\t"
        "v_mfma_f32_16x16x32_bf16 v[20:23], v[140:143], v[124:127], v[20:23]\n\t"
        "ds_read_b128 v[88:91], v197 offset:16384\n\t"
        "v_mfma_f32_16x16x32_bf16 v[36:39], v[140:143], v[128:131], v[36:39]\n\t"
        "ds_read_b128 v[92:95], v197 offset:18432\n\t"
        "v_mfma_f32_16x16x32_bf16 v[52:55], v[140:143], v[132:135], v[52:55]\n\t"
        "ds_read_b64_tr_b16 v[100:101], v198 offset:18496\n\t"
        "v_mfma_f32_16x16x32_bf16 v[8:11], v[144:147], v[120:123], v[8:11]\n\t"
        "ds_read_b64_tr_b16 v[102:103], v198 offset:18640\n\t"
        "v_mfma_f32_16x16x32_bf16 v[24:27], v[144:147], v[124:127], v[24:27]\n\t"
        "v_mfma_f32_16x16x32_bf16 v[40:43], v[144:147], v[128:131], v[40:43]\n\t"
        "ds_read_b64_tr_b16 v[104:105], v198 offset:18528\n\t"
        "v_mfma_f32_16x16x32_bf16 v[56:59], v[144:147], v[132:135], v[56:59]\n\t"
        "ds_read_b64_tr_b16 v[106:107], v198 offset:18672\n\t"
        "v_mfma_f32_16x16x32_bf16 v[12:15], v[148:151], v[120:123], v[12:15]\n\t"
        "ds_read_b64_tr_b16 v[108:109], v198 offset:18560\n\t"
        "v_mfma_f32_16x16x32_bf16 v[28:31], v[148:151], v[124:127], v[28:31]\n\t"
        "ds_read_b64_tr_b16 v[110:111], v198 offset:18704\n\t"
        "v_mfma_f32_16x16x32_bf16 v[44:47], v[148:151], v[128:131], v[44:47]\n\t"
        "v_mfma_f32_16x16x32_bf16 v[60:63], v[148:151], v[132:135], v[60:63]\n\t"
        "ds_read_b64_tr_b16 v[112:113], v199 offset:18464\n\t"
        "v_mfma_f32_16x16x32_bf16 v[64:67], v[152:155], v[120:123], v[64:67]\n\t"
        "ds_read_b64_tr_b16 v[114:115], v199 offset:18608\n\t"
        "v_mfma_f32_16x16x32_bf16 v[68:71], v[152:155], v[124:127], v[68:71]\n\t"
        "ds_read_b64_tr_b16 v[116:117], v200 offset:512\n\t"
        "v_mfma_f32_16x16x32_bf16 v[72:75], v[156:159], v[128:131], v[72:75]\n\t"
        "ds_read_b64_tr_b16 v[118:119], v200 offset:576\n\t"
        "v_mfma_f32_16x16x32_bf16 v[76:79], v[156:159], v[132:135], v[76:79]\n\t"
        "s_waitcnt lgkmcnt(0)\n\t"
        "v_mfma_f32_16x16x32_bf16 v[0:3], v[96:99], v[80:83], v[0:3]\n\t"
        "ds_read_b128 v[120:123], v218 offset:16384\n\t"
        "v_mfma_f32_16x16x32_bf16 v[16:19], v[96:99], v[84:87], v[16:19]\n\t"
        "ds_read_b64_tr_b16 v[136:137], v198 offset:23072\n\t"
        "v_mfma_f32_16x16x32_bf16 v[32:35], v[96:99], v[88:91], v[32:35]\n\t"
        "ds_read_b64_tr_b16 v[138:139], v198 offset:23216\n\t"
        "v_mfma_f32_16x16x32_bf16 v[48:51], v[96:99], v[92:95], v[48:51]\n\t"
        "ds_read_b128 v[124:127], v218 offset:18432\n\t"
        "v_mfma_f32_16x16x32_bf16 v[4:7], v[100:103], v[80:83], v[4:7]\n\t"
        "ds_read_b128 v[128:131], v219 offset:16384\n\t"
        "v_mfma_f32_16x16x32_bf16 v[20:23], v[100:103], v[84:87], v[20:23]\n\t"
        "ds_read_b128 v[132:135], v219 offset:18432\n\t"
        "v_mfma_f32_16x16x32_bf16 v[36:39], v[100:103], v[88:91], v[36:39]\n\t"
        "v_mfma_f32_16x16x32_bf16 v[52:55], v[100:103], v[92:95], v[52:55]\n\t"
        "ds_read_b64_tr_b16 v[140:141], v198 offset:23104\n\t"
        "v_mfma_f32_16x16x32_bf16 v[8:11], v[104:107], v[80:83], v[8:11]\n\t"
        "ds_read_b64_tr_b16 v[142:143], v198 offset:23248\n\t"
        "v_mfma_f32_16x16x32_bf16 v[24:27], v[104:107], v[84:87], v[24:27]\n\t"
        "ds_read_b64_tr_b16 v[144:145], v198 offset:23136\n\t"
        "v_mfma_f32_16x16x32_bf16 v[40:43], v[104:107], v[88:91], v[40:43]\n\t"
        "ds_read_b64_tr_b16 v[146:147], v198 offset:23280\n\t"
        "v_mfma_f32_16x16x32_bf16 v[56:59], v[104:107], v[92:95], v[56:59]\n\t"
        "ds_read_b64_tr_b16 v[148:149], v198 offset:23168\n\t"
        "v_mfma_f32_16x16x32_bf16 v[12:15], v[108:111], v[80:83], v[12:15]\n\t"
        "ds_read_b64_tr_b16 v[150:151], v198 offset:23312\n\t"
        "v_mfma_f32_16x16x32_bf16 v[28:31], v[108:111], v[84:87], v[28:31]\n\t"
        "v_mfma_f32_16x16x32_bf16 v[44:47], v[108:111], v[88:91], v[44:47]\n\t"
        "ds_read_b64_tr_b16 v[152:153], v199 offset:23072\n\t"
        "v_mfma_f32_16x16x32_bf16 v[60:63], v[108:111], v[92:95], v[60:63]\n\t"
        "ds_read_b64_tr_b16 v[154:155], v199 offset:23216\n\t"
        "v_mfma_f32_16x16x32_bf16 v[64:67], v[112:115], v[80:83], v[64:67]\n\t"
        "ds_read_b64_tr_b16 v[156:157], v200 offset:768\n\t"
        "v_mfma_f32_16x16x32_bf16 v[68:71], v[112:115], v[84:87], v[68:71]\n\t"
        "ds_read_b64_tr_b16 v[158:159], v200 offset:832\n\t"
        "v_mfma_f32_16x16x32_bf16 v[72:75], v[116:119], v[88:91], v[72:75]\n\t"
        "s_waitcnt vmcnt(0)\n\t"
        "v_mfma_f32_16x16x32_bf16 v[76:79], v[116:119], v[92:95], v[76:79]\n\t"
        "s_waitcnt lgkmcnt(0)\n\t"
        "s_barrier\n\t"
        "v_mfma_f32_16x16x32_bf16 v[0:3], v[136:139], v[120:123], v[0:3]\n\t"
        "ds_read_b128 v[80:83], v196 offset:32768\n\t"
        "v_mfma_f32_16x16x32_bf16 v[16:19], v[136:139], v[124:127], v[16:19]\n\t"
        "ds_read_b64_tr_b16 v[96:97], v198 offset:36928\n\t"
        "v_mfma_f32_16x16x32_bf16 v[32:35], v[136:139], v[128:131], v[32:35]\n\t"
        "ds_read_b64_tr_b16 v[98:99], v198 offset:37072\n\t"
        "v_mfma_f32_16x16x32_bf16 v[48:51], v[136:139], v[132:135], v[48:51]\n\t"
        "ds_read_b128 v[84:87], v196 offset:34816\n\t"
        "v_mfma_f32_16x16x32_bf16 v[4:7], v[140:143], v[120:123], v[4:7]\n\t"
        "v_mfma_f32_16x16x32_bf16 v[20:23], v[140:143], v[124:127], v[20:23]\n\t"
        "ds_read_b128 v[88:91], v197 offset:32768\n\t"
        "v_mfma_f32_16x16x32_bf16 v[36:39], v[140:143], v[128:131], v[36:39]\n\t"
        "ds_read_b128 v[92:95], v197 offset:34816\n\t"
        "v_mfma_f32_16x16x32_bf16 v[52:55], v[140:143], v[132:135], v[52:55]\n\t"
        "ds_read_b64_tr_b16 v[100:101], v198 offset:36960\n\t"
        "v_mfma_f32_16x16x32_bf16 v[8:11], v[144:147], v[120:123], v[8:11]\n\t"
        "ds_read_b64_tr_b16 v[102:103], v198 offset:37104\n\t"
        "v_mfma_f32_16x16x32_bf16 v[24:27], v[144:147], v[124:127], v[24:27]\n\t"
        "v_mfma_f32_16x16x32_bf16 v[40:43], v[144:147], v[128:131], v[40:43]\n\t"
        "ds_read_b64_tr_b16 v[104:105], v198 offset:36992\n\t"
        "v_mfma_f32_16x16x32_bf16 v[56:59], v[144:147], v[132:135], v[56:59]\n\t"
        "ds_read_b64_tr_b16 v[106:107], v198 offset:37136\n\t"
        "v_mfma_f32_16x16x32_bf16 v[12:15], v[148:151], v[120:123], v[12:15]\n\t"
        "ds_read_b64_tr_b16 v[108:109], v198 offset:37024\n\t"
        "v_mfma_f32_16x16x32_bf16 v[28:31], v[148:151], v[124:127], v[28:31]\n\t"
        "ds_read_b64_tr_b16 v[110:111], v198 offset:37168\n\t"
        "v_mfma_f32_16x16x32_bf16 v[44:47], v[148:151], v[128:131], v[44:47]\n\t"
        "v_mfma_f32_16x16x32_bf16 v[60:63], v[148:151], v[132:135], v[60:63]\n\t"
        "ds_read_b64_tr_b16 v[112:113], v199 offset:36928\n\t"
        "v_mfma_f32_16x16x32_bf16 v[64:67], v[152:155], v[120:123], v[64:67]\n\t"
        "ds_read_b64_tr_b16 v[114:115], v199 offset:37072\n\t"
        "v_mfma_f32_16x16x32_bf16 v[68:71], v[152:155], v[124:127], v[68:71]\n\t"
        "ds_read_b64_tr_b16 v[116:117], v200 offset:1024\n\t"
        "v_mfma_f32_16x16x32_bf16 v[72:75], v[156:159], v[128:131], v[72:75]\n\t"
        "ds_read_b64_tr_b16 v[118:119], v200 offset:1088\n\t"
        "v_mfma_f32_16x16x32_bf16 v[76:79], v[156:159], v[132:135], v[76:79]\n\t"
        "s_waitcnt lgkmcnt(0)\n\t"
        "v_mfma_f32_16x16x32_bf16 v[0:3], v[96:99], v[80:83], v[0:3]\n\t"
        "ds_read_b128 v[120:123], v218 offset:32768\n\t"
        "v_mfma_f32_16x16x32_bf16 v[16:19], v[96:99], v[84:87], v[16:19]\n\t"
        "ds_read_b64_tr_b16 v[136:137], v198 offset:41536\n\t"
        "v_mfma_f32_16x16x32_bf16 v[32:35], v[96:99], v[88:91], v[32:35]\n\t"
        "s_and_b32 s53, s52, 0xff\n\t"
        "s_mul_i32 s53, s53, 1152\n\t"
        "s_add_u32 m0, s54, 2048\n\t"
        "v_add_co_u32 v224, vcc, s53, v220\n\t"
        "v_addc_co_u32 v225, vcc, 0, v221, vcc\n\t"
        "global_load_lds_dword v[224:225], off\n\t"
        "s_and_b32 s53, s52, 0xff\n\t"
        "s_mul_i32 s53, s53, 64\n\t"
        "s_add_u32 m0, s55, 2048\n\t"
        "s_mov_b64 exec, s[50:51]\n\t"
        "v_add_co_u32 v224, vcc, s53, v222\n\t"
        "v_addc_co_u32 v225, vcc, 0, v223, vcc\n\t"
        "global_load_lds_ushort v[224:225], off\n\t"
        "s_mov_b64 exec, -1\n\t"
        "s_lshr_b32 s52, s52, 8\n\t"
        "v_mfma_f32_16x16x32_bf16 v[48:51], v[96:99], v[92:95], v[48:51]\n\t"
        "ds_read_b64_tr_b16 v[138:139], v198 offset:41680\n\t"
        "v_mfma_f32_16x16x32_bf16 v[4:7], v[100:103], v[80:83], v[4:7]\n\t"
        "ds_read_b128 v[124:127], v218 offset:34816\n\t"
        "v_mfma_f32_16x16x32_bf16 v[20:23], v[100:103], v[84:87], v[20:23]\n\t"
        "ds_read_b128 v[128:131], v219 offset:32768\n\t"
        "v_mfma_f32_16x16x32_bf16 v[36:39], v[100:103], v[88:91], v[36:39]\n\t"
        "v_mfma_f32_16x16x32_bf16 v[52:55], v[100:103], v[92:95], v[52:55]\n\t"
        "ds_read_b128 v[132:135], v219 offset:34816\n\t"
        "v_mfma_f32_16x16x32_bf16 v[8:11], v[104:107], v[80:83], v[8:11]\n\t"
        "ds_read_b64_tr_b16 v[140:141], v198 offset:41568\n\t"
        "v_mfma_f32_16x16x32_bf16 v[24:27], v[104:107], v[84:87], v[24:27]\n\t"
        "ds_read_b64_tr_b16 v[142:143], v198 offset:41712\n\t"
        "v_mfma_f32_16x16x32_bf16 v[40:43], v[104:107], v[88:91], v[40:43]\n\t"
        "ds_read_b64_tr_b16 v[144:145], v198 offset:41600\n\t"
        "v_mfma_f32_16x16x32_bf16 v[56:59], v[104:107], v[92:95], v[56:59]\n\t"
        "ds_read_b64_tr_b16 v[146:147], v198 offset:41744\n\t"
        "v_mfma_f32_16x16x32_bf16 v[12:15], v[108:111], v[80:83], v[12:15]\n\t"
        "ds_read_b64_tr_b16 v[148:149], v198 offset:41632\n\t"
        "v_mfma_f32_16x16x32_bf16 v[28:31], v[108:111], v[84:87], v[28:31]\n\t"
        "v_mfma_f32_16x16x32_bf16 v[44:47], v[108:111], v[88:91], v[44:47]\n\t"
        "ds_read_b64_tr_b16 v[150:151], v198 offset:41776\n\t"
        "v_mfma_f32_16x16x32_bf16 v[60:63], v[108:111], v[92:95], v[60:63]\n\t"
        "ds_read_b64_tr_b16 v[152:153], v199 offset:41536\n\t"
        "v_mfma_f32_16x16x32_bf16 v[64:67], v[112:115], v[80:83], v[64:67]\n\t"
        "ds_read_b64_tr_b16 v[154:155], v199 offset:41680\n\t"
        "v_mfma_f32_16x16x32_bf16 v[68:71], v[112:115], v[84:87], v[68:71]\n\t"
        "ds_read_b64_tr_b16 v[156:157], v200 offset:1280\n\t"
        "v_mfma_f32_16x16x32_bf16 v[72:75], v[116:119], v[88:91], v[72:75]\n\t"
        "ds_read_b64_tr_b16 v[158:159], v200 offset:1344\n\t"
        "v_mfma_f32_16x16x32_bf16 v[76:79], v[116:119], v[92:95], v[76:79]\n\t"
        "s_waitcnt lgkmcnt(0)\n\t"
        "s_barrier\n\t"
        "v_mfma_f32_16x16x32_bf16 v[0:3], v[136:139], v[120:123], v[0:3]\n\t"
        "ds_read_b128 v[80:83], v196 offset:49152\n\t"
        "v_mfma_f32_16x16x32_bf16 v[16:19], v[136:139], v[124:127], v[16:19]\n\t"
        "ds_read_b64_tr_b16 v[96:97], v198 offset:55392\n\t"
        "v_mfma_f32_16x16x32_bf16 v[32:35], v[136:139], v[128:131], v[32:35]\n\t"
        "ds_read_b64_tr_b16 v[98:99], v198 offset:55536\n\t"
        "v_mfma_f32_16x16x32_bf16 v[48:51], v[136:139], v[132:135], v[48:51]\n\t"
        "ds_read_b128 v[84:87], v196 offset:51200\n\t"
        "v_mfma_f32_16x16x32_bf16 v[4:7], v[140:143], v[120:123], v[4:7]\n\t"
        "v_mfma_f32_16x16x32_bf16 v[20:23], v[140:143], v[124:127], v[20:23]\n\t"
        "ds_read_b128 v[88:91], v197 offset:49152\n\t"
        "v_mfma_f32_16x16x32_bf16 v[36:39], v[140:143], v[128:131], v[36:39]\n\t"
        "ds_read_b128 v[92:95], v197 offset:51200\n\t"
        "v_mfma_f32_16x16x32_bf16 v[52:55], v[140:143], v[132:135], v[52:55]\n\t"
        "ds_read_b64_tr_b16 v[100:101], v198 offset:55424\n\t"
        "v_mfma_f32_16x16x32_bf16 v[8:11], v[144:147], v[120:123], v[8:11]\n\t"
        "ds_read_b64_tr_b16 v[102:103], v198 offset:55568\n\t"
        "v_mfma_f32_16x16x32_bf16 v[24:27], v[144:147], v[124:127], v[24:27]\n\t"
        "v_mfma_f32_16x16x32_bf16 v[40:43], v[144:147], v[128:131], v[40:43]\n\t"
        "ds_read_b64_tr_b16 v[104:105], v198 offset:55456\n\t"
        "v_mfma_f32_16x16x32_bf16 v[56:59], v[144:147], v[132:135], v[56:59]\n\t"
        "ds_read_b64_tr_b16 v[106:107], v198 offset:55600\n\t"
        "v_mfma_f32_16x16x32_bf16 v[12:15], v[148:151], v[120:123], v[12:15]\n\t"
        "ds_read_b64_tr_b16 v[108:109], v198 offset:55488\n\t"
        "v_mfma_f32_16x16x32_bf16 v[28:31], v[148:151], v[124:127], v[28:31]\n\t"
        "ds_read_b64_tr_b16 v[110:111], v198 offset:55632\n\t"
        "v_mfma_f32_16x16x32_bf16 v[44:47], v[148:151], v[128:131], v[44:47]\n\t"
        "v_mfma_f32_16x16x32_bf16 v[60:63], v[148:151], v[132:135], v[60:63]\n\t"
        "ds_read_b64_tr_b16 v[112:113], v199 offset:55392\n\t"
        "v_mfma_f32_16x16x32_bf16 v[64:67], v[152:155], v[120:123], v[64:67]\n\t"
        "ds_read_b64_tr_b16 v[114:115], v199 offset:55536\n\t"
        "v_mfma_f32_16x16x32_bf16 v[68:71], v[152:155], v[124:127], v[68:71]\n\t"
        "ds_read_b64_tr_b16 v[116:117], v200 offset:1536\n\t"
        "v_mfma_f32_16x16x32_bf16 v[72:75], v[156:159], v[128:131], v[72:75]\n\t"
        "ds_read_b64_tr_b16 v[118:119], v200 offset:1600\n\t"
        "v_mfma_f32_16x16x32_bf16 v[76:79], v[156:159], v[132:135], v[76:79]\n\t"
        "s_waitcnt lgkmcnt(0)\n\t"
        "v_mfma_f32_16x16x32_bf16 v[0:3], v[96:99], v[80:83], v[0:3]\n\t"
        "ds_read_b128 v[120:123], v218 offset:49152\n\t"
        "v_mfma_f32_16x16x32_bf16 v[16:19], v[96:99], v[84:87], v[16:19]\n\t"
        "ds_read_b64_tr_b16 v[136:137], v198 offset:60000\n\t"
        "v_mfma_f32_16x16x32_bf16 v[32:35], v[96:99], v[88:91], v[32:35]\n\t"
        "ds_read_b64_tr_b16 v[138:139], v198 offset:60144\n\t"
        "v_mfma_f32_16x16x32_bf16 v[48:51], v[96:99], v[92:95], v[48:51]\n\t"
        "ds_read_b128 v[124:127], v218 offset:51200\n\t"
        "v_mfma_f32_16x16x32_bf16 v[4:7], v[100:103], v[80:83], v[4:7]\n\t"
        "ds_read_b128 v[128:131], v219 offset:49152\n\t"
        "v_mfma_f32_16x16x32_bf16 v[20:23], v[100:103], v[84:87], v[20:23]\n\t"
        "ds_read_b128 v[132:135], v219 offset:51200\n\t"
        "v_mfma_f32_16x16x32_bf16 v[36:39], v[100:103], v[88:91], v[36:39]\n\t"
        "v_mfma_f32_16x16x32_bf16 v[52:55], v[100:103], v[92:95], v[52:55]\n\t"
        "ds_read_b64_tr_b16 v[140:141], v198 offset:60032\n\t"
        "v_mfma_f32_16x16x32_bf16 v[8:11], v[104:107], v[80:83], v[8:11]\n\t"
        "ds_read_b64_tr_b16 v[142:143], v198 offset:60176\n\t"
        "v_mfma_f32_16x16x32_bf16 v[24:27], v[104:107], v[84:87], v[24:27]\n\t"
        "ds_read_b64_tr_b16 v[144:145], v198 offset:60064\n\t"
        "v_mfma_f32_16x16x32_bf16 v[40:43], v[104:107], v[88:91], v[40:43]\n\t"
        "ds_read_b64_tr_b16 v[146:147], v198 offset:60208\n\t"
        "v_mfma_f32_16x16x32_bf16 v[56:59], v[104:107], v[92:95], v[56:59]\n\t"
        "ds_read_b64_tr_b16 v[148:149], v198 offset:60096\n\t"
        "v_mfma_f32_16x16x32_bf16 v[12:15], v[108:111], v[80:83], v[12:15]\n\t"
        "ds_read_b64_tr_b16 v[150:151], v198 offset:60240\n\t"
        "v_mfma_f32_16x16x32_bf16 v[28:31], v[108:111], v[84:87], v[28:31]\n\t"
        "v_mfma_f32_16x16x32_bf16 v[44:47], v[108:111], v[88:91], v[44:47]\n\t"
        "ds_read_b64_tr_b16 v[152:153], v199 offset:60000\n\t"
        "v_mfma_f32_16x16x32_bf16 v[60:63], v[108:111], v[92:95], v[60:63]\n\t"
        "ds_read_b64_tr_b16 v[154:155], v199 offset:60144\n\t"
        "v_mfma_f32_16x16x32_bf16 v[64:67], v[112:115], v[80:83], v[64:67]\n\t"
        "ds_read_b64_tr_b16 v[156:157], v200 offset:1792\n\t"
        "v_mfma_f32_16x16x32_bf16 v[68:71], v[112:115], v[84:87], v[68:71]\n\t"
        "ds_read_b64_tr_b16 v[158:159], v200 offset:1856\n\t"
        "v_mfma_f32_16x16x32_bf16 v[72:75], v[116:119], v[88:91], v[72:75]\n\t"
        "s_waitcnt vmcnt(0)\n\t"
        "v_mfma_f32_16x16x32_bf16 v[76:79], v[116:119], v[92:95], v[76:79]\n\t"
        "s_waitcnt lgkmcnt(0)\n\t"
        "s_barrier\n\t"
        "v_mfma_f32_16x16x32_bf16 v[0:3], v[136:139], v[120:123], v[0:3]\n\t"
        "ds_read_b128 v[80:83], v196 offset:0\n\t"
        "v_mfma_f32_16x16x32_bf16 v[16:19], v[136:139], v[124:127], v[16:19]\n\t"
        "ds_read_b64_tr_b16 v[96:97], v198 offset:0\n\t"
        "v_mfma_f32_16x16x32_bf16 v[32:35], v[136:139], v[128:131], v[32:35]\n\t"
        "ds_read_b64_tr_b16 v[98:99], v198 offset:144\n\t"
        "v_mfma_f32_16x16x32_bf16 v[48:51], v[136:139], v[132:135], v[48:51]\n\t"
        "ds_read_b128 v[84:87], v196 offset:2048\n\t"
        "v_mfma_f32_16x16x32_bf16 v[4:7], v[140:143], v[120:123], v[4:7]\n\t"
        "v_mfma_f32_16x16x32_bf16 v[20:23], v[140:143], v[124:127], v[20:23]\n\t"
        "ds_read_b128 v[88:91], v197 offset:0\n\t"
        "v_mfma_f32_16x16x32_bf16 v[36:39], v[140:143], v[128:131], v[36:39]\n\t"
        "ds_read_b128 v[92:95], v197 offset:2048\n\t"
        "v_mfma_f32_16x16x32_bf16 v[52:55], v[140:143], v[132:135], v[52:55]\n\t"
        "ds_read_b64_tr_b16 v[100:101], v198 offset:32\n\t"
        "v_mfma_f32_16x16x32_bf16 v[8:11], v[144:147], v[120:123], v[8:11]\n\t"
        "ds_read_b64_tr_b16 v[102:103], v198 offset:176\n\t"
        "v_mfma_f32_16x16x32_bf16 v[24:27], v[144:147], v[124:127], v[24:27]\n\t"
        "v_mfma_f32_16x16x32_bf16 v[40:43], v[144:147], v[128:131], v[40:43]\n\t"
        "ds_read_b64_tr_b16 v[104:105], v198 offset:64\n\t"
        "v_mfma_f32_16x16x32_bf16 v[56:59], v[144:147], v[132:135], v[56:59]\n\t"
        "ds_read_b64_tr_b16 v[106:107], v198 offset:208\n\t"
        "v_mfma_f32_16x16x32_bf16 v[12:15], v[148:151], v[120:123], v[12:15]\n\t"
        "ds_read_b64_tr_b16 v[108:109], v198 offset:96\n\t"
        "v_mfma_f32_16x16x32_bf16 v[28:31], v[148:151], v[124:127], v[28:31]\n\t"
        "ds_read_b64_tr_b16 v[110:111], v198 offset:240\n\t"
        "v_mfma_f32_16x16x32_bf16 v[44:47], v[148:151], v[128:131], v[44:47]\n\t"
        "v_mfma_f32_16x16x32_bf16 v[60:63], v[148:151], v[132:135], v[60:63]\n\t"
        "ds_read_b64_tr_b16 v[112:113], v199 offset:0\n\t"
        "v_mfma_f32_16x16x32_bf16 v[64:67], v[152:155], v[120:123], v[64:67]\n\t"
        "ds_read_b64_tr_b16 v[114:115], v199 offset:144\n\t"
        "v_mfma_f32_16x16x32_bf16 v[68:71], v[152:155], v[124:127], v[68:71]\n\t"
        "ds_read_b64_tr_b16 v[116:117], v200 offset:2048\n\t"
        "v_mfma_f32_16x16x32_bf16 v[72:75], v[156:159], v[128:131], v[72:75]\n\t"
        "ds_read_b64_tr_b16 v[118:119], v200 offset:2112\n\t"
        "v_mfma_f32_16x16x32_bf16 v[76:79], v[156:159], v[132:135], v[76:79]\n\t"
        "s_waitcnt lgkmcnt(0)\n\t"
        "v_mfma_f32_16x16x32_bf16 v[0:3], v[96:99], v[80:83], v[0:3]\n\t"
        "ds_read_b128 v[120:123], v218 offset:0\n\t"
        "v_mfma_f32_16x16x32_bf16 v[16:19], v[96:99], v[84:87], v[16:19]\n\t"
        "ds_read_b64_tr_b16 v[136:137], v198 offset:4608\n\t"
        "v_mfma_f32_16x16x32_bf16 v[32:35], v[96:99], v[88:91], v[32:35]\n\t"
        "s_and_b32 s53, s52, 0xff\n\t"
        "s_mul_i32 s53, s53, 1152\n\t"
        "s_add_u32 m0, s54, 3072\n\t"
        "v_add_co_u32 v224, vcc, s53, v220\n\t"
        "v_addc_co_u32 v225, vcc, 0, v221, vcc\n\t"
        "global_load_lds_dword v[224:225], off\n\t"
        "s_and_b32 s53, s52, 0xff\n\t"
        "s_mul_i32 s53, s53, 64\n\t"
        "s_add_u32 m0, s55, 3072\n\t"
        "s_mov_b64 exec, s[50:51]\n\t"
        "v_add_co_u32 v224, vcc, s53, v222\n\t"
        "v_addc_co_u32 v225, vcc, 0, v223, vcc\n\t"
        "global_load_lds_ushort v[224:225], off\n\t"
        "s_mov_b64 exec, -1\n\t"
        "s_lshr_b32 s52, s52, 8\n\t"
        "v_mfma_f32_16x16x32_bf16 v[48:51], v[96:99], v[92:95], v[48:51]\n\t"
        "ds_read_b64_tr_b16 v[138:139], v198 offset:4752\n\t"
        "v_mfma_f32_16x16x32_bf16 v[4:7], v[100:103], v[80:83], v[4:7]\n\t"
        "ds_read_b128 v[124:127], v218 offset:2048\n\t"
        "v_mfma_f32_16x16x32_bf16 v[20:23], v[100:103], v[84:87], v[20:23]\n\t"
        "ds_read_b128 v[128:131], v219 offset:0\n\t"
        "v_mfma_f32_16x16x32_bf16 v[36:39], v[100:103], v[88:91], v[36:39]\n\t"
        "v_mfma_f32_16x16x32_bf16 v[52:55], v[100:103], v[92:95], v[52:55]\n\t"
        "ds_read_b128 v[132:135], v219 offset:2048\n\t"
        "v_mfma_f32_16x16x32_bf16 v[8:11], v[104:107], v[80:83], v[8:11]\n\t"
        "ds_read_b64_tr_b16 v[140:141], v198 offset:4640\n\t"
        "v_mfma_f32_16x16x32_bf16 v[24:27], v[104:107], v[84:87], v[24:27]\n\t"
        "ds_read_b64_tr_b16 v[142:143], v198 offset:4784\n\t"
        "v_mfma_f32_16x16x32_bf16 v[40:43], v[104:107], v[88:91], v[40:43]\n\t"
        "ds_read_b64_tr_b16 v[144:145], v198 offset:4672\n\t"
        "v_mfma_f32_16x16x32_bf16 v[56:59], v[104:107], v[92:95], v[56:59]\n\t"
        "ds_read_b64_tr_b16 v[146:147], v198 offset:4816\n\t"
        "v_mfma_f32_16x16x32_bf16 v[12:15], v[108:111], v[80:83], v[12:15]\n\t"
        "ds_read_b64_tr_b16 v[148:149], v198 offset:4704\n\t"
        "v_mfma_f32_16x16x32_bf16 v[28:31], v[108:111], v[84:87], v[28:31]\n\t"
        "v_mfma_f32_16x16x32_bf16 v[44:47], v[108:111], v[88:91], v[44:47]\n\t"
        "ds_read_b64_tr_b16 v[150:151], v198 offset:4848\n\t"
        "v_mfma_f32_16x16x32_bf16 v[60:63], v[108:111], v[92:95], v[60:63]\n\t"
        "ds_read_b64_tr_b16 v[152:153], v199 offset:4608\n\t"
        "v_mfma_f32_16x16x32_bf16 v[64:67], v[112:115], v[80:83], v[64:67]\n\t"
        "ds_read_b64_tr_b16 v[154:155], v199 offset:4752\n\t"
        "v_mfma_f32_16x16x32_bf16 v[68:71], v[112:115], v[84:87], v[68:71]\n\t"
        "ds_read_b64_tr_b16 v[156:157], v200 offset:2304\n\t"
        "v_mfma_f32_16x16x32_bf16 v[72:75], v[116:119], v[88:91], v[72:75]\n\t"
        "ds_read_b64_tr_b16 v[158:159], v200 offset:2368\n\t"
        "v_mfma_f32_16x16x32_bf16 v[76:79], v[116:119], v[92:95], v[76:79]\n\t"
        "s_waitcnt lgkmcnt(0)\n\t"
        "s_barrier\n\t"
        "v_mfma_f32_16x16x32_bf16 v[0:3], v[136:139], v[120:123], v[0:3]\n\t"
        "ds_read_b128 v[80:83], v196 offset:16384\n\t"
        "v_mfma_f32_16x16x32_bf16 v[16:19], v[136:139], v[124:127], v[16:19]\n\t"
        "ds_read_b64_tr_b16 v[96:97], v198 offset:18464\n\t"
        "v_mfma_f32_16x16x32_bf16 v[32:35], v[136:139], v[128:131], v[32:35]\n\t"
        "ds_read_b64_tr_b16 v[98:99], v198 offset:18608\n\t"
        "v_mfma_f32_16x16x32_bf16 v[48:51], v[136:139], v[132:135], v[48:51]\n\t"
        "ds_read_b128 v[84:87], v196 offset:18432\n\t"
        "v_mfma_f32_16x16x32_bf16 v[4:7], v[140:143], v[120:123], v[4:7]\n\t"
        "v_mfma_f32_16x16x32_bf16 v[20:23], v[140:143], v[124:127], v[20:23]\n\t"
        "ds_read_b128 v[88:91], v197 offset:16384\n\t"
        "v_mfma_f32_16x16x32_bf16 v[36:39], v[140:143], v[128:131], v[36:39]\n\t"
        "ds_read_b128 v[92:95], v197 offset:18432\n\t"
        "v_mfma_f32_16x16x32_bf16 v[52:55], v[140:143], v[132:135], v[52:55]\n\t"
        "ds_read_b64_tr_b16 v[100:101], v198 offset:18496\n\t"
        "v_mfma_f32_16x16x32_bf16 v[8:11], v[144:147], v[120:123], v[8:11]\n\t"
        "ds_read_b64_tr_b16 v[102:103], v198 offset:18640\n\t"
        "v_mfma_f32_16x16x32_bf16 v[24:27], v[144:147], v[124:127], v[24:27]\n\t"
        "v_mfma_f32_16x16x32_bf16 v[40:43], v[144:147], v[128:131], v[40:43]\n\t"
        "ds_read_b64_tr_b16 v[104:105], v198 offset:18528\n\t"
        "v_mfma_f32_16x16x32_bf16 v[56:59], v[144:147], v[132:135], v[56:59]\n\t"
        "ds_read_b64_tr_b16 v[106:107], v198 offset:18672\n\t"
        "v_mfma_f32_16x16x32_bf16 v[12:15], v[148:151], v[120:123], v[12:15]\n\t"
        "ds_read_b64_tr_b16 v[108:109], v198 offset:18560\n\t"
        "v_mfma_f32_16x16x32_bf16 v[28:31], v[148:151], v[124:127], v[28:31]\n\t"
        "ds_read_b64_tr_b16 v[110:111], v198 offset:18704\n\t"
        "v_mfma_f32_16x16x32_bf16 v[44:47], v[148:151], v[128:131], v[44:47]\n\t"
        "v_mfma_f32_16x16x32_bf16 v[60:63], v[148:151], v[132:135], v[60:63]\n\t"
        "ds_read_b64_tr_b16 v[112:113], v199 offset:18464\n\t"
        "v_mfma_f32_16x16x32_bf16 v[64:67], v[152:155], v[120:123], v[64:67]\n\t"
        "ds_read_b64_tr_b16 v[114:115], v199 offset:18608\n\t"
        "v_mfma_f32_16x16x32_bf16 v[68:71], v[152:155], v[124:127], v[68:71]\n\t"
        "ds_read_b64_tr_b16 v[116:117], v200 offset:2560\n\t"
        "v_mfma_f32_16x16x32_bf16 v[72:75], v[156:159], v[128:131], v[72:75]\n\t"
        "ds_read_b64_tr_b16 v[118:119], v200 offset:2624\n\t"
        "v_mfma_f32_16x16x32_bf16 v[76:79], v[156:159], v[132:135], v[76:79]\n\t"
        "s_waitcnt lgkmcnt(0)\n\t"
        "v_mfma_f32_16x16x32_bf16 v[0:3], v[96:99], v[80:83], v[0:3]\n\t"
        "ds_read_b128 v[120:123], v218 offset:16384\n\t"
        "v_mfma_f32_16x16x32_bf16 v[16:19], v[96:99], v[84:87], v[16:19]\n\t"
        "ds_read_b64_tr_b16 v[136:137], v198 offset:23072\n\t"
        "v_mfma_f32_16x16x32_bf16 v[32:35], v[96:99], v[88:91], v[32:35]\n\t"
        "ds_read_b64_tr_b16 v[138:139], v198 offset:23216\n\t"
        "v_mfma_f32_16x16x32_bf16 v[48:51], v[96:99], v[92:95], v[48:51]\n\t"
        "ds_read_b128 v[124:127], v218 offset:18432\n\t"
        "v_mfma_f32_16x16x32_bf16 v[4:7], v[100:103], v[80:83], v[4:7]\n\t"
        "ds_read_b128 v[128:131], v219 offset:16384\n\t"
        "v_mfma_f32_16x16x32_bf16 v[20:23], v[100:103], v[84:87], v[20:23]\n\t"
        "ds_read_b128 v[132:135], v219 offset:18432\n\t"
        "v_mfma_f32_16x16x32_bf16 v[36:39], v[100:103], v[88:91], v[36:39]\n\t"
        "v_mfma_f32_16x16x32_bf16 v[52:55], v[100:103], v[92:95], v[52:55]\n\t"
        "ds_read_b64_tr_b16 v[140:141], v198 offset:23104\n\t"
        "v_mfma_f32_16x16x32_bf16 v[8:11], v[104:107], v[80:83], v[8:11]\n\t"
        "ds_read_b64_tr_b16 v[142:143], v198 offset:23248\n\t"
        "v_mfma_f32_16x16x32_bf16 v[24:27], v[104:107], v[84:87], v[24:27]\n\t"
        "ds_read_b64_tr_b16 v[144:145], v198 offset:23136\n\t"
        "v_mfma_f32_16x16x32_bf16 v[40:43], v[104:107], v[88:91], v[40:43]\n\t"
        "ds_read_b64_tr_b16 v[146:147], v198 offset:23280\n\t"
        "v_mfma_f32_16x16x32_bf16 v[56:59], v[104:107], v[92:95], v[56:59]\n\t"
        "ds_read_b64_tr_b16 v[148:149], v198 offset:23168\n\t"
        "v_mfma_f32_16x16x32_bf16 v[12:15], v[108:111], v[80:83], v[12:15]\n\t"
        "ds_read_b64_tr_b16 v[150:151], v198 offset:23312\n\t"
        "v_mfma_f32_16x16x32_bf16 v[28:31], v[108:111], v[84:87], v[28:31]\n\t"
        "v_mfma_f32_16x16x32_bf16 v[44:47], v[108:111], v[88:91], v[44:47]\n\t"
        "ds_read_b64_tr_b16 v[152:153], v199 offset:23072\n\t"
        "v_mfma_f32_16x16x32_bf16 v[60:63], v[108:111], v[92:95], v[60:63]\n\t"
        "ds_read_b64_tr_b16 v[154:155], v199 offset:23216\n\t"
        "v_mfma_f32_16x16x32_bf16 v[64:67], v[112:115], v[80:83], v[64:67]\n\t"
        "ds_read_b64_tr_b16 v[156:157], v200 offset:2816\n\t"
        "v_mfma_f32_16x16x32_bf16 v[68:71], v[112:115], v[84:87], v[68:71]\n\t"
        "ds_read_b64_tr_b16 v[158:159], v200 offset:2880\n\t"
        "v_mfma_f32_16x16x32_bf16 v[72:75], v[116:119], v[88:91], v[72:75]\n\t"
        "s_waitcnt vmcnt(0)\n\t"
        "v_mfma_f32_16x16x32_bf16 v[76:79], v[116:119], v[92:95], v[76:79]\n\t"
        "s_waitcnt lgkmcnt(0)\n\t"
        "s_barrier\n\t"
        "v_mfma_f32_16x16x32_bf16 v[0:3], v[136:139], v[120:123], v[0:3]\n\t"
        "ds_read_b128 v[80:83], v196 offset:32768\n\t"
        "v_mfma_f32_16x16x32_bf16 v[16:19], v[136:139], v[124:127], v[16:19]\n\t"
        "ds_read_b64_tr_b16 v[96:97], v198 offset:36928\n\t"
        "v_mfma_f32_16x16x32_bf16 v[32:35], v[136:139], v[128:131], v[32:35]\n\t"
        "ds_read_b64_tr_b16 v[98:99], v198 offset:37072\n\t"
        "v_mfma_f32_16x16x32_bf16 v[48:51], v[136:139], v[132:135], v[48:51]\n\t"
        "ds_read_b128 v[84:87], v196 offset:34816\n\t"
        "v_mfma_f32_16x16x32_bf16 v[4:7], v[140:143], v[120:123], v[4:7]\n\t"
        "v_mfma_f32_16x16x32_bf16 v[20:23], v[140:143], v[124:127], v[20:23]\n\t"
        "ds_read_b128 v[88:91], v197 offset:32768\n\t"
        "v_mfma_f32_16x16x32_bf16 v[36:39], v[140:143], v[128:131], v[36:39]\n\t"
        "ds_read_b128 v[92:95], v197 offset:34816\n\t"
        "v_mfma_f32_16x16x32_bf16 v[52:55], v[140:143], v[132:135], v[52:55]\n\t"
        "ds_read_b64_tr_b16 v[100:101], v198 offset:36960\n\t"
        "v_mfma_f32_16x16x32_bf16 v[8:11], v[144:147], v[120:123], v[8:11]\n\t"
        "ds_read_b64_tr_b16 v[102:103], v198 offset:37104\n\t"
        "v_mfma_f32_16x16x32_bf16 v[24:27], v[144:147], v[124:127], v[24:27]\n\t"
        "v_mfma_f32_16x16x32_bf16 v[40:43], v[144:147], v[128:131], v[40:43]\n\t"
        "ds_read_b64_tr_b16 v[104:105], v198 offset:36992\n\t"
        "v_mfma_f32_16x16x32_bf16 v[56:59], v[144:147], v[132:135], v[56:59]\n\t"
        "ds_read_b64_tr_b16 v[106:107], v198 offset:37136\n\t"
        "v_mfma_f32_16x16x32_bf16 v[12:15], v[148:151], v[120:123], v[12:15]\n\t"
        "ds_read_b64_tr_b16 v[108:109], v198 offset:37024\n\t"
        "v_mfma_f32_16x16x32_bf16 v[28:31], v[148:151], v[124:127], v[28:31]\n\t"
        "ds_read_b64_tr_b16 v[110:111], v198 offset:37168\n\t"
        "v_mfma_f32_16x16x32_bf16 v[44:47], v[148:151], v[128:131], v[44:47]\n\t"
        "v_mfma_f32_16x16x32_bf16 v[60:63], v[148:151], v[132:135], v[60:63]\n\t"
        "ds_read_b64_tr_b16 v[112:113], v199 offset:36928\n\t"
        "v_mfma_f32_16x16x32_bf16 v[64:67], v[152:155], v[120:123], v[64:67]\n\t"
        "ds_read_b64_tr_b16 v[114:115], v199 offset:37072\n\t"
        "v_mfma_f32_16x16x32_bf16 v[68:71], v[152:155], v[124:127], v[68:71]\n\t"
        "ds_read_b64_tr_b16 v[116:117], v200 offset:3072\n\t"
        "v_mfma_f32_16x16x32_bf16 v[72:75], v[156:159], v[128:131], v[72:75]\n\t"
        "ds_read_b64_tr_b16 v[118:119], v200 offset:3136\n\t"
        "v_mfma_f32_16x16x32_bf16 v[76:79], v[156:159], v[132:135], v[76:79]\n\t"
        "s_waitcnt lgkmcnt(0)\n\t"
        "v_mfma_f32_16x16x32_bf16 v[0:3], v[96:99], v[80:83], v[0:3]\n\t"
        "ds_read_b128 v[120:123], v218 offset:32768\n\t"
        "v_mfma_f32_16x16x32_bf16 v[16:19], v[96:99], v[84:87], v[16:19]\n\t"
        "ds_read_b64_tr_b16 v[136:137], v198 offset:41536\n\t"
        "v_mfma_f32_16x16x32_bf16 v[32:35], v[96:99], v[88:91], v[32:35]\n\t"
        "s_cmp_ge_u32 %[w], 2\n\t"
        "s_cbranch_scc1 47f\n\t"
        "s_and_b32 s53, s52, 0xff\n\t"
        "s_mul_i32 s53, s53, 1152\n\t"
        "s_add_u32 m0, s54, 4096\n\t"
        "v_add_co_u32 v224, vcc, s53, v220\n\t"
        "v_addc_co_u32 v225, vcc, 0, v221, vcc\n\t"
        "global_load_lds_dword v[224:225], off\n\t"
        "s_and_b32 s53, s52, 0xff\n\t"
        "s_mul_i32 s53, s53, 64\n\t"
        "s_add_u32 m0, s55, 4096\n\t"
        "s_mov_b64 exec, s[50:51]\n\t"
        "v_add_co_u32 v224, vcc, s53, v222\n\t"
        "v_addc_co_u32 v225, vcc, 0, v223, vcc\n\t"
        "global_load_lds_ushort v[224:225], off\n\t"
        "s_mov_b64 exec, -1\n\t"
        "s_lshr_b32 s52, s52, 8\n\t"
        "47:\n\t"
        "v_mfma_f32_16x16x32_bf16 v[48:51], v[96:99], v[92:95], v[48:51]\n\t"
        "ds_read_b64_tr_b16 v[138:139], v198 offset:41680\n\t"
        "v_mfma_f32_16x16x32_bf16 v[4:7], v[100:103], v[80:83], v[4:7]\n\t"
        "ds_read_b128 v[124:127], v218 offset:34816\n\t"
        "v_mfma_f32_16x16x32_bf16 v[20:23], v[100:103], v[84:87], v[20:23]\n\t"
        "ds_read_b128 v[128:131], v219 offset:32768\n\t"
        "v_mfma_f32_16x16x32_bf16 v[36:39], v[100:103], v[88:91], v[36:39]\n\t"
        "v_mfma_f32_16x16x32_bf16 v[52:55], v[100:103], v[92:95], v[52:55]\n\t"
        "ds_read_b128 v[132:135], v219 offset:34816\n\t"
        "v_mfma_f32_16x16x32_bf16 v[8:11], v[104:107], v[80:83], v[8:11]\n\t"
        "ds_read_b64_tr_b16 v[140:141], v198 offset:41568\n\t"
        "v_mfma_f32_16x16x32_bf16 v[24:27], v[104:107], v[84:87], v[24:27]\n\t"
        "ds_read_b64_tr_b16 v[142:143], v198 offset:41712\n\t"
        "v_mfma_f32_16x16x32_bf16 v[40:43], v[104:107], v[88:91], v[40:43]\n\t"
        "ds_read_b64_tr_b16 v[144:145], v198 offset:41600\n\t"
        "v_mfma_f32_16x16x32_bf16 v[56:59], v[104:107], v[92:95], v[56:59]\n\t"
        "ds_read_b64_tr_b16 v[146:147], v198 offset:41744\n\t"
        "v_mfma_f32_16x16x32_bf16 v[12:15], v[108:111], v[80:83], v[12:15]\n\t"
        "ds_read_b64_tr_b16 v[148:149], v198 offset:41632\n\t"
        "v_mfma_f32_16x16x32_bf16 v[28:31], v[108:111], v[84:87], v[28:31]\n\t"
        "v_mfma_f32_16x16x32_bf16 v[44:47], v[108:111], v[88:91], v[44:47]\n\t"
        "ds_read_b64_tr_b16 v[150:151], v198 offset:41776\n\t"
        "v_mfma_f32_16x16x32_bf16 v[60:63], v[108:111], v[92:95], v[60:63]\n\t"
        "ds_read_b64_tr_b16 v[152:153], v199 offset:41536\n\t"
        "v_mfma_f32_16x16x32_bf16 v[64:67], v[112:115], v[80:83], v[64:67]\n\t"
        "ds_read_b64_tr_b16 v[154:155], v199 offset:41680\n\t"
        "v_mfma_f32_16x16x32_bf16 v[68:71], v[112:115], v[84:87], v[68:71]\n\t"
        "ds_read_b64_tr_b16 v[156:157], v200 offset:3328\n\t"
        "v_mfma_f32_16x16x32_bf16 v[72:75], v[116:119], v[88:91], v[72:75]\n\t"
        "ds_read_b64_tr_b16 v[158:159], v200 offset:3392\n\t"
        "v_mfma_f32_16x16x32_bf16 v[76:79], v[116:119], v[92:95], v[76:79]\n\t"
        "s_waitcnt lgkmcnt(0)\n\t"
        "s_barrier\n\t"
        "v_mfma_f32_16x16x32_bf16 v[0:3], v[136:139], v[120:123], v[0:3]\n\t"
        "ds_read_b128 v[80:83], v196 offset:49152\n\t"
        "v_mfma_f32_16x16x32_bf16 v[16:19], v[136:139], v[124:127], v[16:19]\n\t"
        "ds_read_b64_tr_b16 v[96:97], v198 offset:55392\n\t"
        "v_mfma_f32_16x16x32_bf16 v[32:35], v[136:139], v[128:131], v[32:35]\n\t"
        "ds_read_b64_tr_b16 v[98:99], v198 offset:55536\n\t"
        "v_mfma_f32_16x16x32_bf16 v[48:51], v[136:139], v[132:135], v[48:51]\n\t"
        "ds_read_b128 v[84:87], v196 offset:51200\n\t"
        "v_mfma_f32_16x16x32_bf16 v[4:7], v[140:143], v[120:123], v[4:7]\n\t"
        "v_mfma_f32_16x16x32_bf16 v[20:23], v[140:143], v[124:127], v[20:23]\n\t"
        "ds_read_b128 v[88:91], v197 offset:49152\n\t"
        "v_mfma_f32_16x16x32_bf16 v[36:39], v[140:143], v[128:131], v[36:39]\n\t"
        "ds_read_b128 v[92:95], v197 offset:51200\n\t"
        "v_mfma_f32_16x16x32_bf16 v[52:55], v[140:143], v[132:135], v[52:55]\n\t"
        "ds_read_b64_tr_b16 v[100:101], v198 offset:55424\n\t"
        "v_mfma_f32_16x16x32_bf16 v[8:11], v[144:147], v[120:123], v[8:11]\n\t"
        "ds_read_b64_tr_b16 v[102:103], v198 offset:55568\n\t"
        "v_mfma_f32_16x16x32_bf16 v[24:27], v[144:147], v[124:127], v[24:27]\n\t"
        "v_mfma_f32_16x16x32_bf16 v[40:43], v[144:147], v[128:131], v[40:43]\n\t"
        "ds_read_b64_tr_b16 v[104:105], v198 offset:55456\n\t"
        "v_mfma_f32_16x16x32_bf16 v[56:59], v[144:147], v[132:135], v[56:59]\n\t"
        "ds_read_b64_tr_b16 v[106:107], v198 offset:55600\n\t"
        "v_mfma_f32_16x16x32_bf16 v[12:15], v[148:151], v[120:123], v[12:15]\n\t"
        "ds_read_b64_tr_b16 v[108:109], v198 offset:55488\n\t"
        "v_mfma_f32_16x16x32_bf16 v[28:31], v[148:151], v[124:127], v[28:31]\n\t"
        "ds_read_b64_tr_b16 v[110:111], v198 offset:55632\n\t"
        "v_mfma_f32_16x16x32_bf16 v[44:47], v[148:151], v[128:131], v[44:47]\n\t"
        "v_mfma_f32_16x16x32_bf16 v[60:63], v[148:151], v[132:135], v[60:63]\n\t"
        "ds_read_b64_tr_b16 v[112:113], v199 offset:55392\n\t"
        "v_mfma_f32_16x16x32_bf16 v[64:67], v[152:155], v[120:123], v[64:67]\n\t"
        "ds_read_b64_tr_b16 v[114:115], v199 offset:55536\n\t"
        "v_mfma_f32_16x16x32_bf16 v[68:71], v[152:155], v[124:127], v[68:71]\n\t"
        "ds_read_b64_tr_b16 v[116:117], v200 offset:3584\n\t"
        "v_mfma_f32_16x16x32_bf16 v[72:75], v[156:159], v[128:131], v[72:75]\n\t"
        "ds_read_b64_tr_b16 v[118:119], v200 offset:3648\n\t"
        "v_mfma_f32_16x16x32_bf16 v[76:79], v[156:159], v[132:135], v[76:79]\n\t"
        "s_waitcnt lgkmcnt(0)\n\t"
        "v_mfma_f32_16x16x32_bf16 v[0:3], v[96:99], v[80:83], v[0:3]\n\t"
        "s_waitcnt vmcnt(0)\n\t"
        "ds_read_b128 v[120:123], v218 offset:49152\n\t"
        "v_mfma_f32_16x16x32_bf16 v[16:19], v[96:99], v[84:87], v[16:19]\n\t"
        "ds_read_b64_tr_b16 v[136:137], v198 offset:60000\n\t"
        "v_mfma_f32_16x16x32_bf16 v[32:35], v[96:99], v[88:91], v[32:35]\n\t"
        "ds_read_b64_tr_b16 v[138:139], v198 offset:60144\n\t"
        "v_mfma_f32_16x16x32_bf16 v[48:51], v[96:99], v[92:95], v[48:51]\n\t"
        "ds_read_b128 v[124:127], v218 offset:51200\n\t"
        "v_mfma_f32_16x16x32_bf16 v[4:7], v[100:103], v[80:83], v[4:7]\n\t"
        "ds_read_b128 v[128:131], v219 offset:49152\n\t"
        "ds_read_b128 v[132:135], v219 offset:51200\n\t"
        "v_mfma_f32_16x16x32_bf16 v[20:23], v[100:103], v[84:87], v[20:23]\n\t"
        "ds_read_b64_tr_b16 v[140:141], v198 offset:60032\n\t"
        "v_mfma_f32_16x16x32_bf16 v[36:39], v[100:103], v[88:91], v[36:39]\n\t"
        "ds_read_b64_tr_b16 v[142:143], v198 offset:60176\n\t"
        "v_mfma_f32_16x16x32_bf16 v[52:55], v[100:103], v[92:95], v[52:55]\n\t"
        "ds_read_b64_tr_b16 v[144:145], v198 offset:60064\n\t"
        "v_mfma_f32_16x16x32_bf16 v[8:11], v[104:107], v[80:83], v[8:11]\n\t"
        "ds_read_b64_tr_b16 v[146:147], v198 offset:60208\n\t"
        "ds_read_b64_tr_b16 v[148:149], v198 offset:60096\n\t"
        "v_mfma_f32_16x16x32_bf16 v[24:27], v[104:107], v[84:87], v[24:27]\n\t"
        "ds_read_b64_tr_b16 v[150:151], v198 offset:60240\n\t"
        "v_mfma_f32_16x16x32_bf16 v[40:43], v[104:107], v[88:91], v[40:43]\n\t"
        "ds_read_b64_tr_b16 v[152:153], v199 offset:60000\n\t"
        "v_mfma_f32_16x16x32_bf16 v[56:59], v[104:107], v[92:95], v[56:59]\n\t"
        "ds_read_b64_tr_b16 v[154:155], v199 offset:60144\n\t"
        "v_mfma_f32_16x16x32_bf16 v[12:15], v[108:111], v[80:83], v[12:15]\n\t"
        "ds_read_b64_tr_b16 v[156:157], v200 offset:3840\n\t"
        "ds_read_b64_tr_b16 v[158:159], v200 offset:3904\n\t"
        "v_mfma_f32_16x16x32_bf16 v[28:31], v[108:111], v[84:87], v[28:31]\n\t"
        "global_load_ushort v160, v204, %[wob]\n\t"
        "global_load_ushort v161, v204, %[wob] offset:1152\n\t"
        "v_mfma_f32_16x16x32_bf16 v[44:47], v[108:111], v[88:91], v[44:47]\n\t"
        "global_load_ushort v162, v205, %[wob]\n\t"
        "global_load_ushort v163, v205, %[wob] offset:1152\n\t"
        "v_mfma_f32_16x16x32_bf16 v[60:63], v[108:111], v[92:95], v[60:63]\n\t"
        "global_load_ushort v164, v206, %[wob]\n\t"
        "global_load_ushort v165, v206, %[wob] offset:1152\n\t"
        "v_mfma_f32_16x16x32_bf16 v[64:67], v[112:115], v[80:83], v[64:67]\n\t"
        "global_load_ushort v166, v207, %[wob]\n\t"
        "global_load_ushort v167, v207, %[wob] offset:1152\n\t"
        "s_mov_b64 exec, %[wom]\n\t"
        "global_load_ushort v168, v208, %[wob]\n\t"
        "global_load_ushort v169, v208, %[wob] offset:1152\n\t"
        "s_mov_b64 exec, -1\n\t"
        "v_mfma_f32_16x16x32_bf16 v[68:71], v[112:115], v[84:87], v[68:71]\n\t"
        "s_mov_b64 exec, %[wom]\n\t"
        "global_load_ushort v170, v209, %[wob]\n\t"
        "global_load_ushort v171, v209, %[wob] offset:1152\n\t"
        "s_mov_b64 exec, -1\n\t"
        "v_mfma_f32_16x16x32_bf16 v[72:75], v[116:119], v[88:91], v[72:75]\n\t"
        "s_mov_b64 exec, %[wom]\n\t"
        "global_load_ushort v172, v210, %[wob]\n\t"
        "global_load_ushort v173, v210, %[wob] offset:1152\n\t"
        "s_mov_b64 exec, -1\n\t"
        "v_mfma_f32_16x16x32_bf16 v[76:79], v[116:119], v[92:95], v[76:79]\n\t"
        "s_mov_b64 exec, %[wom]\n\t"
        "global_load_ushort v174, v211, %[wob]\n\t"
        "global_load_ushort v175, v211, %[wob] offset:1152\n\t"
        "s_mov_b64 exec, -1\n\t"
        "s_waitcnt lgkmcnt(0)\n\t"
        "s_barrier\n\t"
        "v_mfma_f32_16x16x32_bf16 v[0:3], v[136:139], v[120:123], v[0:3]\n\t"
        "ds_read_b128 v[80:83], v196 offset:0\n\t"
        "v_mfma_f32_16x16x32_bf16 v[16:19], v[136:139], v[124:127], v[16:19]\n\t"
        "ds_read_b64_tr_b16 v[96:97], v198 offset:0\n\t"
        "v_mfma_f32_16x16x32_bf16 v[32:35], v[136:139], v[128:131], v[32:35]\n\t"
        "ds_read_b64_tr_b16 v[98:99], v198 offset:144\n\t"
        "v_mfma_f32_16x16x32_bf16 v[48:51], v[136:139], v[132:135], v[48:51]\n\t"
        "ds_read_b128 v[84:87], v196 offset:2048\n\t"
        "v_mfma_f32_16x16x32_bf16 v[4:7], v[140:143], v[120:123], v[4:7]\n\t"
        "v_mfma_f32_16x16x32_bf16 v[20:23], v[140:143], v[124:127], v[20:23]\n\t"
        "ds_read_b128 v[88:91], v197 offset:0\n\t"
        "v_mfma_f32_16x16x32_bf16 v[36:39], v[140:143], v[128:131], v[36:39]\n\t"
        "ds_read_b128 v[92:95], v197 offset:2048\n\t"
        "v_mfma_f32_16x16x32_bf16 v[52:55], v[140:143], v[132:135], v[52:55]\n\t"
        "ds_read_b64_tr_b16 v[100:101], v198 offset:32\n\t"
        "v_mfma_f32_16x16x32_bf16 v[8:11], v[144:147], v[120:123], v[8:11]\n\t"
        "ds_read_b64_tr_b16 v[102:103], v198 offset:176\n\t"
        "v_mfma_f32_16x16x32_bf16 v[24:27], v[144:147], v[124:127], v[24:27]\n\t"
        "v_mfma_f32_16x16x32_bf16 v[40:43], v[144:147], v[128:131], v[40:43]\n\t"
        "ds_read_b64_tr_b16 v[104:105], v198 offset:64\n\t"
        "v_mfma_f32_16x16x32_bf16 v[56:59], v[144:147], v[132:135], v[56:59]\n\t"
        "ds_read_b64_tr_b16 v[106:107], v198 offset:208\n\t"
        "v_mfma_f32_16x16x32_bf16 v[12:15], v[148:151], v[120:123], v[12:15]\n\t"
        "ds_read_b64_tr_b16 v[108:109], v198 offset:96\n\t"
        "v_mfma_f32_16x16x32_bf16 v[28:31], v[148:151], v[124:127], v[28:31]\n\t"
        "ds_read_b64_tr_b16 v[110:111], v198 offset:240\n\t"
        "v_mfma_f32_16x16x32_bf16 v[44:47], v[148:151], v[128:131], v[44:47]\n\t"
        "v_mfma_f32_16x16x32_bf16 v[60:63], v[148:151], v[132:135], v[60:63]\n\t"
        "ds_read_b64_tr_b16 v[112:113], v199 offset:0\n\t"
        "v_mfma_f32_16x16x32_bf16 v[64:67], v[152:155], v[120:123], v[64:67]\n\t"
        "ds_read_b64_tr_b16 v[114:115], v199 offset:144\n\t"
        "v_mfma_f32_16x16x32_bf16 v[68:71], v[152:155], v[124:127], v[68:71]\n\t"
        "ds_read_b64_tr_b16 v[116:117], v200 offset:4096\n\t"
        "v_mfma_f32_16x16x32_bf16 v[72:75], v[156:159], v[128:131], v[72:75]\n\t"
        "ds_read_b64_tr_b16 v[118:119], v200 offset:4160\n\t"
        "v_mfma_f32_16x16x32_bf16 v[76:79], v[156:159], v[132:135], v[76:79]\n\t"
        "s_waitcnt lgkmcnt(0)\n\t"
        "v_mfma_f32_16x16x32_bf16 v[0:3], v[96:99], v[80:83], v[0:3]\n\t"
        "ds_read_b128 v[120:123], v218 offset:0\n\t"
        "v_mfma_f32_16x16x32_bf16 v[16:19], v[96:99], v[84:87], v[16:19]\n\t"
        "ds_read_b64_tr_b16 v[136:137], v198 offset:4608\n\t"
        "v_mfma_f32_16x16x32_bf16 v[32:35], v[96:99], v[88:91], v[32:35]\n\t"
        "ds_read_b64_tr_b16 v[138:139], v198 offset:4752\n\t"
        "v_mfma_f32_16x16x32_bf16 v[48:51], v[96:99], v[92:95], v[48:51]\n\t"
        "ds_read_b128 v[124:127], v218 offset:2048\n\t"
        "v_mfma_f32_16x16x32_bf16 v[4:7], v[100:103], v[80:83], v[4:7]\n\t"
        "ds_read_b128 v[128:131], v219 offset:0\n\t"
        "v_mfma_f32_16x16x32_bf16 v[20:23], v[100:103], v[84:87], v[20:23]\n\t"
        "ds_read_b128 v[132:135], v219 offset:2048\n\t"
        "v_mfma_f32_16x16x32_bf16 v[36:39], v[100:103], v[88:91], v[36:39]\n\t"
        "v_mfma_f32_16x16x32_bf16 v[52:55], v[100:103], v[92:95], v[52:55]\n\t"
        "ds_read_b64_tr_b16 v[140:141], v198 offset:4640\n\t"
        "v_mfma_f32_16x16x32_bf16 v[8:11], v[104:107], v[80:83], v[8:11]\n\t"
        "ds_read_b64_tr_b16 v[142:143], v198 offset:4784\n\t"
        "v_mfma_f32_16x16x32_bf16 v[24:27], v[104:107], v[84:87], v[24:27]\n\t"
        "ds_read_b64_tr_b16 v[144:145], v198 offset:4672\n\t"
        "v_mfma_f32_16x16x32_bf16 v[40:43], v[104:107], v[88:91], v[40:43]\n\t"
        "ds_read_b64_tr_b16 v[146:147], v198 offset:4816\n\t"
        "v_mfma_f32_16x16x32_bf16 v[56:59], v[104:107], v[92:95], v[56:59]\n\t"
        "ds_read_b64_tr_b16 v[148:149], v198 offset:4704\n\t"
        "v_mfma_f32_16x16x32_bf16 v[12:15], v[108:111], v[80:83], v[12:15]\n\t"
        "ds_read_b64_tr_b16 v[150:151], v198 offset:4848\n\t"
        "v_mfma_f32_16x16x32_bf16 v[28:31], v[108:111], v[84:87], v[28:31]\n\t"
        "v_mfma_f32_16x16x32_bf16 v[44:47], v[108:111], v[88:91], v[44:47]\n\t"
        "ds_read_b64_tr_b16 v[152:153], v199 offset:4608\n\t"
        "v_mfma_f32_16x16x32_bf16 v[60:63], v[108:111], v[92:95], v[60:63]\n\t"
        "ds_read_b64_tr_b16 v[154:155], v199 offset:4752\n\t"
        "v_mfma_f32_16x16x32_bf16 v[64:67], v[112:115], v[80:83], v[64:67]\n\t"
        "ds_read_b64_tr_b16 v[156:157], v200 offset:4352\n\t"
        "v_mfma_f32_16x16x32_bf16 v[68:71], v[112:115], v[84:87], v[68:71]\n\t"
        "ds_read_b64_tr_b16 v[158:159], v200 offset:4416\n\t"
        "v_mfma_f32_16x16x32_bf16 v[72:75], v[116:119], v[88:91], v[72:75]\n\t"
        "s_mov_b32 exec_lo, 0x0011ffff\n\t"
        "s_mov_b32 exec_hi, 0x0011ffff\n\t"
        "ds_read_u16 v176, v245 offset:0\n\t"
        "ds_read_u16 v177, v245 offset:36\n\t"
        "ds_read_u16 v178, v245 offset:72\n\t"
        "ds_read_u16 v179, v245 offset:108\n\t"
        "ds_read_u16 v180, v245 offset:144\n\t"
        "ds_read_u16 v181, v245 offset:180\n\t"
        "ds_read_u16 v182, v245 offset:216\n\t"
        "ds_read_u16 v183, v245 offset:252\n\t"
        "s_mov_b32 exec_hi, 0\n\t"
        "ds_read_u16 v192, v245 offset:576\n\t"
        "ds_read_u16 v193, v245 offset:612\n\t"
        "s_mov_b64 exec, -1\n\t"
        "v_mfma_f32_16x16x32_bf16 v[76:79], v[116:119], v[92:95], v[76:79]\n\t"
        "s_waitcnt lgkmcnt(0)\n\t"
        "v_mfma_f32_16x16x32_bf16 v[72:75], v[156:159], v[128:131], v[72:75]\n\t"
        "v_lshl_or_b32 v184, v177, 16, v176\n\t"
        "v_lshl_or_b32 v185, v179, 16, v178\n\t"
        "v_mfma_f32_16x16x32_bf16 v[76:79], v[156:159], v[132:135], v[76:79]\n\t"
        "v_lshl_or_b32 v186, v181, 16, v180\n\t"
        "v_lshl_or_b32 v187, v183, 16, v182\n\t"
        "v_mfma_f32_16x16x32_bf16 v[64:67], v[152:155], v[120:123], v[64:67]\n\t"
        "v_lshl_or_b32 v188, v193, 16, v192\n\t"
        "s_nop 0\n\t"
        "v_mfma_f32_16x16x32_bf16 v[68:71], v[152:155], v[124:127], v[68:71]\n\t"
        "s_nop 1\n\t"
        "v_mfma_f32_16x16x32_bf16 v[0:3], v[136:139], v[120:123], v[0:3]\n\t"
        "s_nop 1\n\t"
        "v_mfma_f32_16x16x32_bf16 v[16:19], v[136:139], v[124:127], v[16:19]\n\t"
        "v_pk_mul_f32 v[64:65], v[64:65], v[214:215] op_sel_hi:[1,0]\n\t"
        "v_pk_mul_f32 v[66:67], v[66:67], v[214:215] op_sel_hi:[1,0]\n\t"
        "v_cvt_pk_bf16_f32 v80, v64, v65\n\t"
        "v_cvt_pk_bf16_f32 v81, v66, v67\n\t"
        "ds_write_b64 v215, v[80:81] offset:0\n\t"
        "v_mfma_f32_16x16x32_bf16 v[32:35], v[136:139], v[128:131], v[32:35]\n\t"
        "v_pk_mul_f32 v[68:69], v[68:69], v[214:215] op_sel_hi:[1,0]\n\t"
        "v_pk_mul_f32 v[70:71], v[70:71], v[214:215] op_sel_hi:[1,0]\n\t"
        "v_cvt_pk_bf16_f32 v82, v68, v69\n\t"
        "v_cvt_pk_bf16_f32 v83, v70, v71\n\t"
        "ds_write_b64 v215, v[82:83] offset:4736\n\t"
        "v_mfma_f32_16x16x32_bf16 v[48:51], v[136:139], v[132:135], v[48:51]\n\t"
        "v_pk_mul_f32 v[0:1], v[0:1], s[44:45]\n\t"
        "v_pk_mul_f32 v[2:3], v[2:3], s[44:45]\n\t"
        "v_cvt_pk_bf16_f32 v84, v0, v1\n\t"
        "v_cvt_pk_bf16_f32 v85, v2, v3\n\t"
        "ds_write_b64 v212, v[84:85] offset:0\n\t"
        "v_mfma_f32_16x16x32_bf16 v[4:7], v[140:143], v[120:123], v[4:7]\n\t"
        "v_pk_mul_f32 v[16:17], v[16:17], s[44:45]\n\t"
        "v_pk_mul_f32 v[18:19], v[18:19], s[44:45]\n\t"
        "v_cvt_pk_bf16_f32 v86, v16, v17\n\t"
        "v_cvt_pk_bf16_f32 v87, v18, v19\n\t"
        "ds_write_b64 v212, v[86:87] offset:4736\n\t"
        "v_mfma_f32_16x16x32_bf16 v[20:23], v[140:143], v[124:127], v[20:23]\n\t"
        "v_pk_mul_f32 v[32:33], v[32:33], s[44:45]\n\t"
        "v_pk_mul_f32 v[34:35], v[34:35], s[44:45]\n\t"
        "v_cvt_pk_bf16_f32 v88, v32, v33\n\t"
        "v_cvt_pk_bf16_f32 v89, v34, v35\n\t"
        "ds_write_b64 v213, v[88:89] offset:9472\n\t"
        "v_mfma_f32_16x16x32_bf16 v[36:39], v[140:143], v[128:131], v[36:39]\n\t"
        "v_pk_mul_f32 v[48:49], v[48:49], s[44:45]\n\t"
        "v_pk_mul_f32 v[50:51], v[50:51], s[44:45]\n\t"
        "v_cvt_pk_bf16_f32 v90, v48, v49\n\t"
        "v_cvt_pk_bf16_f32 v91, v50, v51\n\t"
        "ds_write_b64 v213, v[90:91] offset:14208\n\t"
        "v_mfma_f32_16x16x32_bf16 v[52:55], v[140:143], v[132:135], v[52:55]\n\t"
        "v_pk_mul_f32 v[4:5], v[4:5], s[44:45]\n\t"
        "v_pk_mul_f32 v[6:7], v[6:7], s[44:45]\n\t"
        "v_cvt_pk_bf16_f32 v92, v4, v5\n\t"
        "v_cvt_pk_bf16_f32 v93, v6, v7\n\t"
        "ds_write_b64 v212, v[92:93] offset:32\n\t"
        "v_mfma_f32_16x16x32_bf16 v[8:11], v[144:147], v[120:123], v[8:11]\n\t"
        "v_pk_mul_f32 v[20:21], v[20:21], s[44:45]\n\t"
        "v_pk_mul_f32 v[22:23], v[22:23], s[44:45]\n\t"
        "v_cvt_pk_bf16_f32 v94, v20, v21\n\t"
        "v_cvt_pk_bf16_f32 v95, v22, v23\n\t"
        "ds_write_b64 v212, v[94:95] offset:4768\n\t"
        "v_mfma_f32_16x16x32_bf16 v[24:27], v[144:147], v[124:127], v[24:27]\n\t"
        "v_pk_mul_f32 v[36:37], v[36:37], s[44:45]\n\t"
        "v_pk_mul_f32 v[38:39], v[38:39], s[44:45]\n\t"
        "v_cvt_pk_bf16_f32 v80, v36, v37\n\t"
        "v_cvt_pk_bf16_f32 v81, v38, v39\n\t"
        "ds_write_b64 v213, v[80:81] offset:9504\n\t"
        "v_mfma_f32_16x16x32_bf16 v[40:43], v[144:147], v[128:131], v[40:43]\n\t"
        "v_pk_mul_f32 v[52:53], v[52:53], s[44:45]\n\t"
        "v_pk_mul_f32 v[54:55], v[54:55], s[44:45]\n\t"
        "v_cvt_pk_bf16_f32 v82, v52, v53\n\t"
        "v_cvt_pk_bf16_f32 v83, v54, v55\n\t"
        "ds_write_b64 v213, v[82:83] offset:14240\n\t"
        "v_mfma_f32_16x16x32_bf16 v[56:59], v[144:147], v[132:135], v[56:59]\n\t"
        "v_pk_mul_f32 v[8:9], v[8:9], s[44:45]\n\t"
        "v_pk_mul_f32 v[10:11], v[10:11], s[44:45]\n\t"
        "v_cvt_pk_bf16_f32 v84, v8, v9\n\t"
        "v_cvt_pk_bf16_f32 v85, v10, v11\n\t"
        "ds_write_b64 v212, v[84:85] offset:64\n\t"
        "v_mfma_f32_16x16x32_bf16 v[12:15], v[148:151], v[120:123], v[12:15]\n\t"
        "v_pk_mul_f32 v[24:25], v[24:25], s[44:45]\n\t"
        "v_pk_mul_f32 v[26:27], v[26:27], s[44:45]\n\t"
        "v_cvt_pk_bf16_f32 v86, v24, v25\n\t"
        "v_cvt_pk_bf16_f32 v87, v26, v27\n\t"
        "ds_write_b64 v212, v[86:87] offset:4800\n\t"
        "v_mfma_f32_16x16x32_bf16 v[28:31], v[148:151], v[124:127], v[28:31]\n\t"
        "v_pk_mul_f32 v[40:41], v[40:41], s[44:45]\n\t"
        "v_pk_mul_f32 v[42:43], v[42:43], s[44:45]\n\t"
        "v_cvt_pk_bf16_f32 v88, v40, v41\n\t"
        "v_cvt_pk_bf16_f32 v89, v42, v43\n\t"
        "ds_write_b64 v213, v[88:89] offset:9536\n\t"
        "v_mfma_f32_16x16x32_bf16 v[44:47], v[148:151], v[128:131], v[44:47]\n\t"
        "v_pk_mul_f32 v[56:57], v[56:57], s[44:45]\n\t"
        "v_pk_mul_f32 v[58:59], v[58:59], s[44:45]\n\t"
        "v_cvt_pk_bf16_f32 v90, v56, v57\n\t"
        "v_cvt_pk_bf16_f32 v91, v58, v59\n\t"
        "ds_write_b64 v213, v[90:91] offset:14272\n\t"
        "v_mfma_f32_16x16x32_bf16 v[60:63], v[148:151], v[132:135], v[60:63]\n\t"
        "v_pk_mul_f32 v[12:13], v[12:13], s[44:45]\n\t"
        "v_pk_mul_f32 v[14:15], v[14:15], s[44:45]\n\t"
        "v_cvt_pk_bf16_f32 v92, v12, v13\n\t"
        "v_cvt_pk_bf16_f32 v93, v14, v15\n\t"
        "ds_write_b64 v212, v[92:93] offset:96\n\t"
        "v_pk_mul_f32 v[28:29], v[28:29], s[44:45]\n\t"
        "v_pk_mul_f32 v[30:31], v[30:31], s[44:45]\n\t"
        "v_cvt_pk_bf16_f32 v94, v28, v29\n\t"
        "v_cvt_pk_bf16_f32 v95, v30, v31\n\t"
        "ds_write_b64 v212, v[94:95] offset:4832\n\t"
        "v_pk_mul_f32 v[44:45], v[44:45], s[44:45]\n\t"
        "v_pk_mul_f32 v[46:47], v[46:47], s[44:45]\n\t"
        "v_cvt_pk_bf16_f32 v80, v44, v45\n\t"
        "v_cvt_pk_bf16_f32 v81, v46, v47\n\t"
        "ds_write_b64 v213, v[80:81] offset:9568\n\t"
        "v_pk_mul_f32 v[60:61], v[60:61], s[44:45]\n\t"
        "v_pk_mul_f32 v[62:63], v[62:63], s[44:45]\n\t"
        "v_cvt_pk_bf16_f32 v82, v60, v61\n\t"
        "v_cvt_pk_bf16_f32 v83, v62, v63\n\t"
        "ds_write_b64 v213, v[82:83] offset:14304\n\t"
        "v_cvt_pk_bf16_f32 v100, v72, v73\n\t"
        "v_cvt_pk_bf16_f32 v101, v74, v75\n\t"
        "v_cvt_pk_bf16_f32 v102, v76, v77\n\t"
        "v_cvt_pk_bf16_f32 v103, v78, v79\n\t"
        "v_mul_f32 v104, 0xbfb8aa3b, v72\n\t"
        "v_mul_f32 v105, 0xbfb8aa3b, v76\n\t"
        "v_exp_f32 v104, v104\n\t"
        "v_exp_f32 v105, v105\n\t"
        "v_mov_b32 v106, 0x3f80\n\t"
        "v_add_f32 v104, 1.0, v104\n\t"
        "v_add_f32 v105, 1.0, v105\n\t"
        "v_rcp_f32 v104, v104\n\t"
        "v_rcp_f32 v105, v105\n\t"
        "s_mov_b64 exec, 0xffff\n\t"
        "global_store_dword v201, v100, %[wsb] offset:0 sc1\n\t"
        "global_store_short v203, v101, %[wsb] offset:0 sc1\n\t"
        "global_store_short_d16_hi v203, v101, %[wsb] offset:16 sc1\n\t"
        "global_store_short v203, v106, %[wsb] offset:32 sc1\n\t"
        "global_store_dword v201, v102, %[wsb] offset:64 sc1\n\t"
        "global_store_short v203, v103, %[wsb] offset:608 sc1\n\t"
        "global_store_short_d16_hi v203, v103, %[wsb] offset:624 sc1\n\t"
        "global_store_short v203, v106, %[wsb] offset:640 sc1\n\t"
        "s_mov_b32 s46, 0xffff0000\n\t"
        "s_mov_b32 s47, 0\n\t"
        "s_mov_b64 exec, s[46:47]\n\t"
        "global_store_dword v201, v104, %[wsb] offset:0 sc1\n\t"
        "global_store_dword v201, v105, %[wsb] offset:64 sc1\n\t"
        "s_mov_b64 exec, -1\n\t"
        "69:\n\t"
            : "=&{v[184:187]}"(PA0), "=&{v[188:191]}"(PA1), "=&{v[228:231]}"(EP0), "=&{v[232:235]}"(EP1), "=&{v[236:239]}"(EP2), "=&{v[246:249]}"(EZ0), "=&{v[250:253]}"(EZ1), "=&{v245}"(PHA),
              "=&{v[160:175]}"(WO), "+{v[216:217]}"(V6)
            : "{v244}"(lane), "{v[196:199]}"(V1), "{v[200:203]}"(V2), "{v[204:207]}"(V3), "{v[208:211]}"(V4), "{v[212:215]}"(V5), "{v[220:223]}"(EA4),
              [blk] "s"(blk), [w] "s"(w), [wob] "s"(wob), [wsb] "s"(wsb), [wom] "s"(wom),
              [ver] "s"(ver), [cb] "s"(cb), [isq] "s"(isq), [sc0] "s"(sc0)
            : "v0", "v1", "v2", "v3", "v4", "v5", "v6", "v7", "v8", "v9", "v10", "v11", "v12", "v13", "v14", "v15", "v16", "v17", "v18", "v19", "v20", "v21", "v22", "v23", "v24", "v25", "v26", "v27", "v28", "v29", "v30", "v31", "v32", "v33", "v34", "v35", "v36", "v37", "v38", "v39", "v40", "v41", "v42", "v43", "v44", "v45", "v46", "v47", "v48", "v49", "v50", "v51", "v52", "v53", "v54", "v55", "v56", "v57", "v58", "v59", "v60", "v61", "v62", "v63", "v64", "v65", "v66", "v67", "v68", "v69", "v70", "v71", "v72", "v73", "v74", "v75", "v76", "v77", "v78", "v79", "v80", "v81", "v82", "v83", "v84", "v85", "v86", "v87", "v88", "v89", "v90", "v91", "v92", "v93", "v94", "v95", "v96", "v97", "v98", "v99", "v100", "v101", "v102", "v103", "v104", "v105", "v106", "v107", "v108", "v109", "v110", "v111", "v112", "v113", "v114", "v115", "v116", "v117", "v118", "v119", "v120", "v121", "v122", "v123", "v124", "v125", "v126", "v127", "v128", "v129", "v130", "v131", "v132", "v133", "v134", "v135", "v136", "v137", "v138", "v139", "v140", "v141", "v142", "v143", "v144", "v145", "v146", "v147", "v148", "v149", "v150", "v151", "v152", "v153", "v154", "v155", "v156", "v157", "v158", "v159", "v176", "v177", "v178", "v179", "v180", "v181", "v182", "v183", "v192", "v193", "v194", "v195", "v218", "v219", "v224", "v225", "v226", "v227", "v240", "v241", "v242", "v243", "s42", "s43", "s44", "s45", "s46", "s47", "s48", "s49", "s50", "s51", "s52", "s53", "s54", "s55", "scc", "vcc", "m0", "memory");

    }
    STAMP(0, 1);
    {

        asm volatile(
        "s_mov_b32 s62, 0x3fb8aa3b\n\t"
        "s_mov_b32 s63, s62\n\t"
        "s_mov_b64 s[52:53], %[pqb]\n\t"
        "s_add_u32 s54, s52, 2304\n\t"
        "s_addc_u32 s55, s53, 0\n\t"
        "s_bitcmp1_b32 %[w], 0\n\t"
        "s_mov_b32 s44, -1\n\t"
        "s_cselect_b32 s44, 0xfffcfffc, s44\n\t"
        "s_mov_b32 s45, s44\n\t"
        "s_mov_b32 s46, 0x00070007\n\t"
        "s_cselect_b32 s46, 0x001f001f, s46\n\t"
        "s_mov_b32 s47, s46\n\t"
        "s_mov_b32 s48, 0x00040004\n\t"
        "s_cselect_b32 s48, 0x00100010, s48\n\t"
        "s_mov_b32 s49, s48\n\t"
        "s_cmp_lt_u32 %[w], 4\n\t"
        "s_cbranch_scc0 70f\n\t"
        "s_setprio 1\n\t"
        "70:\n\t"
        "s_waitcnt lgkmcnt(0)\n\t"
        "s_barrier\n\t"
        "s_cmp_eq_u32 %[isq], 0\n\t"
        "s_cbranch_scc1 71f\n\t"
        "ds_read2_b32 v[32:33], v228 offset0:0 offset1:1\n\t"
        "ds_read2_b32 v[34:35], v228 offset0:2 offset1:3\n\t"
        "ds_read_b32 v246, v230 offset:32\n\t"
        "ds_read2_b32 v[36:37], v229 offset0:0 offset1:1\n\t"
        "ds_read2_b32 v[38:39], v229 offset0:2 offset1:3\n\t"
        "ds_read_b32 v250, v230 offset:9504\n\t"
        "ds_read2_b32 v[88:89], v228 offset0:36 offset1:37\n\t"
        "ds_read2_b32 v[90:91], v228 offset0:38 offset1:39\n\t"
        "ds_read_b32 v96, v228 offset:176\n\t"
        "ds_read2_b32 v[92:93], v229 offset0:36 offset1:37\n\t"
        "ds_read2_b32 v[94:95], v229 offset0:38 offset1:39\n\t"
        "ds_read_b32 v97, v229 offset:176\n\t"
        "s_waitcnt lgkmcnt(9)\n\t"
        "v_mfma_f32_32x32x16_bf16 v[0:15], v[184:187], v[32:35], 0\n\t"
        "s_nop 1\n\t"
        "v_mfma_f32_32x32x16_bf16 v[0:15], v[188:191], v[246:249], v[0:15]\n\t"
        "s_waitcnt lgkmcnt(6)\n\t"
        "v_mfma_f32_32x32x16_bf16 v[16:31], v[184:187], v[36:39], 0\n\t"
        "s_nop 1\n\t"
        "v_mfma_f32_32x32x16_bf16 v[16:31], v[188:191], v[250:253], v[16:31]\n\t"
        "s_waitcnt lgkmcnt(0)\n\t"
        "global_store_dwordx4 v233, v[88:91], %[qmb] sc1\n\t"
        "global_store_dwordx4 v233, v[92:95], %[qmb] offset:1024 sc1\n\t"
        "s_mov_b32 exec_hi, 0\n\t"
        "global_store_dword v234, v96, %[qtb] sc1\n\t"
        "global_store_dword v234, v97, %[qtb] offset:128 sc1\n\t"
        "s_mov_b32 exec_hi, -1\n\t"
        "s_nop 1\n\t"
        "v_max3_f32 v48, v0, v1, v2\n\t"
        "v_max3_f32 v49, v3, v4, v5\n\t"
        "v_max3_f32 v50, v6, v7, v8\n\t"
        "v_min3_f32 v58, v0, v1, v2\n\t"
        "v_min3_f32 v59, v3, v4, v5\n\t"
        "v_min3_f32 v60, v6, v7, v8\n\t"
        "v_max3_f32 v98, v48, v49, v50\n\t"
        "v_max3_f32 v68, v16, v17, v18\n\t"
        "v_max3_f32 v100, -v58, -v59, -v60\n\t"
        "v_max3_f32 v69, v19, v20, v21\n\t"
        "s_nop 0\n\t"
        "v_max3_f32 v70, v22, v23, v24\n\t"
        "v_permlane32_swap_b32 v98, v100\n\t"
        "v_min3_f32 v78, v16, v17, v18\n\t"
        "v_max_f32 v98, v98, v100\n\t"
        "v_min3_f32 v79, v19, v20, v21\n\t"
        "v_mul_f32 v98, 0xbfb8aa3b, v98\n\t"
        "v_min3_f32 v80, v22, v23, v24\n\t"
        "v_mov_b32 v100, v98\n\t"
        "v_max3_f32 v102, v68, v69, v70\n\t"
        "s_nop 0\n\t"
        "v_max3_f32 v104, -v78, -v79, -v80\n\t"
        "v_permlane32_swap_b32 v98, v100\n\t"
        "s_nop 0\n\t"
        "v_pk_fma_f32 v[48:49], v[0:1], s[62:63], v[98:99] op_sel_hi:[1,0,0]\n\t"
        "v_permlane32_swap_b32 v102, v104\n\t"
        "v_pk_fma_f32 v[50:51], v[2:3], s[62:63], v[98:99] op_sel_hi:[1,0,0]\n\t"
        "v_max_f32 v102, v102, v104\n\t"
        "v_pk_fma_f32 v[52:53], v[4:5], s[62:63], v[98:99] op_sel_hi:[1,0,0]\n\t"
        "v_mul_f32 v102, 0xbfb8aa3b, v102\n\t"
        "v_pk_fma_f32 v[54:55], v[6:7], s[62:63], v[98:99] op_sel_hi:[1,0,0]\n\t"
        "v_mov_b32 v104, v102\n\t"
        "v_fma_f32 v56, v8, s62, v98\n\t"
        "s_nop 0\n\t"
        "v_pk_fma_f32 v[58:59], v[0:1], s[62:63], v[100:101] op_sel_hi:[1,0,0] neg_lo:[0,1,0] neg_hi:[0,1,0]\n\t"
        "v_permlane32_swap_b32 v102, v104\n\t"
        "v_pk_fma_f32 v[60:61], v[2:3], s[62:63], v[100:101] op_sel_hi:[1,0,0] neg_lo:[0,1,0] neg_hi:[0,1,0]\n\t"
        "v_pk_fma_f32 v[68:69], v[16:17], s[62:63], v[102:103] op_sel_hi:[1,0,0]\n\t"
        "v_pk_fma_f32 v[62:63], v[4:5], s[62:63], v[100:101] op_sel_hi:[1,0,0] neg_lo:[0,1,0] neg_hi:[0,1,0]\n\t"
        "v_pk_fma_f32 v[70:71], v[18:19], s[62:63], v[102:103] op_sel_hi:[1,0,0]\n\t"
        "v_pk_fma_f32 v[64:65], v[6:7], s[62:63], v[100:101] op_sel_hi:[1,0,0] neg_lo:[0,1,0] neg_hi:[0,1,0]\n\t"
        "v_pk_fma_f32 v[72:73], v[20:21], s[62:63], v[102:103] op_sel_hi:[1,0,0]\n\t"
        "v_fma_f32 v66, v8, -s62, v100\n\t"
        "v_pk_fma_f32 v[74:75], v[22:23], s[62:63], v[102:103] op_sel_hi:[1,0,0]\n\t"
        "v_exp_f32 v48, v48\n\t"
        "v_fma_f32 v76, v24, s62, v102\n\t"
        "v_exp_f32 v49, v49\n\t"
        "v_pk_fma_f32 v[78:79], v[16:17], s[62:63], v[104:105] op_sel_hi:[1,0,0] neg_lo:[0,1,0] neg_hi:[0,1,0]\n\t"
        "v_exp_f32 v50, v50\n\t"
        "v_pk_fma_f32 v[80:81], v[18:19], s[62:63], v[104:105] op_sel_hi:[1,0,0] neg_lo:[0,1,0] neg_hi:[0,1,0]\n\t"
        "v_exp_f32 v51, v51\n\t"
        "v_pk_fma_f32 v[82:83], v[20:21], s[62:63], v[104:105] op_sel_hi:[1,0,0] neg_lo:[0,1,0] neg_hi:[0,1,0]\n\t"
        "v_exp_f32 v52, v52\n\t"
        "v_pk_fma_f32 v[84:85], v[22:23], s[62:63], v[104:105] op_sel_hi:[1,0,0] neg_lo:[0,1,0] neg_hi:[0,1,0]\n\t"
        "v_exp_f32 v53, v53\n\t"
        "v_fma_f32 v86, v24, -s62, v104\n\t"
        "v_exp_f32 v54, v54\n\t"
        "v_exp_f32 v68, v68\n\t"
        "v_exp_f32 v55, v55\n\t"
        "v_exp_f32 v69, v69\n\t"
        "v_exp_f32 v56, v56\n\t"
        "v_exp_f32 v70, v70\n\t"
        "v_exp_f32 v58, v58\n\t"
        "v_exp_f32 v71, v71\n\t"
        "v_exp_f32 v59, v59\n\t"
        "v_exp_f32 v72, v72\n\t"
        "v_exp_f32 v60, v60\n\t"
        "v_exp_f32 v73, v73\n\t"
        "v_exp_f32 v61, v61\n\t"
        "v_exp_f32 v74, v74\n\t"
        "v_exp_f32 v62, v62\n\t"
        "v_exp_f32 v75, v75\n\t"
        "v_exp_f32 v63, v63\n\t"
        "v_exp_f32 v76, v76\n\t"
        "v_exp_f32 v64, v64\n\t"
        "v_exp_f32 v78, v78\n\t"
        "v_exp_f32 v65, v65\n\t"
        "v_exp_f32 v79, v79\n\t"
        "v_exp_f32 v66, v66\n\t"
        "v_exp_f32 v80, v80\n\t"
        "v_pk_add_f32 v[0:1], v[48:49], v[50:51]\n\t"
        "v_exp_f32 v81, v81\n\t"
        "v_pk_add_f32 v[2:3], v[52:53], v[54:55]\n\t"
        "v_exp_f32 v82, v82\n\t"
        "v_pk_add_f32 v[6:7], v[58:59], v[60:61]\n\t"
        "v_exp_f32 v83, v83\n\t"
        "v_pk_add_f32 v[8:9], v[62:63], v[64:65]\n\t"
        "v_exp_f32 v84, v84\n\t"
        "v_pk_add_f32 v[0:1], v[0:1], v[2:3]\n\t"
        "v_exp_f32 v85, v85\n\t"
        "v_pk_add_f32 v[6:7], v[6:7], v[8:9]\n\t"
        "v_exp_f32 v86, v86\n\t"
        "v_add_f32 v0, v0, v1\n\t"
        "v_pk_add_f32 v[16:17], v[68:69], v[70:71]\n\t"
        "v_add_f32 v6, v6, v7\n\t"
        "v_pk_add_f32 v[18:19], v[72:73], v[74:75]\n\t"
        "v_add_f32 v98, v0, v56\n\t"
        "v_pk_add_f32 v[22:23], v[78:79], v[80:81]\n\t"
        "v_add_f32 v100, v6, v66\n\t"
        "v_pk_add_f32 v[24:25], v[82:83], v[84:85]\n\t"
        "s_nop 0\n\t"
        "v_pk_add_f32 v[16:17], v[16:17], v[18:19]\n\t"
        "v_permlane32_swap_b32 v98, v100\n\t"
        "v_pk_add_f32 v[22:23], v[22:23], v[24:25]\n\t"
        "v_add_f32 v98, v98, v100\n\t"
        "v_add_f32 v16, v16, v17\n\t"
        "v_rcp_f32 v98, v98\n\t"
        "v_add_f32 v22, v22, v23\n\t"
        "s_nop 0\n\t"
        "v_add_f32 v102, v16, v76\n\t"
        "v_mov_b32 v100, v98\n\t"
        "v_add_f32 v104, v22, v86\n\t"
        "s_nop 0\n\t"
        "v_permlane32_swap_b32 v98, v100\n\t"
        "v_permlane32_swap_b32 v102, v104\n\t"
        "v_pk_mul_f32 v[48:49], v[48:49], v[98:99] op_sel_hi:[1,0]\n\t"
        "v_add_f32 v102, v102, v104\n\t"
        "v_pk_mul_f32 v[50:51], v[50:51], v[98:99] op_sel_hi:[1,0]\n\t"
        "v_rcp_f32 v102, v102\n\t"
        "v_pk_mul_f32 v[52:53], v[52:53], v[98:99] op_sel_hi:[1,0]\n\t"
        "s_nop 0\n\t"
        "v_pk_mul_f32 v[54:55], v[54:55], v[98:99] op_sel_hi:[1,0]\n\t"
        "v_mov_b32 v104, v102\n\t"
        "v_mul_f32 v56, v56, v98\n\t"
        "s_nop 0\n\t"
        "v_pk_mul_f32 v[58:59], v[58:59], v[100:101] op_sel_hi:[1,0]\n\t"
        "v_permlane32_swap_b32 v102, v104\n\t"
        "v_pk_mul_f32 v[60:61], v[60:61], v[100:101] op_sel_hi:[1,0]\n\t"
        "v_pk_mul_f32 v[68:69], v[68:69], v[102:103] op_sel_hi:[1,0]\n\t"
        "v_pk_mul_f32 v[62:63], v[62:63], v[100:101] op_sel_hi:[1,0]\n\t"
        "v_pk_mul_f32 v[70:71], v[70:71], v[102:103] op_sel_hi:[1,0]\n\t"
        "v_pk_mul_f32 v[64:65], v[64:65], v[100:101] op_sel_hi:[1,0]\n\t"
        "v_pk_mul_f32 v[72:73], v[72:73], v[102:103] op_sel_hi:[1,0]\n\t"
        "v_mul_f32 v66, v66, v100\n\t"
        "v_pk_mul_f32 v[74:75], v[74:75], v[102:103] op_sel_hi:[1,0]\n\t"
        "v_cvt_pk_bf16_f32 v106, v48, v49\n\t"
        "v_mul_f32 v76, v76, v102\n\t"
        "v_cvt_pk_bf16_f32 v107, v50, v51\n\t"
        "v_pk_mul_f32 v[78:79], v[78:79], v[104:105] op_sel_hi:[1,0]\n\t"
        "v_cvt_pk_bf16_f32 v108, v52, v53\n\t"
        "v_pk_mul_f32 v[80:81], v[80:81], v[104:105] op_sel_hi:[1,0]\n\t"
        "v_cvt_pk_bf16_f32 v109, v54, v55\n\t"
        "v_pk_mul_f32 v[82:83], v[82:83], v[104:105] op_sel_hi:[1,0]\n\t"
        "v_cvt_pk_bf16_f32 v110, v58, v59\n\t"
        "v_pk_mul_f32 v[84:85], v[84:85], v[104:105] op_sel_hi:[1,0]\n\t"
        "v_cvt_pk_bf16_f32 v111, v60, v61\n\t"
        "v_mul_f32 v86, v86, v104\n\t"
        "v_cvt_pk_bf16_f32 v112, v62, v63\n\t"
        "v_cvt_pk_bf16_f32 v116, v68, v69\n\t"
        "v_cvt_pk_bf16_f32 v113, v64, v65\n\t"
        "v_cvt_pk_bf16_f32 v117, v70, v71\n\t"
        "v_cvt_pk_bf16_f32 v114, v56, v66\n\t"
        "v_cvt_pk_bf16_f32 v118, v72, v73\n\t"
        "v_cvt_pk_bf16_f32 v119, v74, v75\n\t"
        "global_store_dwordx4 v233, v[106:109], s[52:53] sc1\n\t"
        "v_cvt_pk_bf16_f32 v120, v78, v79\n\t"
        "global_store_dwordx4 v233, v[110:113], s[52:53] offset:1024 sc1\n\t"
        "v_cvt_pk_bf16_f32 v121, v80, v81\n\t"
        "global_store_dword v234, v114, s[52:53] offset:2048 sc1\n\t"
        "v_cvt_pk_bf16_f32 v122, v82, v83\n\t"
        "v_cvt_pk_bf16_f32 v123, v84, v85\n\t"
        "v_cvt_pk_bf16_f32 v124, v76, v86\n\t"
        "global_store_dwordx4 v233, v[116:119], s[54:55] sc1\n\t"
        "global_store_dwordx4 v233, v[120:123], s[54:55] offset:1024 sc1\n\t"
        "global_store_dword v234, v124, s[54:55] offset:2048 sc1\n\t"
        "s_cmp_lt_u32 %[w], 4\n\t"
        "s_cbranch_scc0 79f\n\t"
        "s_waitcnt vmcnt(10)\n\t"
        "v_lshl_or_b32 v160, v161, 16, v160\n\t"
        "v_lshl_or_b32 v161, v163, 16, v162\n\t"
        "v_lshl_or_b32 v162, v165, 16, v164\n\t"
        "v_lshl_or_b32 v163, v167, 16, v166\n\t"
        "v_lshl_or_b32 v168, v169, 16, v168\n\t"
        "v_lshl_or_b32 v169, v171, 16, v170\n\t"
        "v_lshl_or_b32 v170, v173, 16, v172\n\t"
        "v_lshl_or_b32 v171, v175, 16, v174\n\t"
        "global_store_dwordx4 v216, v[160:163], %[wsb] sc1\n\t"
        "s_mov_b64 exec, %[wom]\n\t"
        "global_store_dwordx4 v217, v[168:171], %[wsb] sc1\n\t"
        "s_mov_b64 exec, -1\n\t"
        "s_branch 79f\n\t"
        "71:\n\t"
        "ds_read2_b32 v[32:33], v228 offset0:0 offset1:1\n\t"
        "ds_read2_b32 v[34:35], v228 offset0:2 offset1:3\n\t"
        "ds_read_b32 v246, v230 offset:32\n\t"
        "ds_read2_b32 v[36:37], v229 offset0:0 offset1:1\n\t"
        "ds_read2_b32 v[38:39], v229 offset0:2 offset1:3\n\t"
        "ds_read_b32 v250, v230 offset:9504\n\t"
        "s_waitcnt lgkmcnt(3)\n\t"
        "v_mfma_f32_32x32x16_bf16 v[0:15], v[184:187], v[32:35], 0\n\t"
        "s_nop 1\n\t"
        "v_mfma_f32_32x32x16_bf16 v[0:15], v[188:191], v[246:249], v[0:15]\n\t"
        "s_waitcnt lgkmcnt(0)\n\t"
        "v_mfma_f32_32x32x16_bf16 v[16:31], v[184:187], v[36:39], 0\n\t"
        "s_nop 1\n\t"
        "v_mfma_f32_32x32x16_bf16 v[16:31], v[188:191], v[250:253], v[16:31]\n\t"
        "s_nop 7\n\t"
        "v_max3_f32 v48, v0, v1, v2\n\t"
        "v_max3_f32 v49, v3, v4, v5\n\t"
        "v_max3_f32 v50, v6, v7, v8\n\t"
        "v_min3_f32 v58, v0, v1, v2\n\t"
        "v_min3_f32 v59, v3, v4, v5\n\t"
        "v_min3_f32 v60, v6, v7, v8\n\t"
        "v_max3_f32 v98, v48, v49, v50\n\t"
        "v_max3_f32 v68, v16, v17, v18\n\t"
        "v_max3_f32 v100, -v58, -v59, -v60\n\t"
        "v_max3_f32 v69, v19, v20, v21\n\t"
        "s_nop 0\n\t"
        "v_max3_f32 v70, v22, v23, v24\n\t"
        "v_permlane32_swap_b32 v98, v100\n\t"
        "v_min3_f32 v78, v16, v17, v18\n\t"
        "v_max_f32 v98, v98, v100\n\t"
        "v_min3_f32 v79, v19, v20, v21\n\t"
        "v_mul_f32 v98, 0xbfb8aa3b, v98\n\t"
        "v_min3_f32 v80, v22, v23, v24\n\t"
        "v_mov_b32 v100, v98\n\t"
        "v_max3_f32 v102, v68, v69, v70\n\t"
        "s_nop 0\n\t"
        "v_max3_f32 v104, -v78, -v79, -v80\n\t"
        "v_permlane32_swap_b32 v98, v100\n\t"
        "s_nop 0\n\t"
        "v_pk_fma_f32 v[48:49], v[0:1], s[62:63], v[98:99] op_sel_hi:[1,0,0]\n\t"
        "v_permlane32_swap_b32 v102, v104\n\t"
        "v_pk_fma_f32 v[50:51], v[2:3], s[62:63], v[98:99] op_sel_hi:[1,0,0]\n\t"
        "v_max_f32 v102, v102, v104\n\t"
        "v_pk_fma_f32 v[52:53], v[4:5], s[62:63], v[98:99] op_sel_hi:[1,0,0]\n\t"
        "v_mul_f32 v102, 0xbfb8aa3b, v102\n\t"
        "v_pk_fma_f32 v[54:55], v[6:7], s[62:63], v[98:99] op_sel_hi:[1,0,0]\n\t"
        "v_mov_b32 v104, v102\n\t"
        "v_fma_f32 v56, v8, s62, v98\n\t"
        "s_nop 0\n\t"
        "v_pk_fma_f32 v[58:59], v[0:1], s[62:63], v[100:101] op_sel_hi:[1,0,0] neg_lo:[0,1,0] neg_hi:[0,1,0]\n\t"
        "v_permlane32_swap_b32 v102, v104\n\t"
        "v_pk_fma_f32 v[60:61], v[2:3], s[62:63], v[100:101] op_sel_hi:[1,0,0] neg_lo:[0,1,0] neg_hi:[0,1,0]\n\t"
        "v_pk_fma_f32 v[68:69], v[16:17], s[62:63], v[102:103] op_sel_hi:[1,0,0]\n\t"
        "v_pk_fma_f32 v[62:63], v[4:5], s[62:63], v[100:101] op_sel_hi:[1,0,0] neg_lo:[0,1,0] neg_hi:[0,1,0]\n\t"
        "v_pk_fma_f32 v[70:71], v[18:19], s[62:63], v[102:103] op_sel_hi:[1,0,0]\n\t"
        "v_pk_fma_f32 v[64:65], v[6:7], s[62:63], v[100:101] op_sel_hi:[1,0,0] neg_lo:[0,1,0] neg_hi:[0,1,0]\n\t"
        "v_pk_fma_f32 v[72:73], v[20:21], s[62:63], v[102:103] op_sel_hi:[1,0,0]\n\t"
        "v_fma_f32 v66, v8, -s62, v100\n\t"
        "v_pk_fma_f32 v[74:75], v[22:23], s[62:63], v[102:103] op_sel_hi:[1,0,0]\n\t"
        "v_exp_f32 v48, v48\n\t"
        "v_fma_f32 v76, v24, s62, v102\n\t"
        "v_exp_f32 v49, v49\n\t"
        "v_pk_fma_f32 v[78:79], v[16:17], s[62:63], v[104:105] op_sel_hi:[1,0,0] neg_lo:[0,1,0] neg_hi:[0,1,0]\n\t"
        "v_exp_f32 v50, v50\n\t"
        "v_pk_fma_f32 v[80:81], v[18:19], s[62:63], v[104:105] op_sel_hi:[1,0,0] neg_lo:[0,1,0] neg_hi:[0,1,0]\n\t"
        "v_exp_f32 v51, v51\n\t"
        "v_pk_fma_f32 v[82:83], v[20:21], s[62:63], v[104:105] op_sel_hi:[1,0,0] neg_lo:[0,1,0] neg_hi:[0,1,0]\n\t"
        "v_exp_f32 v52, v52\n\t"
        "v_pk_fma_f32 v[84:85], v[22:23], s[62:63], v[104:105] op_sel_hi:[1,0,0] neg_lo:[0,1,0] neg_hi:[0,1,0]\n\t"
        "v_exp_f32 v53, v53\n\t"
        "v_fma_f32 v86, v24, -s62, v104\n\t"
        "v_exp_f32 v54, v54\n\t"
        "v_exp_f32 v68, v68\n\t"
        "v_exp_f32 v55, v55\n\t"
        "v_exp_f32 v69, v69\n\t"
        "v_exp_f32 v56, v56\n\t"
        "v_exp_f32 v70, v70\n\t"
        "v_exp_f32 v58, v58\n\t"
        "v_exp_f32 v71, v71\n\t"
        "v_exp_f32 v59, v59\n\t"
        "v_exp_f32 v72, v72\n\t"
        "v_exp_f32 v60, v60\n\t"
        "v_exp_f32 v73, v73\n\t"
        "v_exp_f32 v61, v61\n\t"
        "v_exp_f32 v74, v74\n\t"
        "v_exp_f32 v62, v62\n\t"
        "v_exp_f32 v75, v75\n\t"
        "v_exp_f32 v63, v63\n\t"
        "v_exp_f32 v76, v76\n\t"
        "v_exp_f32 v64, v64\n\t"
        "v_exp_f32 v78, v78\n\t"
        "v_exp_f32 v65, v65\n\t"
        "v_exp_f32 v79, v79\n\t"
        "v_exp_f32 v66, v66\n\t"
        "v_exp_f32 v80, v80\n\t"
        "v_pk_add_f32 v[0:1], v[48:49], v[50:51]\n\t"
        "v_exp_f32 v81, v81\n\t"
        "v_pk_add_f32 v[2:3], v[52:53], v[54:55]\n\t"
        "v_exp_f32 v82, v82\n\t"
        "v_pk_add_f32 v[6:7], v[58:59], v[60:61]\n\t"
        "v_exp_f32 v83, v83\n\t"
        "v_pk_add_f32 v[8:9], v[62:63], v[64:65]\n\t"
        "v_exp_f32 v84, v84\n\t"
        "v_pk_add_f32 v[0:1], v[0:1], v[2:3]\n\t"
        "v_exp_f32 v85, v85\n\t"
        "v_pk_add_f32 v[6:7], v[6:7], v[8:9]\n\t"
        "v_exp_f32 v86, v86\n\t"
        "v_add_f32 v0, v0, v1\n\t"
        "v_pk_add_f32 v[16:17], v[68:69], v[70:71]\n\t"
        "v_add_f32 v6, v6, v7\n\t"
        "v_pk_add_f32 v[18:19], v[72:73], v[74:75]\n\t"
        "v_add_f32 v98, v0, v56\n\t"
        "v_pk_add_f32 v[22:23], v[78:79], v[80:81]\n\t"
        "v_add_f32 v100, v6, v66\n\t"
        "v_pk_add_f32 v[24:25], v[82:83], v[84:85]\n\t"
        "s_nop 0\n\t"
        "v_pk_add_f32 v[16:17], v[16:17], v[18:19]\n\t"
        "v_permlane32_swap_b32 v98, v100\n\t"
        "v_pk_add_f32 v[22:23], v[22:23], v[24:25]\n\t"
        "v_add_f32 v98, v98, v100\n\t"
        "v_add_f32 v16, v16, v17\n\t"
        "v_rcp_f32 v98, v98\n\t"
        "v_add_f32 v22, v22, v23\n\t"
        "s_nop 0\n\t"
        "v_add_f32 v102, v16, v76\n\t"
        "v_mov_b32 v100, v98\n\t"
        "v_add_f32 v104, v22, v86\n\t"
        "s_nop 0\n\t"
        "v_permlane32_swap_b32 v98, v100\n\t"
        "v_permlane32_swap_b32 v102, v104\n\t"
        "v_pk_mul_f32 v[48:49], v[48:49], v[98:99] op_sel_hi:[1,0]\n\t"
        "v_add_f32 v102, v102, v104\n\t"
        "v_pk_mul_f32 v[50:51], v[50:51], v[98:99] op_sel_hi:[1,0]\n\t"
        "v_rcp_f32 v102, v102\n\t"
        "v_pk_mul_f32 v[52:53], v[52:53], v[98:99] op_sel_hi:[1,0]\n\t"
        "s_nop 0\n\t"
        "v_pk_mul_f32 v[54:55], v[54:55], v[98:99] op_sel_hi:[1,0]\n\t"
        "v_mov_b32 v104, v102\n\t"
        "v_mul_f32 v56, v56, v98\n\t"
        "s_nop 0\n\t"
        "v_pk_mul_f32 v[58:59], v[58:59], v[100:101] op_sel_hi:[1,0]\n\t"
        "v_permlane32_swap_b32 v102, v104\n\t"
        "v_pk_mul_f32 v[60:61], v[60:61], v[100:101] op_sel_hi:[1,0]\n\t"
        "v_pk_mul_f32 v[68:69], v[68:69], v[102:103] op_sel_hi:[1,0]\n\t"
        "v_pk_mul_f32 v[62:63], v[62:63], v[100:101] op_sel_hi:[1,0]\n\t"
        "v_pk_mul_f32 v[70:71], v[70:71], v[102:103] op_sel_hi:[1,0]\n\t"
        "v_pk_mul_f32 v[64:65], v[64:65], v[100:101] op_sel_hi:[1,0]\n\t"
        "v_pk_mul_f32 v[72:73], v[72:73], v[102:103] op_sel_hi:[1,0]\n\t"
        "v_mul_f32 v66, v66, v100\n\t"
        "v_pk_mul_f32 v[74:75], v[74:75], v[102:103] op_sel_hi:[1,0]\n\t"
        "v_cvt_pk_bf16_f32 v106, v48, v49\n\t"
        "v_mul_f32 v76, v76, v102\n\t"
        "v_cvt_pk_bf16_f32 v107, v50, v51\n\t"
        "v_pk_mul_f32 v[78:79], v[78:79], v[104:105] op_sel_hi:[1,0]\n\t"
        "v_cvt_pk_bf16_f32 v108, v52, v53\n\t"
        "v_pk_mul_f32 v[80:81], v[80:81], v[104:105] op_sel_hi:[1,0]\n\t"
        "v_cvt_pk_bf16_f32 v109, v54, v55\n\t"
        "v_pk_mul_f32 v[82:83], v[82:83], v[104:105] op_sel_hi:[1,0]\n\t"
        "v_cvt_pk_bf16_f32 v110, v58, v59\n\t"
        "v_pk_mul_f32 v[84:85], v[84:85], v[104:105] op_sel_hi:[1,0]\n\t"
        "v_cvt_pk_bf16_f32 v111, v60, v61\n\t"
        "v_mul_f32 v86, v86, v104\n\t"
        "v_cvt_pk_bf16_f32 v112, v62, v63\n\t"
        "v_cvt_pk_bf16_f32 v116, v68, v69\n\t"
        "v_cvt_pk_bf16_f32 v113, v64, v65\n\t"
        "v_cvt_pk_bf16_f32 v117, v70, v71\n\t"
        "v_cvt_pk_bf16_f32 v114, v56, v66\n\t"
        "v_cvt_pk_bf16_f32 v118, v72, v73\n\t"
        "v_cvt_pk_bf16_f32 v119, v74, v75\n\t"
        "ds_write_b128 v231, v[106:109] offset:0\n\t"
        "v_cvt_pk_bf16_f32 v120, v78, v79\n\t"
        "ds_write_b128 v231, v[110:113] offset:32\n\t"
        "v_cvt_pk_bf16_f32 v121, v80, v81\n\t"
        "ds_write_b32 v232, v114 offset:0\n\t"
        "v_cvt_pk_bf16_f32 v122, v82, v83\n\t"
        "v_cvt_pk_bf16_f32 v123, v84, v85\n\t"
        "v_cvt_pk_bf16_f32 v124, v76, v86\n\t"
        "ds_write_b128 v231, v[116:119] offset:11264\n\t"
        "ds_write_b128 v231, v[120:123] offset:11296\n\t"
        "ds_write_b32 v232, v124 offset:11264\n\t"
        "s_cmp_lt_u32 %[w], 4\n\t"
        "s_cbranch_scc0 72f\n\t"
        "v_mov_b32 v126, 0x3f803f80\n\t"
        "s_waitcnt lgkmcnt(3)\n\t"
        "ds_read_b64_tr_b16 v[12:13], v236 offset:0\n\t"
        "ds_read_b64_tr_b16 v[14:15], v236 offset:4736\n\t"
        "ds_read_b64_tr_b16 v[0:1], v235 offset:0\n\t"
        "ds_read_b64_tr_b16 v[2:3], v235 offset:5632\n\t"
        "ds_read_b64_tr_b16 v[16:17], v236 offset:32\n\t"
        "ds_read_b64_tr_b16 v[18:19], v236 offset:4768\n\t"
        "ds_read_b64_tr_b16 v[4:5], v235 offset:32\n\t"
        "ds_read_b64_tr_b16 v[6:7], v235 offset:5664\n\t"
        "ds_read_b64_tr_b16 v[8:9], v235 offset:64\n\t"
        "ds_read_b64_tr_b16 v[10:11], v235 offset:5696\n\t"
        "s_waitcnt lgkmcnt(10)\n\t"
        "ds_read_b64_tr_b16 v[32:33], v236 offset:9472\n\t"
        "ds_read_b64_tr_b16 v[34:35], v236 offset:14208\n\t"
        "ds_read_b64_tr_b16 v[20:21], v235 offset:11264\n\t"
        "ds_read_b64_tr_b16 v[22:23], v235 offset:16896\n\t"
        "ds_read_b64_tr_b16 v[36:37], v236 offset:9504\n\t"
        "s_waitcnt lgkmcnt(5)\n\t"
        "v_cndmask_b32 v16, v16, v126, s[48:49]\n\t"
        "v_cndmask_b32 v17, v17, v126, s[48:49]\n\t"
        "v_cndmask_b32 v18, v18, v126, s[48:49]\n\t"
        "v_cndmask_b32 v19, v19, v126, s[48:49]\n\t"
        "v_mfma_f32_16x16x32_bf16 v[80:83], v[0:3], v[12:15], 0\n\t"
        "ds_read_b64_tr_b16 v[38:39], v236 offset:14240\n\t"
        "v_mfma_f32_16x16x32_bf16 v[88:91], v[4:7], v[12:15], 0\n\t"
        "ds_read_b64_tr_b16 v[24:25], v235 offset:11296\n\t"
        "v_mfma_f32_16x16x32_bf16 v[96:99], v[8:11], v[12:15], 0\n\t"
        "ds_read_b64_tr_b16 v[26:27], v235 offset:16928\n\t"
        "v_mfma_f32_16x16x32_bf16 v[84:87], v[0:3], v[16:19], 0\n\t"
        "ds_read_b64_tr_b16 v[28:29], v235 offset:11328\n\t"
        "v_mfma_f32_16x16x32_bf16 v[92:95], v[4:7], v[16:19], 0\n\t"
        "ds_read_b64_tr_b16 v[30:31], v235 offset:16960\n\t"
        "v_mfma_f32_16x16x32_bf16 v[100:103], v[8:11], v[16:19], 0\n\t"
        "s_waitcnt lgkmcnt(0)\n\t"
        "v_cndmask_b32 v36, v36, v126, s[48:49]\n\t"
        "v_cndmask_b32 v37, v37, v126, s[48:49]\n\t"
        "v_cndmask_b32 v38, v38, v126, s[48:49]\n\t"
        "v_cndmask_b32 v39, v39, v126, s[48:49]\n\t"
        "v_mfma_f32_16x16x32_bf16 v[80:83], v[20:23], v[32:35], v[80:83]\n\t"
        "ds_read_b64_tr_b16 v[52:53], v236 offset:18944\n\t"
        "v_mfma_f32_16x16x32_bf16 v[88:91], v[24:27], v[32:35], v[88:91]\n\t"
        "ds_read_b64_tr_b16 v[54:55], v236 offset:23680\n\t"
        "v_mfma_f32_16x16x32_bf16 v[96:99], v[28:31], v[32:35], v[96:99]\n\t"
        "ds_read_b64_tr_b16 v[56:57], v236 offset:18976\n\t"
        "v_mfma_f32_16x16x32_bf16 v[84:87], v[20:23], v[36:39], v[84:87]\n\t"
        "ds_read_b64_tr_b16 v[58:59], v236 offset:23712\n\t"
        "v_mfma_f32_16x16x32_bf16 v[92:95], v[24:27], v[36:39], v[92:95]\n\t"
        "ds_read_b64_tr_b16 v[72:73], v236 offset:28416\n\t"
        "v_mfma_f32_16x16x32_bf16 v[100:103], v[28:31], v[36:39], v[100:103]\n\t"
        "ds_read_b64_tr_b16 v[74:75], v236 offset:33152\n\t"
        "ds_read_b64_tr_b16 v[76:77], v236 offset:28448\n\t"
        "ds_read_b64_tr_b16 v[78:79], v236 offset:33184\n\t"
        "s_waitcnt vmcnt(0)\n\t"
        "v_lshl_or_b32 v160, v161, 16, v160\n\t"
        "v_lshl_or_b32 v161, v163, 16, v162\n\t"
        "v_lshl_or_b32 v162, v165, 16, v164\n\t"
        "v_lshl_or_b32 v163, v167, 16, v166\n\t"
        "v_lshl_or_b32 v168, v169, 16, v168\n\t"
        "v_lshl_or_b32 v169, v171, 16, v170\n\t"
        "v_lshl_or_b32 v170, v173, 16, v172\n\t"
        "v_lshl_or_b32 v171, v175, 16, v174\n\t"
        "global_store_dwordx4 v216, v[160:163], %[wsb] sc1\n\t"
        "s_mov_b64 exec, %[wom]\n\t"
        "global_store_dwordx4 v217, v[168:171], %[wsb] sc1\n\t"
        "s_mov_b64 exec, -1\n\t"
        "s_waitcnt lgkmcnt(0)\n\t"
        "v_cndmask_b32 v56, v56, v126, s[48:49]\n\t"
        "v_cndmask_b32 v57, v57, v126, s[48:49]\n\t"
        "v_cndmask_b32 v58, v58, v126, s[48:49]\n\t"
        "v_cndmask_b32 v59, v59, v126, s[48:49]\n\t"
        "v_cndmask_b32 v76, v76, v126, s[48:49]\n\t"
        "v_cndmask_b32 v77, v77, v126, s[48:49]\n\t"
        "v_cndmask_b32 v78, v78, v126, s[48:49]\n\t"
        "v_cndmask_b32 v79, v79, v126, s[48:49]\n\t"
        "s_barrier\n\t"
        "ds_read_b64_tr_b16 v[40:41], v235 offset:22528\n\t"
        "ds_read_b64_tr_b16 v[42:43], v235 offset:28160\n\t"
        "ds_read_b64_tr_b16 v[44:45], v235 offset:22560\n\t"
        "ds_read_b64_tr_b16 v[46:47], v235 offset:28192\n\t"
        "ds_read_b64_tr_b16 v[48:49], v235 offset:22592\n\t"
        "ds_read_b64_tr_b16 v[50:51], v235 offset:28224\n\t"
        "ds_read_b64_tr_b16 v[60:61], v235 offset:33792\n\t"
        "ds_read_b64_tr_b16 v[62:63], v235 offset:39424\n\t"
        "ds_read_b64_tr_b16 v[64:65], v235 offset:33824\n\t"
        "ds_read_b64_tr_b16 v[66:67], v235 offset:39456\n\t"
        "ds_read_b64_tr_b16 v[68:69], v235 offset:33856\n\t"
        "ds_read_b64_tr_b16 v[70:71], v235 offset:39488\n\t"
        "s_waitcnt lgkmcnt(6)\n\t"
        "v_mfma_f32_16x16x32_bf16 v[80:83], v[40:43], v[52:55], v[80:83]\n\t"
        "v_mfma_f32_16x16x32_bf16 v[88:91], v[44:47], v[52:55], v[88:91]\n\t"
        "v_mfma_f32_16x16x32_bf16 v[96:99], v[48:51], v[52:55], v[96:99]\n\t"
        "v_mfma_f32_16x16x32_bf16 v[84:87], v[40:43], v[56:59], v[84:87]\n\t"
        "v_mfma_f32_16x16x32_bf16 v[92:95], v[44:47], v[56:59], v[92:95]\n\t"
        "v_mfma_f32_16x16x32_bf16 v[100:103], v[48:51], v[56:59], v[100:103]\n\t"
        "s_waitcnt lgkmcnt(0)\n\t"
        "v_mfma_f32_16x16x32_bf16 v[80:83], v[60:63], v[72:75], v[80:83]\n\t"
        "v_mfma_f32_16x16x32_bf16 v[88:91], v[64:67], v[72:75], v[88:91]\n\t"
        "v_mfma_f32_16x16x32_bf16 v[96:99], v[68:71], v[72:75], v[96:99]\n\t"
        "v_mfma_f32_16x16x32_bf16 v[84:87], v[60:63], v[76:79], v[84:87]\n\t"
        "v_mfma_f32_16x16x32_bf16 v[92:95], v[64:67], v[76:79], v[92:95]\n\t"
        "v_mfma_f32_16x16x32_bf16 v[100:103], v[68:71], v[76:79], v[100:103]\n\t"
        "s_nop 7\n\t"
        "s_mov_b64 exec, s[44:45]\n\t"
        "global_store_dwordx4 v237, v[80:83], %[kvp] offset:-128 sc1\n\t"
        "global_store_dwordx4 v237, v[88:91], %[kvp] offset:1088 sc1\n\t"
        "s_mov_b64 exec, s[46:47]\n\t"
        "global_store_dwordx4 v237, v[84:87], %[kvp] offset:896 sc1\n\t"
        "global_store_dwordx4 v237, v[92:95], %[kvp] offset:2112 sc1\n\t"
        "s_and_b32 s44, s44, 0xffff\n\t"
        "s_mov_b32 s45, 0\n\t"
        "s_and_b32 s46, s46, 0xffff\n\t"
        "s_mov_b32 s47, 0\n\t"
        "s_mov_b64 exec, s[44:45]\n\t"
        "global_store_dwordx4 v238, v[96:99], %[kvp] offset:2400 sc1\n\t"
        "s_mov_b64 exec, s[46:47]\n\t"
        "global_store_dwordx4 v238, v[100:103], %[kvp] offset:2656 sc1\n\t"
        "s_mov_b64 exec, -1\n\t"
        "s_branch 79f\n\t"
        "72:\n\t"
        "s_waitcnt lgkmcnt(0)\n\t"
        "s_barrier\n\t"
        "79:\n\t"
            :
            : "{v244}"(lane), "{v[184:187]}"(PA0), "{v[188:191]}"(PA1), "{v[228:231]}"(EP0), "{v[232:235]}"(EP1), "{v[236:239]}"(EP2), "{v[246:249]}"(EZ0), "{v[250:253]}"(EZ1),
              "{v[160:175]}"(WO), "{v[216:217]}"(V6), [wsb] "s"(wsb), [wom] "s"(wom), [w] "s"(w), [isq] "s"(isq), [pqb] "s"(pqb), [qmb] "s"(qmb), [qtb] "s"(qtb), [kvp] "s"(kvp)
            : "v0", "v1", "v2", "v3", "v4", "v5", "v6", "v7", "v8", "v9", "v10", "v11", "v12", "v13", "v14", "v15", "v16", "v17", "v18", "v19", "v20", "v21", "v22", "v23", "v24", "v25", "v26", "v27", "v28", "v29", "v30", "v31", "v32", "v33", "v34", "v35", "v36", "v37", "v38", "v39", "v40", "v41", "v42", "v43", "v44", "v45", "v46", "v47", "v48", "v49", "v50", "v51", "v52", "v53", "v54", "v55", "v56", "v57", "v58", "v59", "v60", "v61", "v62", "v63", "v64", "v65", "v66", "v67", "v68", "v69", "v70", "v71", "v72", "v73", "v74", "v75", "v76", "v77", "v78", "v79", "v80", "v81", "v82", "v83", "v84", "v85", "v86", "v87", "v88", "v89", "v90", "v91", "v92", "v93", "v94", "v95", "v96", "v97", "v98", "v99", "v100", "v101", "v102", "v103", "v104", "v105", "v106", "v107", "v108", "v109", "v110", "v111", "v112", "v113", "v114", "v115", "v116", "v117", "v118", "v119", "v120", "v121", "v122", "v123", "v124", "v125", "v126", "v127", "s40", "s41", "s42", "s43", "s44", "s45", "s46", "s47", "s48", "s49", "s50", "s51", "s52", "s53", "s54", "s55", "s56", "s57", "s58", "s59", "s60", "s61", "s62", "s63", "scc", "vcc", "memory");

    }
    STAMP(0, 2); STAMP(0, 3); STAMP(0, 4);
}

#define K2_LDS (KV_BYTES + 2560)
__global__ __launch_bounds__(512, 2) void k2_attn(char* __restrict__ ws) {
    int b; asm volatile("s_mov_b32 %0, %1" : "=s"(b) : "s"(__builtin_amdgcn_workgroup_id_x()));
    STAMP(1, 0);

    asm volatile(
        "s_load_dwordx2 s[40:41], %[karg], 0x0\n\t"
        "v_lshrrev_b32 v226, 6, v244\n\t"
        "v_and_b32 v241, 63, v244\n\t"
        "s_mov_b32 s42, 0x2000000\n\t"
        "v_readfirstlane_b32 s44, v226\n\t"
        "s_mov_b32 s43, 0x00020000\n\t"
        "v_lshlrev_b32 v158, 4, v241\n\t"
        "v_and_b32 v240, 31, v241\n\t"
        "v_lshlrev_b32 v159, 2, v240\n\t"
        "v_mov_b32 v104, 0\n\t"
        "v_mov_b32 v106, 0\n\t"
        "s_and_b32 s47, %[b], 7\n\t"
        "s_bfe_u32 s48, %[b], 0x10003\n\t"
        "s_lshr_b32 s49, %[b], 4\n\t"
        "s_lshr_b32 s45, s44, 2\n\t"
        "s_and_b32 s51, s44, 3\n\t"
        "s_lshl_b32 s83, s47, 1\n\t"
        "s_or_b32 s83, s83, s48\n\t"
        "s_lshl_b32 s84, s83, 2\n\t"
        "s_or_b32 s84, s84, s51\n\t"
        "s_lshl_b32 s85, s49, 1\n\t"
        "s_or_b32 s85, s85, s45\n\t"
        "s_lshl_b32 s86, s85, 6\n\t"
        "s_or_b32 s86, s86, s84\n\t"
        "s_xor_b32 s87, s86, 64\n\t"
        "s_lshl_b32 s68, s87, 10\n\t"
        "s_add_u32 s68, s68, 155648\n\t"
        "s_lshl_b32 s69, s86, 10\n\t"
        "s_add_u32 s69, s69, 155648\n\t"
        "s_lshl_b32 s70, s87, 7\n\t"
        "s_add_u32 s70, s70, 2252800\n\t"
        "s_lshl_b32 s71, s86, 7\n\t"
        "s_add_u32 s71, s71, 2252800\n\t"
        "s_lshl_b32 s72, s45, 10\n\t"
        "s_lshl_b32 s73, s45, 7\n\t"
        "s_add_u32 s73, s73, 2048\n\t"
        "s_mov_b32 s53, 0\n\t"
        "s_waitcnt lgkmcnt(0)\n\t"
        "5:\n\t"
        "s_lshl_b32 s52, s44, 10\n\t"
        "buffer_load_dwordx4 v[96:99], v158, s[40:43], s68 offen\n\t"
        "buffer_load_dwordx4 v[100:103], v158, s[40:43], s69 offen\n\t"
        "buffer_load_dwordx4 v[116:119], v158, s[40:43], 0 offen\n\t"
        "buffer_load_dword v122, v159, s[40:43], 0 offen offset:2048\n\t"
        "s_mov_b32 exec_hi, 0\n\t"
        "buffer_load_dword v104, v159, s[40:43], s70 offen\n\t"
        "buffer_load_dword v106, v159, s[40:43], s71 offen\n\t"
        "s_mov_b32 exec_hi, -1\n\t"
        "s_cmp_eq_u32 s45, 0\n\t"
        "s_cbranch_scc1 6f\n\t"
        "buffer_load_dwordx4 v[112:115], v158, s[40:43], s72 offen\n\t"
        "buffer_load_dword v120, v159, s[40:43], s73 offen\n\t"
        "6:\n\t"
        "v_mov_b32 v105, 0\n\t"
        "v_mov_b32 v107, 0\n\t"
        "v_mov_b32 v121, 0x3f803f80\n\t"
        "v_mov_b32 v123, 0x3f803f80\n\t"
        "v_mov_b32 v224, 0xff800000\n\t"
        "v_mov_b32 v225, 0xff800000\n\t"
        "v_mov_b32 v48, 0\n\t"
        "v_mov_b32 v49, 0\n\t"
        "v_mov_b32 v50, 0\n\t"
        "v_mov_b32 v51, 0\n\t"
        "v_mov_b32 v52, 0\n\t"
        "v_mov_b32 v53, 0\n\t"
        "v_mov_b32 v54, 0\n\t"
        "v_mov_b32 v55, 0\n\t"
        "v_mov_b32 v56, 0\n\t"
        "v_mov_b32 v57, 0\n\t"
        "v_mov_b32 v58, 0\n\t"
        "v_mov_b32 v59, 0\n\t"
        "v_mov_b32 v60, 0\n\t"
        "v_mov_b32 v61, 0\n\t"
        "v_mov_b32 v62, 0\n\t"
        "v_mov_b32 v63, 0\n\t"
        "v_mov_b32 v128, 0\n\t"
        "v_mov_b32 v129, 0\n\t"
        "v_mov_b32 v130, 0\n\t"
        "v_mov_b32 v131, 0\n\t"
        "v_mov_b32 v132, 0\n\t"
        "v_mov_b32 v133, 0\n\t"
        "v_mov_b32 v134, 0\n\t"
        "v_mov_b32 v135, 0\n\t"
        "v_mov_b32 v136, 0\n\t"
        "v_mov_b32 v137, 0\n\t"
        "v_mov_b32 v138, 0\n\t"
        "v_mov_b32 v139, 0\n\t"
        "v_mov_b32 v140, 0\n\t"
        "v_mov_b32 v141, 0\n\t"
        "v_mov_b32 v142, 0\n\t"
        "v_mov_b32 v143, 0\n\t"
        "v_mov_b32 v64, 0\n\t"
        "v_mov_b32 v65, 0\n\t"
        "v_mov_b32 v66, 0\n\t"
        "v_mov_b32 v67, 0\n\t"
        "v_mov_b32 v68, 0\n\t"
        "v_mov_b32 v69, 0\n\t"
        "v_mov_b32 v70, 0\n\t"
        "v_mov_b32 v71, 0\n\t"
        "v_mov_b32 v72, 0\n\t"
        "v_mov_b32 v73, 0\n\t"
        "v_mov_b32 v74, 0\n\t"
        "v_mov_b32 v75, 0\n\t"
        "v_mov_b32 v76, 0\n\t"
        "v_mov_b32 v77, 0\n\t"
        "v_mov_b32 v78, 0\n\t"
        "v_mov_b32 v79, 0\n\t"
        "v_mov_b32 v80, 0\n\t"
        "v_mov_b32 v81, 0\n\t"
        "v_mov_b32 v82, 0\n\t"
        "v_mov_b32 v83, 0\n\t"
        "v_mov_b32 v84, 0\n\t"
        "v_mov_b32 v85, 0\n\t"
        "v_mov_b32 v86, 0\n\t"
        "v_mov_b32 v87, 0\n\t"
        "v_mov_b32 v88, 0\n\t"
        "v_mov_b32 v89, 0\n\t"
        "v_mov_b32 v90, 0\n\t"
        "v_mov_b32 v91, 0\n\t"
        "v_mov_b32 v92, 0\n\t"
        "v_mov_b32 v93, 0\n\t"
        "v_mov_b32 v94, 0\n\t"
        "v_mov_b32 v95, 0\n\t"
        "v_mov_b32 v144, 0xdddddddd\n\t"
        "s_mov_b32 m0, s52\n\t"
        "s_nop 0\n\t"
        "buffer_load_dwordx4 v158, s[40:43], s52 offen lds\n\t"
        "s_add_u32 s52, s52, 8192\n\t"
        "s_mov_b32 m0, s52\n\t"
        "s_nop 0\n\t"
        "buffer_load_dwordx4 v158, s[40:43], s52 offen lds\n\t"
        "s_add_u32 s52, s52, 8192\n\t"
        "s_mov_b32 m0, s52\n\t"
        "s_nop 0\n\t"
        "buffer_load_dwordx4 v158, s[40:43], s52 offen lds\n\t"
        "s_add_u32 s52, s52, 8192\n\t"
        "s_cmp_eq_u32 s53, 0\n\t"
        "s_cbranch_scc1 11f\n\t"
        "s_mov_b32 s46, 16\n\t"
        "10:\n\t"
        "s_mov_b32 m0, s52\n\t"
        "s_nop 0\n\t"
        "buffer_load_dwordx4 v158, s[40:43], s52 offen lds\n\t"
        "s_add_u32 s52, s52, 8192\n\t"
        "s_sub_u32 s46, s46, 1\n\t"
        "s_cmp_lg_u32 s46, 0\n\t"
        "s_cbranch_scc1 10b\n\t"
        "s_lshl_b32 s52, s44, 10\n\t"
        "s_add_u32 s52, s52, 24576\n\t"
        "11:\n\t"
        "s_mov_b32 s54, 0x0007ffff\n\t"
        "s_mov_b32 s55, 0x0007ffff\n\t"
        "s_lshl_b32 s88, s45, 12\n\t"
        "s_add_u32 s88, s88, 2304\n\t"
        "v_add_u32 v145, s88, v158\n\t"
        "s_lshl_b32 s88, s45, 9\n\t"
        "s_add_u32 s88, s88, 20224\n\t"
        "v_add_u32 v146, s88, v159\n\t"
        "s_mul_i32 s88, s45, 4864\n\t"
        "s_add_u32 s88, s88, 10496\n\t"
        "v_lshrrev_b32 v227, 5, v241\n\t"
        "v_mul_u32_u24 v228, 304, v227\n\t"
        "v_lshl_add_u32 v228, v240, 4, v228\n\t"
        "v_add_u32 v147, s88, v228\n\t"
        "v_lshrrev_b32 v228, 4, v240\n\t"
        "v_mul_u32_u24 v228, 36864, v228\n\t"
        "v_and_b32 v229, 15, v240\n\t"
        "v_lshl_add_u32 v228, v229, 4, v228\n\t"
        "v_lshl_add_u32 v232, v227, 8, v228\n\t"
        "s_mul_i32 s88, s44, 3072\n\t"
        "v_add_u32 v233, s88, v158\n\t"
        "s_xor_b32 s88, s44, 4\n\t"
        "s_mul_i32 s88, s88, 3072\n\t"
        "v_add_u32 v234, s88, v158\n\t"
        "v_lshlrev_b32 v228, 5, v240\n\t"
        "v_lshl_add_u32 v228, v227, 4, v228\n\t"
        "v_add_u32 v235, 155648, v228\n\t"
        "v_lshlrev_b32 v228, 3, v240\n\t"
        "v_lshl_add_u32 v228, v227, 2, v228\n\t"
        "v_add_u32 v236, 157696, v228\n\t"
        "v_mov_b32 v237, 157952\n\t"
        "v_mov_b32 v185, 0\n\t"
        "v_mov_b32 v157, 0\n\t"
        "v_and_b32 v226, 255, v244\n\t"
        "v_cmp_le_u32 vcc, 76, v226\n\t"
        "v_lshlrev_b32 v231, 4, v226\n\t"
        "v_subrev_u32 v229, 152, v226\n\t"
        "v_cndmask_b32 v227, 0, 1, vcc\n\t"
        "v_cmp_le_u32 vcc, 152, v226\n\t"
        "v_mul_u32_u24 v228, 76, v227\n\t"
        "v_sub_u32 v228, v226, v228\n\t"
        "v_lshlrev_b32 v227, 10, v227\n\t"
        "v_and_b32 v230, 3, v228\n\t"
        "v_lshl_add_u32 v227, v230, 3, v227\n\t"
        "v_lshrrev_b32 v228, 2, v228\n\t"
        "v_cndmask_b32 v228, v228, v229, vcc\n\t"
        "v_and_b32 v229, 4, v228\n\t"
        "v_and_b32 v230, 8, v228\n\t"
        "v_lshrrev_b32 v230, 1, v230\n\t"
        "v_lshl_or_b32 v229, v229, 1, v230\n\t"
        "v_and_b32 v228, 19, v228\n\t"
        "v_or_b32 v228, v228, v229\n\t"
        "v_lshl_add_u32 v227, v228, 5, v227\n\t"
        "v_lshlrev_b32 v228, 3, v228\n\t"
        "v_add_u32 v228, 2048, v228\n\t"
        "v_cndmask_b32 v227, v227, v228, vcc\n\t"
        "v_add_u32 v230, 155648, v227\n\t"
        "s_lshl_b32 s88, s49, 1\n\t"
        "s_or_b32 s88, s88, s48\n\t"
        "s_mul_i32 s74, s88, 43776\n\t"
        "s_add_u32 s74, s74, 7233536\n\t"
        "s_lshl_b32 s89, s47, 3\n\t"
        "s_add_u32 s89, s89, s44\n\t"
        "s_lshl_b32 s75, s88, 6\n\t"
        "s_add_u32 s75, s75, s89\n\t"
        "s_mul_i32 s75, s75, 2304\n\t"
        "s_add_u32 s75, s75, 2514944\n\t"
        "s_mul_i32 s76, s89, 73728\n\t"
        "s_lshr_b32 s90, s88, 2\n\t"
        "s_mul_i32 s90, s90, 2304\n\t"
        "s_add_u32 s76, s76, s90\n\t"
        "s_add_u32 s76, s76, 9969664\n\t"
        "s_and_b32 s90, s88, 3\n\t"
        "s_lshl_b32 s91, s90, 2\n\t"
        "s_add_u32 s77, s76, s91\n\t"
        "s_add_u32 s77, s77, 2048\n\t"
        "s_lshl_b32 s91, s90, 9\n\t"
        "s_add_u32 s76, s76, s91\n\t"
        "s_mul_i32 s78, s84, 73728\n\t"
        "s_lshr_b32 s90, s85, 2\n\t"
        "s_mul_i32 s90, s90, 2304\n\t"
        "s_add_u32 s78, s78, s90\n\t"
        "s_add_u32 s78, s78, 9988096\n\t"
        "s_and_b32 s90, s85, 3\n\t"
        "s_lshl_b32 s91, s90, 2\n\t"
        "s_add_u32 s79, s78, s91\n\t"
        "s_add_u32 s79, s79, 2048\n\t"
        "s_lshl_b32 s91, s90, 9\n\t"
        "s_add_u32 s78, s78, s91\n\t"
        "s_cmp_eq_u32 s51, 2\n\t"
        "s_cselect_b32 s80, -1, -1\n\t"
        "s_cselect_b32 s81, 0x7ff, -1\n\t"
        "s_lshr_b32 s88, %[b], 3\n\t"
        "s_lshl_b32 s88, s88, 3\n\t"
        "s_add_u32 s88, s88, s44\n\t"
        "s_mul_i32 s82, s88, 5248\n\t"
        "v_min_u32 v238, 40, v241\n\t"
        "v_lshlrev_b32 v238, 7, v238\n\t"
        "v_add_u32 v238, s82, v238\n\t"
        "v_min_u32 v238, 1326976, v238\n\t"
        "s_mov_b32 s82, 8642560\n\t"
        "s_cmp_lg_u32 s53, 0\n\t"
        "s_cbranch_scc1 13f\n\t"
        "s_cmp_lg_u32 s44, 0\n\t"
        "s_cbranch_scc1 12f\n\t"
        "v_mov_b32 v228, 0\n\t"
        "v_mov_b32 v229, 0\n\t"
        "ds_write_b64 v237, v[228:229]\n\t"
        "12:\n\t"
        "s_waitcnt vmcnt(3)\n\t"
        "v_mfma_f32_32x32x16_bf16 v[0:15], v[116:119], v[96:99], 0\n\t"
        "v_mfma_f32_32x32x16_bf16 v[16:31], v[116:119], v[100:103], 0\n\t"
        "v_mfma_f32_32x32x8_bf16 v[0:15], v[122:123], v[104:105], v[0:15]\n\t"
        "v_mfma_f32_32x32x8_bf16 v[16:31], v[122:123], v[106:107], v[16:31]\n\t"
        "s_nop 10\n\t"
        "v_max3_f32 v224, v0, v1, v224\n\t"
        "v_max3_f32 v224, v2, v3, v224\n\t"
        "v_max3_f32 v224, v4, v5, v224\n\t"
        "v_max3_f32 v224, v6, v7, v224\n\t"
        "v_max3_f32 v224, v8, v9, v224\n\t"
        "v_max3_f32 v224, v10, v11, v224\n\t"
        "v_max3_f32 v224, v12, v13, v224\n\t"
        "v_max3_f32 v224, v14, v15, v224\n\t"
        "v_max3_f32 v225, v16, v17, v225\n\t"
        "v_max3_f32 v225, v18, v19, v225\n\t"
        "v_max3_f32 v225, v20, v21, v225\n\t"
        "v_max3_f32 v225, v22, v23, v225\n\t"
        "v_max3_f32 v225, v24, v25, v225\n\t"
        "v_max3_f32 v225, v26, v27, v225\n\t"
        "v_max3_f32 v225, v28, v29, v225\n\t"
        "v_max3_f32 v225, v30, v31, v225\n\t"
        "s_branch 18f\n\t"
        "13:\n\t"
        "s_mov_b32 s46, 0\n\t"
        "s_waitcnt vmcnt(0) lgkmcnt(0)\n\t"
        "s_barrier\n\t"
        "s_mov_b32 s83, 0\n\t"
        "s_mov_b32 s84, 2048\n\t"
        "14:\n\t"
        "v_add_u32 v228, s83, v158\n\t"
        "v_add_u32 v229, s84, v159\n\t"
        "ds_read_b128 v[116:119], v228\n\t"
        "ds_read_b32 v122, v229\n\t"
        "s_add_u32 s46, s46, 1\n\t"
        "s_waitcnt lgkmcnt(0)\n\t"
        "v_mfma_f32_32x32x16_bf16 v[0:15], v[116:119], v[96:99], 0\n\t"
        "v_mfma_f32_32x32x16_bf16 v[16:31], v[116:119], v[100:103], 0\n\t"
        "v_mfma_f32_32x32x8_bf16 v[0:15], v[122:123], v[104:105], v[0:15]\n\t"
        "v_mfma_f32_32x32x8_bf16 v[16:31], v[122:123], v[106:107], v[16:31]\n\t"
        "s_lshr_b32 s85, s46, 5\n\t"
        "s_and_b32 s86, s46, 31\n\t"
        "s_lshl_b32 s83, s85, 10\n\t"
        "s_lshl_b32 s84, s85, 7\n\t"
        "s_add_u32 s84, s84, 2048\n\t"
        "s_cmp_eq_u32 s86, 0\n\t"
        "s_cbranch_scc1 17f\n\t"
        "s_sub_u32 s86, s86, 1\n\t"
        "s_lshr_b32 s87, s86, 2\n\t"
        "s_mul_i32 s87, s87, 18944\n\t"
        "s_add_u32 s87, s87, 2304\n\t"
        "s_and_b32 s86, s86, 3\n\t"
        "s_lshl_b32 s83, s85, 12\n\t"
        "s_add_u32 s83, s83, s87\n\t"
        "s_lshl_b32 s88, s86, 10\n\t"
        "s_add_u32 s83, s83, s88\n\t"
        "s_lshl_b32 s84, s85, 9\n\t"
        "s_add_u32 s84, s84, s87\n\t"
        "s_lshl_b32 s88, s86, 7\n\t"
        "s_add_u32 s84, s84, s88\n\t"
        "s_add_u32 s84, s84, 17920\n\t"
        "17:\n\t"
        "s_nop 13\n\t"
        "v_max3_f32 v224, v0, v1, v224\n\t"
        "v_max3_f32 v225, v16, v17, v225\n\t"
        "v_max3_f32 v224, v2, v3, v224\n\t"
        "v_max3_f32 v225, v18, v19, v225\n\t"
        "v_max3_f32 v224, v4, v5, v224\n\t"
        "v_max3_f32 v225, v20, v21, v225\n\t"
        "v_max3_f32 v224, v6, v7, v224\n\t"
        "v_max3_f32 v225, v22, v23, v225\n\t"
        "v_max3_f32 v224, v8, v9, v224\n\t"
        "v_max3_f32 v225, v24, v25, v225\n\t"
        "v_max3_f32 v224, v10, v11, v224\n\t"
        "v_max3_f32 v225, v26, v27, v225\n\t"
        "v_max3_f32 v224, v12, v13, v224\n\t"
        "v_max3_f32 v225, v28, v29, v225\n\t"
        "v_max3_f32 v224, v14, v15, v224\n\t"
        "v_max3_f32 v225, v30, v31, v225\n\t"
        "s_cmp_lg_u32 s46, 64\n\t"
        "s_cbranch_scc1 14b\n\t"
        "18:\n\t"
        "s_cmp_lg_u32 s45, 0\n\t"
        "s_cbranch_scc1 19f\n\t"
        "s_cmp_lg_u32 s53, 0\n\t"
        "s_cbranch_scc1 7f\n\t"
        "v_mov_b32 v112, v116\n\t"
        "v_mov_b32 v113, v117\n\t"
        "v_mov_b32 v114, v118\n\t"
        "v_mov_b32 v115, v119\n\t"
        "v_mov_b32 v120, v122\n\t"
        "s_branch 19f\n\t"
        "7:\n\t"
        "ds_read_b128 v[112:115], v158\n\t"
        "ds_read_b32 v120, v159 offset:2048\n\t"
        "s_waitcnt lgkmcnt(0)\n\t"
        "19:\n\t"
        "v_mov_b32 v226, v224\n\t"
        "v_mov_b32 v227, v225\n\t"
        "s_mov_b32 s46, 0x07060302\n\t"
        "s_nop 0\n\t"
        "v_permlane32_swap_b32 v224, v226\n\t"
        "v_permlane32_swap_b32 v225, v227\n\t"
        "s_nop 0\n\t"
        "v_max_f32 v224, v224, v226\n\t"
        "v_max_f32 v225, v225, v227\n\t"
        "v_xor_b32 v224, 0x80000000, v224\n\t"
        "v_xor_b32 v225, 0x80000000, v225\n\t"
        "v_and_b32 v226, 0xffff0000, v224\n\t"
        "v_and_b32 v227, 0xffff0000, v225\n\t"
        "v_sub_f32 v224, v224, v226\n\t"
        "v_sub_f32 v225, v225, v227\n\t"
        "v_and_b32 v228, 0xffff0000, v224\n\t"
        "v_and_b32 v229, 0xffff0000, v225\n\t"
        "v_sub_f32 v224, v224, v228\n\t"
        "v_sub_f32 v225, v225, v229\n\t"
        "v_lshrrev_b32 v105, 16, v224\n\t"
        "v_lshrrev_b32 v107, 16, v225\n\t"
        "s_mov_b32 exec_hi, 0\n\t"
        "v_perm_b32 v105, v228, v226, s46\n\t"
        "v_perm_b32 v107, v229, v227, s46\n\t"
        "s_mov_b32 exec_hi, -1\n\t"
        "s_mov_b32 s50, -1\n\t"
        "s_cmp_eq_u32 s45, 0\n\t"
        "s_cbranch_scc1 15f\n\t"
        "s_setprio 2\n\t"
        "s_mov_b32 s50, 3\n\t"
        "s_branch 16f\n\t"
        "15:\n\t"
        "s_setprio 1\n\t"
        "16:\n\t"
        "v_mfma_f32_32x32x16_bf16 v[0:15], v[112:115], v[96:99], 0\n\t"
        "v_mfma_f32_32x32x8_bf16 v[0:15], v[120:121], v[104:105], v[0:15]\n\t"
        "s_mov_b32 s46, 8\n\t"
        "s_waitcnt vmcnt(0) lgkmcnt(0)\n\t"
        "s_barrier\n\t"
        "s_nop 7\n\t"
        "20:\n\t"
        "v_mfma_f32_32x32x16_bf16 v[16:31], v[112:115], v[100:103], 0\n\t"
        "s_mov_b32 m0, s52\n\t"
        "v_exp_f32 v0, v0\n\t"
        "v_exp_f32 v1, v1\n\t"
        "ds_read_b128 v[116:119], v145 offset:0\n\t"
        "ds_read_b32 v122, v146 offset:0\n\t"
        "v_exp_f32 v2, v2\n\t"
        "v_exp_f32 v3, v3\n\t"
        "v_mfma_f32_32x32x8_bf16 v[16:31], v[120:121], v[106:107], v[16:31]\n\t"
        "buffer_load_dwordx4 v158, s[40:43], s52 offen lds\n\t"
        "s_add_u32 s52, s52, 8192\n\t"
        "v_exp_f32 v4, v4\n\t"
        "v_exp_f32 v5, v5\n\t"
        "s_mov_b64 exec, s[54:55]\n\t"
        "ds_read_b128 v[128:131], v147 offset:0\n\t"
        "ds_read_b128 v[132:135], v147 offset:608\n\t"
        "s_mov_b64 exec, -1\n\t"
        "v_exp_f32 v6, v6\n\t"
        "v_exp_f32 v7, v7\n\t"
        "v_smfmac_f32_32x32x32_bf16 v[80:95], v[136:139], v[48:55], v144\n\t"
        "v_exp_f32 v8, v8\n\t"
        "v_exp_f32 v9, v9\n\t"
        "v_exp_f32 v10, v10\n\t"
        "v_exp_f32 v11, v11\n\t"
        "v_smfmac_f32_32x32x32_bf16 v[80:95], v[140:143], v[56:63], v144\n\t"
        "v_exp_f32 v12, v12\n\t"
        "v_exp_f32 v13, v13\n\t"
        "v_exp_f32 v14, v14\n\t"
        "v_exp_f32 v15, v15\n\t"
        "s_waitcnt lgkmcnt(2)\n\t"
        "v_mfma_f32_32x32x16_bf16 v[32:47], v[116:119], v[96:99], 0\n\t"
        "s_mov_b32 m0, s52\n\t"
        "v_exp_f32 v16, v16\n\t"
        "v_exp_f32 v17, v17\n\t"
        "v_exp_f32 v18, v18\n\t"
        "v_exp_f32 v19, v19\n\t"
        "v_mfma_f32_32x32x8_bf16 v[32:47], v[122:123], v[104:105], v[32:47]\n\t"
        "buffer_load_dwordx4 v158, s[40:43], s52 offen lds\n\t"
        "s_add_u32 s52, s52, 8192\n\t"
        "v_exp_f32 v20, v20\n\t"
        "v_exp_f32 v21, v21\n\t"
        "v_exp_f32 v22, v22\n\t"
        "v_exp_f32 v23, v23\n\t"
        "s_waitcnt lgkmcnt(0)\n\t"
        "v_smfmac_f32_32x32x32_bf16 v[64:79], v[128:131], v[0:7], v144\n\t"
        "v_exp_f32 v24, v24\n\t"
        "v_exp_f32 v25, v25\n\t"
        "v_exp_f32 v26, v26\n\t"
        "v_exp_f32 v27, v27\n\t"
        "v_smfmac_f32_32x32x32_bf16 v[64:79], v[132:135], v[8:15], v144\n\t"
        "v_exp_f32 v28, v28\n\t"
        "v_exp_f32 v29, v29\n\t"
        "v_exp_f32 v30, v30\n\t"
        "v_exp_f32 v31, v31\n\t"
        "v_mfma_f32_32x32x16_bf16 v[48:63], v[116:119], v[100:103], 0\n\t"
        "s_mov_b32 m0, s52\n\t"
        "v_exp_f32 v32, v32\n\t"
        "v_exp_f32 v33, v33\n\t"
        "ds_read_b128 v[112:115], v145 offset:1024\n\t"
        "ds_read_b32 v120, v146 offset:128\n\t"
        "v_exp_f32 v34, v34\n\t"
        "v_exp_f32 v35, v35\n\t"
        "v_mfma_f32_32x32x8_bf16 v[48:63], v[122:123], v[106:107], v[48:63]\n\t"
        "buffer_load_dwordx4 v158, s[40:43], s52 offen lds\n\t"
        "s_add_u32 s52, s52, 8192\n\t"
        "v_exp_f32 v36, v36\n\t"
        "v_exp_f32 v37, v37\n\t"
        "s_mov_b64 exec, s[54:55]\n\t"
        "ds_read_b128 v[136:139], v147 offset:1216\n\t"
        "ds_read_b128 v[140:143], v147 offset:1824\n\t"
        "s_mov_b64 exec, -1\n\t"
        "v_exp_f32 v38, v38\n\t"
        "v_exp_f32 v39, v39\n\t"
        "v_smfmac_f32_32x32x32_bf16 v[80:95], v[128:131], v[16:23], v144\n\t"
        "v_exp_f32 v40, v40\n\t"
        "v_exp_f32 v41, v41\n\t"
        "v_exp_f32 v42, v42\n\t"
        "v_exp_f32 v43, v43\n\t"
        "v_smfmac_f32_32x32x32_bf16 v[80:95], v[132:135], v[24:31], v144\n\t"
        "v_exp_f32 v44, v44\n\t"
        "v_exp_f32 v45, v45\n\t"
        "v_exp_f32 v46, v46\n\t"
        "v_exp_f32 v47, v47\n\t"
        "s_waitcnt lgkmcnt(2)\n\t"
        "v_mfma_f32_32x32x16_bf16 v[0:15], v[112:115], v[96:99], 0\n\t"
        "s_mov_b32 m0, s52\n\t"
        "v_exp_f32 v48, v48\n\t"
        "v_exp_f32 v49, v49\n\t"
        "v_exp_f32 v50, v50\n\t"
        "v_exp_f32 v51, v51\n\t"
        "v_mfma_f32_32x32x8_bf16 v[0:15], v[120:121], v[104:105], v[0:15]\n\t"
        "buffer_load_dwordx4 v158, s[40:43], s52 offen lds\n\t"
        "s_add_u32 s52, s52, 8192\n\t"
        "v_exp_f32 v52, v52\n\t"
        "v_exp_f32 v53, v53\n\t"
        "v_exp_f32 v54, v54\n\t"
        "v_exp_f32 v55, v55\n\t"
        "s_waitcnt lgkmcnt(0)\n\t"
        "v_smfmac_f32_32x32x32_bf16 v[64:79], v[136:139], v[32:39], v144\n\t"
        "v_exp_f32 v56, v56\n\t"
        "v_exp_f32 v57, v57\n\t"
        "v_exp_f32 v58, v58\n\t"
        "v_exp_f32 v59, v59\n\t"
        "v_smfmac_f32_32x32x32_bf16 v[64:79], v[140:143], v[40:47], v144\n\t"
        "v_exp_f32 v60, v60\n\t"
        "v_exp_f32 v61, v61\n\t"
        "v_exp_f32 v62, v62\n\t"
        "v_exp_f32 v63, v63\n\t"
        "v_mfma_f32_32x32x16_bf16 v[16:31], v[112:115], v[100:103], 0\n\t"
        "s_mov_b32 m0, s52\n\t"
        "v_exp_f32 v0, v0\n\t"
        "v_exp_f32 v1, v1\n\t"
        "ds_read_b128 v[116:119], v145 offset:2048\n\t"
        "ds_read_b32 v122, v146 offset:256\n\t"
        "v_exp_f32 v2, v2\n\t"
        "v_exp_f32 v3, v3\n\t"
        "v_mfma_f32_32x32x8_bf16 v[16:31], v[120:121], v[106:107], v[16:31]\n\t"
        "buffer_load_dwordx4 v158, s[40:43], s52 offen lds\n\t"
        "s_add_u32 s52, s52, 8192\n\t"
        "v_exp_f32 v4, v4\n\t"
        "v_exp_f32 v5, v5\n\t"
        "s_mov_b64 exec, s[54:55]\n\t"
        "ds_read_b128 v[128:131], v147 offset:2432\n\t"
        "ds_read_b128 v[132:135], v147 offset:3040\n\t"
        "s_mov_b64 exec, -1\n\t"
        "v_exp_f32 v6, v6\n\t"
        "v_exp_f32 v7, v7\n\t"
        "v_smfmac_f32_32x32x32_bf16 v[80:95], v[136:139], v[48:55], v144\n\t"
        "v_exp_f32 v8, v8\n\t"
        "v_exp_f32 v9, v9\n\t"
        "v_exp_f32 v10, v10\n\t"
        "v_exp_f32 v11, v11\n\t"
        "v_smfmac_f32_32x32x32_bf16 v[80:95], v[140:143], v[56:63], v144\n\t"
        "v_exp_f32 v12, v12\n\t"
        "v_exp_f32 v13, v13\n\t"
        "v_exp_f32 v14, v14\n\t"
        "v_exp_f32 v15, v15\n\t"
        "s_waitcnt lgkmcnt(2)\n\t"
        "v_mfma_f32_32x32x16_bf16 v[32:47], v[116:119], v[96:99], 0\n\t"
        "s_mov_b32 m0, s52\n\t"
        "v_exp_f32 v16, v16\n\t"
        "v_exp_f32 v17, v17\n\t"
        "v_exp_f32 v18, v18\n\t"
        "v_exp_f32 v19, v19\n\t"
        "v_mfma_f32_32x32x8_bf16 v[32:47], v[122:123], v[104:105], v[32:47]\n\t"
        "buffer_load_dwordx4 v158, s[40:43], s52 offen lds\n\t"
        "s_add_u32 s52, s52, 8192\n\t"
        "v_exp_f32 v20, v20\n\t"
        "v_exp_f32 v21, v21\n\t"
        "v_exp_f32 v22, v22\n\t"
        "v_exp_f32 v23, v23\n\t"
        "s_waitcnt lgkmcnt(0)\n\t"
        "v_smfmac_f32_32x32x32_bf16 v[64:79], v[128:131], v[0:7], v144\n\t"
        "v_exp_f32 v24, v24\n\t"
        "v_exp_f32 v25, v25\n\t"
        "v_exp_f32 v26, v26\n\t"
        "v_exp_f32 v27, v27\n\t"
        "v_smfmac_f32_32x32x32_bf16 v[64:79], v[132:135], v[8:15], v144\n\t"
        "v_exp_f32 v28, v28\n\t"
        "v_exp_f32 v29, v29\n\t"
        "v_exp_f32 v30, v30\n\t"
        "v_exp_f32 v31, v31\n\t"
        "v_mfma_f32_32x32x16_bf16 v[48:63], v[116:119], v[100:103], 0\n\t"
        "s_mov_b32 m0, s52\n\t"
        "v_exp_f32 v32, v32\n\t"
        "v_exp_f32 v33, v33\n\t"
        "ds_read_b128 v[112:115], v145 offset:3072\n\t"
        "ds_read_b32 v120, v146 offset:384\n\t"
        "v_exp_f32 v34, v34\n\t"
        "v_exp_f32 v35, v35\n\t"
        "v_mfma_f32_32x32x8_bf16 v[48:63], v[122:123], v[106:107], v[48:63]\n\t"
        "buffer_load_dwordx4 v158, s[40:43], s52 offen lds\n\t"
        "s_add_u32 s52, s52, 8192\n\t"
        "v_exp_f32 v36, v36\n\t"
        "v_exp_f32 v37, v37\n\t"
        "s_mov_b64 exec, s[54:55]\n\t"
        "ds_read_b128 v[136:139], v147 offset:3648\n\t"
        "ds_read_b128 v[140:143], v147 offset:4256\n\t"
        "s_mov_b64 exec, -1\n\t"
        "v_exp_f32 v38, v38\n\t"
        "v_exp_f32 v39, v39\n\t"
        "v_smfmac_f32_32x32x32_bf16 v[80:95], v[128:131], v[16:23], v144\n\t"
        "v_exp_f32 v40, v40\n\t"
        "v_exp_f32 v41, v41\n\t"
        "v_exp_f32 v42, v42\n\t"
        "v_exp_f32 v43, v43\n\t"
        "v_smfmac_f32_32x32x32_bf16 v[80:95], v[132:135], v[24:31], v144\n\t"
        "v_exp_f32 v44, v44\n\t"
        "v_exp_f32 v45, v45\n\t"
        "v_exp_f32 v46, v46\n\t"
        "v_exp_f32 v47, v47\n\t"
        "s_waitcnt lgkmcnt(2)\n\t"
        "v_mfma_f32_32x32x16_bf16 v[0:15], v[112:115], v[96:99], 0\n\t"
        "s_mov_b32 m0, s52\n\t"
        "v_exp_f32 v48, v48\n\t"
        "v_exp_f32 v49, v49\n\t"
        "v_exp_f32 v50, v50\n\t"
        "v_exp_f32 v51, v51\n\t"
        "v_mfma_f32_32x32x8_bf16 v[0:15], v[120:121], v[104:105], v[0:15]\n\t"
        "buffer_load_dwordx4 v158, s[40:43], s52 offen lds\n\t"
        "s_add_u32 s52, s52, 8192\n\t"
        "v_exp_f32 v52, v52\n\t"
        "v_exp_f32 v53, v53\n\t"
        "v_exp_f32 v54, v54\n\t"
        "v_exp_f32 v55, v55\n\t"
        "s_waitcnt lgkmcnt(0)\n\t"
        "v_smfmac_f32_32x32x32_bf16 v[64:79], v[136:139], v[32:39], v144\n\t"
        "v_exp_f32 v56, v56\n\t"
        "v_exp_f32 v57, v57\n\t"
        "v_exp_f32 v58, v58\n\t"
        "v_exp_f32 v59, v59\n\t"
        "v_smfmac_f32_32x32x32_bf16 v[64:79], v[140:143], v[40:47], v144\n\t"
        "v_exp_f32 v60, v60\n\t"
        "v_exp_f32 v61, v61\n\t"
        "v_exp_f32 v62, v62\n\t"
        "v_exp_f32 v63, v63\n\t"
        "v_add_u32 v145, 18944, v145\n\t"
        "v_add_u32 v146, 18944, v146\n\t"
        "v_add_u32 v147, 18944, v147\n\t"
        "s_sub_u32 s46, s46, 1\n\t"
        "s_cmp_eq_u32 s46, 6\n\t"
        "s_cbranch_scc1 25f\n\t"
        "s_waitcnt vmcnt(3)\n\t"
        "s_barrier\n\t"
        "s_branch 20b\n\t"
        "25:\n\t"
        "v_mfma_f32_32x32x16_bf16 v[16:31], v[112:115], v[100:103], 0\n\t"
        "v_exp_f32 v0, v0\n\t"
        "v_exp_f32 v1, v1\n\t"
        "ds_read_b128 v[116:119], v145 offset:0\n\t"
        "ds_read_b32 v122, v146 offset:0\n\t"
        "v_exp_f32 v2, v2\n\t"
        "v_exp_f32 v3, v3\n\t"
        "v_mfma_f32_32x32x8_bf16 v[16:31], v[120:121], v[106:107], v[16:31]\n\t"
        "v_exp_f32 v4, v4\n\t"
        "v_exp_f32 v5, v5\n\t"
        "s_mov_b64 exec, s[54:55]\n\t"
        "ds_read_b128 v[128:131], v147 offset:0\n\t"
        "ds_read_b128 v[132:135], v147 offset:608\n\t"
        "s_mov_b64 exec, -1\n\t"
        "v_exp_f32 v6, v6\n\t"
        "v_exp_f32 v7, v7\n\t"
        "v_smfmac_f32_32x32x32_bf16 v[80:95], v[136:139], v[48:55], v144\n\t"
        "v_exp_f32 v8, v8\n\t"
        "v_exp_f32 v9, v9\n\t"
        "v_exp_f32 v10, v10\n\t"
        "v_exp_f32 v11, v11\n\t"
        "v_smfmac_f32_32x32x32_bf16 v[80:95], v[140:143], v[56:63], v144\n\t"
        "v_exp_f32 v12, v12\n\t"
        "v_exp_f32 v13, v13\n\t"
        "v_exp_f32 v14, v14\n\t"
        "v_exp_f32 v15, v15\n\t"
        "s_waitcnt lgkmcnt(2)\n\t"
        "v_mfma_f32_32x32x16_bf16 v[32:47], v[116:119], v[96:99], 0\n\t"
        "v_exp_f32 v16, v16\n\t"
        "v_exp_f32 v17, v17\n\t"
        "v_exp_f32 v18, v18\n\t"
        "v_exp_f32 v19, v19\n\t"
        "v_mfma_f32_32x32x8_bf16 v[32:47], v[122:123], v[104:105], v[32:47]\n\t"
        "v_exp_f32 v20, v20\n\t"
        "v_exp_f32 v21, v21\n\t"
        "v_exp_f32 v22, v22\n\t"
        "v_exp_f32 v23, v23\n\t"
        "s_waitcnt lgkmcnt(0)\n\t"
        "v_smfmac_f32_32x32x32_bf16 v[64:79], v[128:131], v[0:7], v144\n\t"
        "v_exp_f32 v24, v24\n\t"
        "v_exp_f32 v25, v25\n\t"
        "v_exp_f32 v26, v26\n\t"
        "v_exp_f32 v27, v27\n\t"
        "v_smfmac_f32_32x32x32_bf16 v[64:79], v[132:135], v[8:15], v144\n\t"
        "v_exp_f32 v28, v28\n\t"
        "v_exp_f32 v29, v29\n\t"
        "v_exp_f32 v30, v30\n\t"
        "v_exp_f32 v31, v31\n\t"
        "v_mfma_f32_32x32x16_bf16 v[48:63], v[116:119], v[100:103], 0\n\t"
        "v_exp_f32 v32, v32\n\t"
        "v_exp_f32 v33, v33\n\t"
        "ds_read_b128 v[112:115], v145 offset:1024\n\t"
        "ds_read_b32 v120, v146 offset:128\n\t"
        "v_exp_f32 v34, v34\n\t"
        "v_exp_f32 v35, v35\n\t"
        "v_mfma_f32_32x32x8_bf16 v[48:63], v[122:123], v[106:107], v[48:63]\n\t"
        "v_exp_f32 v36, v36\n\t"
        "v_exp_f32 v37, v37\n\t"
        "s_mov_b64 exec, s[54:55]\n\t"
        "ds_read_b128 v[136:139], v147 offset:1216\n\t"
        "ds_read_b128 v[140:143], v147 offset:1824\n\t"
        "s_mov_b64 exec, -1\n\t"
        "v_exp_f32 v38, v38\n\t"
        "v_exp_f32 v39, v39\n\t"
        "v_smfmac_f32_32x32x32_bf16 v[80:95], v[128:131], v[16:23], v144\n\t"
        "v_exp_f32 v40, v40\n\t"
        "v_exp_f32 v41, v41\n\t"
        "v_exp_f32 v42, v42\n\t"
        "v_exp_f32 v43, v43\n\t"
        "v_smfmac_f32_32x32x32_bf16 v[80:95], v[132:135], v[24:31], v144\n\t"
        "v_exp_f32 v44, v44\n\t"
        "v_exp_f32 v45, v45\n\t"
        "v_exp_f32 v46, v46\n\t"
        "v_exp_f32 v47, v47\n\t"
        "s_waitcnt lgkmcnt(2)\n\t"
        "v_mfma_f32_32x32x16_bf16 v[0:15], v[112:115], v[96:99], 0\n\t"
        "v_exp_f32 v48, v48\n\t"
        "v_exp_f32 v49, v49\n\t"
        "v_exp_f32 v50, v50\n\t"
        "v_exp_f32 v51, v51\n\t"
        "v_mfma_f32_32x32x8_bf16 v[0:15], v[120:121], v[104:105], v[0:15]\n\t"
        "v_exp_f32 v52, v52\n\t"
        "v_exp_f32 v53, v53\n\t"
        "v_exp_f32 v54, v54\n\t"
        "v_exp_f32 v55, v55\n\t"
        "s_waitcnt lgkmcnt(0)\n\t"
        "v_smfmac_f32_32x32x32_bf16 v[64:79], v[136:139], v[32:39], v144\n\t"
        "v_exp_f32 v56, v56\n\t"
        "v_exp_f32 v57, v57\n\t"
        "v_exp_f32 v58, v58\n\t"
        "v_exp_f32 v59, v59\n\t"
        "v_smfmac_f32_32x32x32_bf16 v[64:79], v[140:143], v[40:47], v144\n\t"
        "v_exp_f32 v60, v60\n\t"
        "v_exp_f32 v61, v61\n\t"
        "v_exp_f32 v62, v62\n\t"
        "v_exp_f32 v63, v63\n\t"
        "v_mfma_f32_32x32x16_bf16 v[16:31], v[112:115], v[100:103], 0\n\t"
        "v_exp_f32 v0, v0\n\t"
        "v_exp_f32 v1, v1\n\t"
        "ds_read_b128 v[116:119], v145 offset:2048\n\t"
        "ds_read_b32 v122, v146 offset:256\n\t"
        "v_exp_f32 v2, v2\n\t"
        "v_exp_f32 v3, v3\n\t"
        "v_mfma_f32_32x32x8_bf16 v[16:31], v[120:121], v[106:107], v[16:31]\n\t"
        "v_exp_f32 v4, v4\n\t"
        "v_exp_f32 v5, v5\n\t"
        "s_mov_b64 exec, s[54:55]\n\t"
        "ds_read_b128 v[128:131], v147 offset:2432\n\t"
        "ds_read_b128 v[132:135], v147 offset:3040\n\t"
        "s_mov_b64 exec, -1\n\t"
        "v_exp_f32 v6, v6\n\t"
        "v_exp_f32 v7, v7\n\t"
        "v_smfmac_f32_32x32x32_bf16 v[80:95], v[136:139], v[48:55], v144\n\t"
        "v_exp_f32 v8, v8\n\t"
        "v_exp_f32 v9, v9\n\t"
        "v_exp_f32 v10, v10\n\t"
        "v_exp_f32 v11, v11\n\t"
        "v_smfmac_f32_32x32x32_bf16 v[80:95], v[140:143], v[56:63], v144\n\t"
        "v_exp_f32 v12, v12\n\t"
        "v_exp_f32 v13, v13\n\t"
        "v_exp_f32 v14, v14\n\t"
        "v_exp_f32 v15, v15\n\t"
        "s_waitcnt lgkmcnt(2)\n\t"
        "v_mfma_f32_32x32x16_bf16 v[32:47], v[116:119], v[96:99], 0\n\t"
        "v_exp_f32 v16, v16\n\t"
        "v_exp_f32 v17, v17\n\t"
        "v_exp_f32 v18, v18\n\t"
        "v_exp_f32 v19, v19\n\t"
        "v_mfma_f32_32x32x8_bf16 v[32:47], v[122:123], v[104:105], v[32:47]\n\t"
        "v_exp_f32 v20, v20\n\t"
        "v_exp_f32 v21, v21\n\t"
        "v_exp_f32 v22, v22\n\t"
        "v_exp_f32 v23, v23\n\t"
        "s_waitcnt lgkmcnt(0)\n\t"
        "v_smfmac_f32_32x32x32_bf16 v[64:79], v[128:131], v[0:7], v144\n\t"
        "v_exp_f32 v24, v24\n\t"
        "v_exp_f32 v25, v25\n\t"
        "v_exp_f32 v26, v26\n\t"
        "v_exp_f32 v27, v27\n\t"
        "v_smfmac_f32_32x32x32_bf16 v[64:79], v[132:135], v[8:15], v144\n\t"
        "v_exp_f32 v28, v28\n\t"
        "v_exp_f32 v29, v29\n\t"
        "v_exp_f32 v30, v30\n\t"
        "v_exp_f32 v31, v31\n\t"
        "v_mfma_f32_32x32x16_bf16 v[48:63], v[116:119], v[100:103], 0\n\t"
        "v_exp_f32 v32, v32\n\t"
        "v_exp_f32 v33, v33\n\t"
        "ds_read_b128 v[112:115], v145 offset:3072\n\t"
        "ds_read_b32 v120, v146 offset:384\n\t"
        "v_exp_f32 v34, v34\n\t"
        "v_exp_f32 v35, v35\n\t"
        "v_mfma_f32_32x32x8_bf16 v[48:63], v[122:123], v[106:107], v[48:63]\n\t"
        "v_exp_f32 v36, v36\n\t"
        "v_exp_f32 v37, v37\n\t"
        "s_mov_b64 exec, s[54:55]\n\t"
        "ds_read_b128 v[136:139], v147 offset:3648\n\t"
        "ds_read_b128 v[140:143], v147 offset:4256\n\t"
        "s_mov_b64 exec, -1\n\t"
        "v_exp_f32 v38, v38\n\t"
        "v_exp_f32 v39, v39\n\t"
        "v_smfmac_f32_32x32x32_bf16 v[80:95], v[128:131], v[16:23], v144\n\t"
        "v_exp_f32 v40, v40\n\t"
        "v_exp_f32 v41, v41\n\t"
        "v_exp_f32 v42, v42\n\t"
        "v_exp_f32 v43, v43\n\t"
        "v_smfmac_f32_32x32x32_bf16 v[80:95], v[132:135], v[24:31], v144\n\t"
        "v_exp_f32 v44, v44\n\t"
        "v_exp_f32 v45, v45\n\t"
        "v_exp_f32 v46, v46\n\t"
        "v_exp_f32 v47, v47\n\t"
        "s_waitcnt lgkmcnt(2)\n\t"
        "v_mfma_f32_32x32x16_bf16 v[0:15], v[112:115], v[96:99], 0\n\t"
        "v_exp_f32 v48, v48\n\t"
        "v_exp_f32 v49, v49\n\t"
        "v_exp_f32 v50, v50\n\t"
        "v_exp_f32 v51, v51\n\t"
        "v_mfma_f32_32x32x8_bf16 v[0:15], v[120:121], v[104:105], v[0:15]\n\t"
        "v_exp_f32 v52, v52\n\t"
        "v_exp_f32 v53, v53\n\t"
        "v_exp_f32 v54, v54\n\t"
        "v_exp_f32 v55, v55\n\t"
        "s_waitcnt lgkmcnt(0)\n\t"
        "v_smfmac_f32_32x32x32_bf16 v[64:79], v[136:139], v[32:39], v144\n\t"
        "v_exp_f32 v56, v56\n\t"
        "v_exp_f32 v57, v57\n\t"
        "v_exp_f32 v58, v58\n\t"
        "v_exp_f32 v59, v59\n\t"
        "v_smfmac_f32_32x32x32_bf16 v[64:79], v[140:143], v[40:47], v144\n\t"
        "v_exp_f32 v60, v60\n\t"
        "v_exp_f32 v61, v61\n\t"
        "v_exp_f32 v62, v62\n\t"
        "v_exp_f32 v63, v63\n\t"
        "v_add_u32 v145, 18944, v145\n\t"
        "v_add_u32 v146, 18944, v146\n\t"
        "v_add_u32 v147, 18944, v147\n\t"
        "26:\n\t"
        "s_sub_u32 s46, s46, 1\n\t"
        "s_cmp_eq_u32 s46, 5\n\t"
        "s_cbranch_scc1 30f\n\t"
        "s_cmp_eq_u32 s46, 3\n\t"
        "s_cbranch_scc1 33f\n\t"
        "21:\n\t"
        "s_cmp_lg_u32 s46, s50\n\t"
        "s_cbranch_scc1 22f\n\t"
        "s_setprio 0\n\t"
        "22:\n\t"
        "s_cmp_eq_u32 s46, 2\n\t"
        "s_cbranch_scc1 35f\n\t"
        "23:\n\t"
        "s_cmp_lg_u32 s46, 0\n\t"
        "s_cbranch_scc1 25b\n\t"
        "s_branch 40f\n\t"
        "30:\n\t"
        "s_waitcnt vmcnt(0)\n\t"
        "s_barrier\n\t"
        "s_cmp_lg_u32 s53, 0\n\t"
        "s_cbranch_scc1 21b\n\t"
        "s_cmp_lg_u32 s45, 0\n\t"
        "s_cbranch_scc1 31f\n\t"
        "s_cmp_eq_u32 s51, 3\n\t"
        "s_cbranch_scc1 31f\n\t"
        "s_mov_b32 s88, s74\n\t"
        "s_mov_b64 exec, s[80:81]\n\t"
        "buffer_load_dwordx4 v[160:163], v231, s[40:43], s88 offen\n\t"
        "s_add_u32 s88, s88, 2736\n\t"
        "buffer_load_dwordx4 v[164:167], v231, s[40:43], s88 offen\n\t"
        "s_add_u32 s88, s88, 2736\n\t"
        "buffer_load_dwordx4 v[168:171], v231, s[40:43], s88 offen\n\t"
        "s_add_u32 s88, s88, 2736\n\t"
        "buffer_load_dwordx4 v[172:175], v231, s[40:43], s88 offen\n\t"
        "s_add_u32 s88, s88, 2736\n\t"
        "buffer_load_dwordx4 v[176:179], v231, s[40:43], s88 offen\n\t"
        "s_add_u32 s88, s88, 2736\n\t"
        "buffer_load_dwordx4 v[180:183], v231, s[40:43], s88 offen\n\t"
        "s_add_u32 s88, s88, 2736\n\t"
        "buffer_load_dwordx4 v[184:187], v231, s[40:43], s88 offen\n\t"
        "s_add_u32 s88, s88, 2736\n\t"
        "buffer_load_dwordx4 v[188:191], v231, s[40:43], s88 offen\n\t"
        "s_add_u32 s88, s88, 2736\n\t"
        "buffer_load_dwordx4 v[192:195], v231, s[40:43], s88 offen\n\t"
        "s_add_u32 s88, s88, 2736\n\t"
        "buffer_load_dwordx4 v[196:199], v231, s[40:43], s88 offen\n\t"
        "s_add_u32 s88, s88, 2736\n\t"
        "buffer_load_dwordx4 v[200:203], v231, s[40:43], s88 offen\n\t"
        "s_add_u32 s88, s88, 2736\n\t"
        "buffer_load_dwordx4 v[204:207], v231, s[40:43], s88 offen\n\t"
        "s_add_u32 s88, s88, 2736\n\t"
        "buffer_load_dwordx4 v[208:211], v231, s[40:43], s88 offen\n\t"
        "s_add_u32 s88, s88, 2736\n\t"
        "buffer_load_dwordx4 v[212:215], v231, s[40:43], s88 offen\n\t"
        "s_add_u32 s88, s88, 2736\n\t"
        "buffer_load_dwordx4 v[216:219], v231, s[40:43], s88 offen\n\t"
        "s_add_u32 s88, s88, 2736\n\t"
        "buffer_load_dwordx4 v[220:223], v231, s[40:43], s88 offen\n\t"
        "s_mov_b64 exec, -1\n\t"
        "31:\n\t"
        "buffer_load_dwordx4 v[148:151], v158, s[40:43], s75 offen\n\t"
        "buffer_load_dwordx4 v[152:155], v158, s[40:43], s75 offen offset:1024\n\t"
        "v_lshlrev_b32 v227, 2, v241\n\t"
        "buffer_load_dword v156, v227, s[40:43], s75 offen offset:2048\n\t"
        "s_branch 21b\n\t"
        "33:\n\t"
        "s_cmp_lg_u32 s53, 0\n\t"
        "s_cbranch_scc1 21b\n\t"
        "s_waitcnt vmcnt(0)\n\t"
        "buffer_load_dword v239, v238, s[40:43], s82 offen\n\t"
        "s_cmp_lg_u32 s44, 7\n\t"
        "s_cbranch_scc1 34f\n\t"
        "s_getpc_b64 s[88:89]\n\t"
        "97:\n\t"
        "s_add_u32 s88, s88, 99f-97b\n\t"
        "s_addc_u32 s89, s89, 0\n\t"
        "s_and_b32 s88, s88, 0xffffff80\n\t"
        "v_min_u32 v227, 43, v241\n\t"
        "v_lshlrev_b32 v227, 7, v227\n\t"
        "global_load_dword v239, v227, s[88:89]\n\t"
        "34:\n\t"
        "s_cmp_lg_u32 s45, 0\n\t"
        "s_cbranch_scc1 21b\n\t"
        "s_cmp_eq_u32 s51, 3\n\t"
        "s_cbranch_scc1 21b\n\t"
        "v_pk_add_f32 v[160:161], v[160:161], v[164:165]\n\t"
        "v_pk_add_f32 v[162:163], v[162:163], v[166:167]\n\t"
        "v_pk_add_f32 v[168:169], v[168:169], v[172:173]\n\t"
        "v_pk_add_f32 v[170:171], v[170:171], v[174:175]\n\t"
        "v_pk_add_f32 v[176:177], v[176:177], v[180:181]\n\t"
        "v_pk_add_f32 v[178:179], v[178:179], v[182:183]\n\t"
        "v_pk_add_f32 v[184:185], v[184:185], v[188:189]\n\t"
        "v_pk_add_f32 v[186:187], v[186:187], v[190:191]\n\t"
        "v_pk_add_f32 v[192:193], v[192:193], v[196:197]\n\t"
        "v_pk_add_f32 v[194:195], v[194:195], v[198:199]\n\t"
        "v_pk_add_f32 v[200:201], v[200:201], v[204:205]\n\t"
        "v_pk_add_f32 v[202:203], v[202:203], v[206:207]\n\t"
        "v_pk_add_f32 v[208:209], v[208:209], v[212:213]\n\t"
        "v_pk_add_f32 v[210:211], v[210:211], v[214:215]\n\t"
        "v_pk_add_f32 v[216:217], v[216:217], v[220:221]\n\t"
        "v_pk_add_f32 v[218:219], v[218:219], v[222:223]\n\t"
        "v_pk_add_f32 v[160:161], v[160:161], v[168:169]\n\t"
        "v_pk_add_f32 v[162:163], v[162:163], v[170:171]\n\t"
        "v_pk_add_f32 v[176:177], v[176:177], v[184:185]\n\t"
        "v_pk_add_f32 v[178:179], v[178:179], v[186:187]\n\t"
        "v_pk_add_f32 v[192:193], v[192:193], v[200:201]\n\t"
        "v_pk_add_f32 v[194:195], v[194:195], v[202:203]\n\t"
        "v_pk_add_f32 v[208:209], v[208:209], v[216:217]\n\t"
        "v_pk_add_f32 v[210:211], v[210:211], v[218:219]\n\t"
        "v_pk_add_f32 v[160:161], v[160:161], v[176:177]\n\t"
        "v_pk_add_f32 v[162:163], v[162:163], v[178:179]\n\t"
        "v_pk_add_f32 v[192:193], v[192:193], v[208:209]\n\t"
        "v_pk_add_f32 v[194:195], v[194:195], v[210:211]\n\t"
        "v_pk_add_f32 v[160:161], v[160:161], v[192:193]\n\t"
        "v_pk_add_f32 v[162:163], v[162:163], v[194:195]\n\t"
        "v_cvt_pk_bf16_f32 v164, v160, v161\n\t"
        "v_cvt_pk_bf16_f32 v165, v162, v163\n\t"
        "s_mov_b64 exec, s[80:81]\n\t"
        "ds_write_b64 v230, v[164:165]\n\t"
        "s_mov_b64 exec, 1\n\t"
        "v_mov_b32 v166, 1\n\t"
        "ds_add_u32 v237, v166\n\t"
        "s_mov_b64 exec, -1\n\t"
        "s_branch 21b\n\t"
        "35:\n\t"
        "s_cmp_lg_u32 s53, 0\n\t"
        "s_cbranch_scc1 23b\n\t"
        "v_mfma_f32_32x32x16_bf16 v[16:31], v[112:115], v[100:103], 0\n\t"
        "v_exp_f32 v0, v0\n\t"
        "v_exp_f32 v1, v1\n\t"
        "ds_read_b128 v[116:119], v145 offset:0\n\t"
        "ds_read_b32 v122, v146 offset:0\n\t"
        "v_exp_f32 v2, v2\n\t"
        "v_exp_f32 v3, v3\n\t"
        "v_mfma_f32_32x32x8_bf16 v[16:31], v[120:121], v[106:107], v[16:31]\n\t"
        "v_exp_f32 v4, v4\n\t"
        "v_exp_f32 v5, v5\n\t"
        "s_mov_b64 exec, s[54:55]\n\t"
        "ds_read_b128 v[128:131], v147 offset:0\n\t"
        "ds_read_b128 v[132:135], v147 offset:608\n\t"
        "s_mov_b64 exec, -1\n\t"
        "v_exp_f32 v6, v6\n\t"
        "v_exp_f32 v7, v7\n\t"
        "v_smfmac_f32_32x32x32_bf16 v[80:95], v[136:139], v[48:55], v144\n\t"
        "v_exp_f32 v8, v8\n\t"
        "v_exp_f32 v9, v9\n\t"
        "v_exp_f32 v10, v10\n\t"
        "v_exp_f32 v11, v11\n\t"
        "v_smfmac_f32_32x32x32_bf16 v[80:95], v[140:143], v[56:63], v144\n\t"
        "v_exp_f32 v12, v12\n\t"
        "v_exp_f32 v13, v13\n\t"
        "v_exp_f32 v14, v14\n\t"
        "v_exp_f32 v15, v15\n\t"
        "s_waitcnt lgkmcnt(2)\n\t"
        "v_mfma_f32_32x32x16_bf16 v[32:47], v[116:119], v[96:99], 0\n\t"
        "v_exp_f32 v16, v16\n\t"
        "v_exp_f32 v17, v17\n\t"
        "v_exp_f32 v18, v18\n\t"
        "v_exp_f32 v19, v19\n\t"
        "v_mfma_f32_32x32x8_bf16 v[32:47], v[122:123], v[104:105], v[32:47]\n\t"
        "v_exp_f32 v20, v20\n\t"
        "v_exp_f32 v21, v21\n\t"
        "v_exp_f32 v22, v22\n\t"
        "v_exp_f32 v23, v23\n\t"
        "s_waitcnt lgkmcnt(0)\n\t"
        "ds_read_b32 v186, v237\n\t"
        "ds_read_b128 v[176:179], v235\n\t"
        "ds_read_b128 v[180:183], v235 offset:1024\n\t"
        "ds_read_b32 v184, v236\n\t"
        "v_mov_b32 v185, 0\n\t"
        "v_smfmac_f32_32x32x32_bf16 v[64:79], v[128:131], v[0:7], v144\n\t"
        "v_exp_f32 v24, v24\n\t"
        "v_exp_f32 v25, v25\n\t"
        "v_exp_f32 v26, v26\n\t"
        "v_exp_f32 v27, v27\n\t"
        "v_smfmac_f32_32x32x32_bf16 v[64:79], v[132:135], v[8:15], v144\n\t"
        "v_exp_f32 v28, v28\n\t"
        "v_exp_f32 v29, v29\n\t"
        "v_exp_f32 v30, v30\n\t"
        "v_exp_f32 v31, v31\n\t"
        "v_mfma_f32_32x32x16_bf16 v[48:63], v[116:119], v[100:103], 0\n\t"
        "v_exp_f32 v32, v32\n\t"
        "v_exp_f32 v33, v33\n\t"
        "ds_read_b128 v[112:115], v145 offset:1024\n\t"
        "ds_read_b32 v120, v146 offset:128\n\t"
        "v_exp_f32 v34, v34\n\t"
        "v_exp_f32 v35, v35\n\t"
        "v_mfma_f32_32x32x8_bf16 v[48:63], v[122:123], v[106:107], v[48:63]\n\t"
        "v_exp_f32 v36, v36\n\t"
        "v_exp_f32 v37, v37\n\t"
        "s_mov_b64 exec, s[54:55]\n\t"
        "ds_read_b128 v[136:139], v147 offset:1216\n\t"
        "ds_read_b128 v[140:143], v147 offset:1824\n\t"
        "s_mov_b64 exec, -1\n\t"
        "v_exp_f32 v38, v38\n\t"
        "v_exp_f32 v39, v39\n\t"
        "v_smfmac_f32_32x32x32_bf16 v[80:95], v[128:131], v[16:23], v144\n\t"
        "v_exp_f32 v40, v40\n\t"
        "v_exp_f32 v41, v41\n\t"
        "v_exp_f32 v42, v42\n\t"
        "v_exp_f32 v43, v43\n\t"
        "v_smfmac_f32_32x32x32_bf16 v[80:95], v[132:135], v[24:31], v144\n\t"
        "v_exp_f32 v44, v44\n\t"
        "v_exp_f32 v45, v45\n\t"
        "v_exp_f32 v46, v46\n\t"
        "v_exp_f32 v47, v47\n\t"
        "s_waitcnt lgkmcnt(2)\n\t"
        "v_mfma_f32_32x32x16_bf16 v[0:15], v[112:115], v[96:99], 0\n\t"
        "v_exp_f32 v48, v48\n\t"
        "v_exp_f32 v49, v49\n\t"
        "v_cmp_ne_u32 vcc, 3, v186\n\t"
        "s_cbranch_vccnz 36f\n\t"
        "37:\n\t"
        "v_exp_f32 v50, v50\n\t"
        "v_exp_f32 v51, v51\n\t"
        "v_mfma_f32_32x32x8_bf16 v[0:15], v[120:121], v[104:105], v[0:15]\n\t"
        "v_exp_f32 v52, v52\n\t"
        "v_exp_f32 v53, v53\n\t"
        "v_mfma_f32_32x32x16_bf16 v[160:175], v[176:179], v[148:151], 0\n\t"
        "v_exp_f32 v54, v54\n\t"
        "v_exp_f32 v55, v55\n\t"
        "s_waitcnt lgkmcnt(0)\n\t"
        "v_smfmac_f32_32x32x32_bf16 v[64:79], v[136:139], v[32:39], v144\n\t"
        "v_exp_f32 v56, v56\n\t"
        "v_exp_f32 v57, v57\n\t"
        "v_exp_f32 v58, v58\n\t"
        "v_exp_f32 v59, v59\n\t"
        "v_smfmac_f32_32x32x32_bf16 v[64:79], v[140:143], v[40:47], v144\n\t"
        "v_exp_f32 v60, v60\n\t"
        "v_exp_f32 v61, v61\n\t"
        "v_exp_f32 v62, v62\n\t"
        "v_exp_f32 v63, v63\n\t"
        "v_mfma_f32_32x32x16_bf16 v[16:31], v[112:115], v[100:103], 0\n\t"
        "v_exp_f32 v0, v0\n\t"
        "v_exp_f32 v1, v1\n\t"
        "ds_read_b128 v[116:119], v145 offset:2048\n\t"
        "ds_read_b32 v122, v146 offset:256\n\t"
        "v_exp_f32 v2, v2\n\t"
        "v_exp_f32 v3, v3\n\t"
        "v_mfma_f32_32x32x8_bf16 v[16:31], v[120:121], v[106:107], v[16:31]\n\t"
        "v_exp_f32 v4, v4\n\t"
        "v_exp_f32 v5, v5\n\t"
        "v_mfma_f32_32x32x16_bf16 v[160:175], v[180:183], v[152:155], v[160:175]\n\t"
        "s_mov_b64 exec, s[54:55]\n\t"
        "ds_read_b128 v[128:131], v147 offset:2432\n\t"
        "ds_read_b128 v[132:135], v147 offset:3040\n\t"
        "s_mov_b64 exec, -1\n\t"
        "v_exp_f32 v6, v6\n\t"
        "v_exp_f32 v7, v7\n\t"
        "v_smfmac_f32_32x32x32_bf16 v[80:95], v[136:139], v[48:55], v144\n\t"
        "v_exp_f32 v8, v8\n\t"
        "v_exp_f32 v9, v9\n\t"
        "v_exp_f32 v10, v10\n\t"
        "v_exp_f32 v11, v11\n\t"
        "v_smfmac_f32_32x32x32_bf16 v[80:95], v[140:143], v[56:63], v144\n\t"
        "v_exp_f32 v12, v12\n\t"
        "v_exp_f32 v13, v13\n\t"
        "v_exp_f32 v14, v14\n\t"
        "v_exp_f32 v15, v15\n\t"
        "s_waitcnt lgkmcnt(2)\n\t"
        "v_mfma_f32_32x32x16_bf16 v[32:47], v[116:119], v[96:99], 0\n\t"
        "v_exp_f32 v16, v16\n\t"
        "v_exp_f32 v17, v17\n\t"
        "v_exp_f32 v18, v18\n\t"
        "v_exp_f32 v19, v19\n\t"
        "v_mfma_f32_32x32x8_bf16 v[32:47], v[122:123], v[104:105], v[32:47]\n\t"
        "v_exp_f32 v20, v20\n\t"
        "v_exp_f32 v21, v21\n\t"
        "v_mfma_f32_32x32x8_bf16 v[160:175], v[184:185], v[156:157], v[160:175]\n\t"
        "v_exp_f32 v22, v22\n\t"
        "v_exp_f32 v23, v23\n\t"
        "s_waitcnt lgkmcnt(0)\n\t"
        "v_smfmac_f32_32x32x32_bf16 v[64:79], v[128:131], v[0:7], v144\n\t"
        "v_exp_f32 v24, v24\n\t"
        "v_exp_f32 v25, v25\n\t"
        "v_exp_f32 v26, v26\n\t"
        "v_exp_f32 v27, v27\n\t"
        "v_smfmac_f32_32x32x32_bf16 v[64:79], v[132:135], v[8:15], v144\n\t"
        "v_exp_f32 v28, v28\n\t"
        "v_exp_f32 v29, v29\n\t"
        "v_exp_f32 v30, v30\n\t"
        "v_exp_f32 v31, v31\n\t"
        "v_mfma_f32_32x32x16_bf16 v[48:63], v[116:119], v[100:103], 0\n\t"
        "v_exp_f32 v32, v32\n\t"
        "v_exp_f32 v33, v33\n\t"
        "ds_read_b128 v[112:115], v145 offset:3072\n\t"
        "ds_read_b32 v120, v146 offset:384\n\t"
        "v_exp_f32 v34, v34\n\t"
        "v_exp_f32 v35, v35\n\t"
        "v_mfma_f32_32x32x8_bf16 v[48:63], v[122:123], v[106:107], v[48:63]\n\t"
        "v_exp_f32 v36, v36\n\t"
        "v_exp_f32 v37, v37\n\t"
        "v_mov_b32 v188, v170\n\t"
        "s_mov_b64 exec, s[54:55]\n\t"
        "ds_read_b128 v[136:139], v147 offset:3648\n\t"
        "ds_read_b128 v[140:143], v147 offset:4256\n\t"
        "s_mov_b64 exec, -1\n\t"
        "v_exp_f32 v38, v38\n\t"
        "v_exp_f32 v39, v39\n\t"
        "v_permlane32_swap_b32 v170, v188\n\t"
        "v_smfmac_f32_32x32x32_bf16 v[80:95], v[128:131], v[16:23], v144\n\t"
        "v_exp_f32 v40, v40\n\t"
        "v_exp_f32 v41, v41\n\t"
        "v_exp_f32 v42, v42\n\t"
        "v_exp_f32 v43, v43\n\t"
        "v_add_f32 v188, 0x358637bd, v170\n\t"
        "v_smfmac_f32_32x32x32_bf16 v[80:95], v[132:135], v[24:31], v144\n\t"
        "v_exp_f32 v44, v44\n\t"
        "v_exp_f32 v45, v45\n\t"
        "v_rcp_f32 v188, v188\n\t"
        "v_exp_f32 v46, v46\n\t"
        "v_exp_f32 v47, v47\n\t"
        "s_waitcnt lgkmcnt(2)\n\t"
        "v_mfma_f32_32x32x16_bf16 v[0:15], v[112:115], v[96:99], 0\n\t"
        "v_exp_f32 v48, v48\n\t"
        "v_exp_f32 v49, v49\n\t"
        "v_pk_mul_f32 v[160:161], v[160:161], v[188:189] op_sel_hi:[1,0]\n\t"
        "v_pk_mul_f32 v[162:163], v[162:163], v[188:189] op_sel_hi:[1,0]\n\t"
        "v_exp_f32 v50, v50\n\t"
        "v_exp_f32 v51, v51\n\t"
        "v_pk_mul_f32 v[164:165], v[164:165], v[188:189] op_sel_hi:[1,0]\n\t"
        "v_pk_mul_f32 v[166:167], v[166:167], v[188:189] op_sel_hi:[1,0]\n\t"
        "v_mfma_f32_32x32x8_bf16 v[0:15], v[120:121], v[104:105], v[0:15]\n\t"
        "v_exp_f32 v52, v52\n\t"
        "v_exp_f32 v53, v53\n\t"
        "v_pk_mul_f32 v[168:169], v[168:169], v[188:189] op_sel_hi:[1,0]\n\t"
        "v_cvt_pk_bf16_f32 v160, v160, v161\n\t"
        "v_exp_f32 v54, v54\n\t"
        "v_exp_f32 v55, v55\n\t"
        "v_cvt_pk_bf16_f32 v161, v162, v163\n\t"
        "v_cvt_pk_bf16_f32 v162, v164, v165\n\t"
        "s_waitcnt lgkmcnt(0)\n\t"
        "v_smfmac_f32_32x32x32_bf16 v[64:79], v[136:139], v[32:39], v144\n\t"
        "v_exp_f32 v56, v56\n\t"
        "v_exp_f32 v57, v57\n\t"
        "v_cvt_pk_bf16_f32 v163, v166, v167\n\t"
        "v_cvt_pk_bf16_f32 v164, v168, v169\n\t"
        "v_exp_f32 v58, v58\n\t"
        "v_exp_f32 v59, v59\n\t"
        "s_waitcnt vmcnt(0)\n\t"
        "buffer_store_dwordx4 v[160:163], v232, s[40:43], s76 offen sc1\n\t"
        "s_mov_b32 exec_hi, 0\n\t"
        "buffer_store_dword v164, v232, s[40:43], s77 offen\n\t"
        "s_mov_b32 exec_hi, -1\n\t"
        "v_smfmac_f32_32x32x32_bf16 v[64:79], v[140:143], v[40:47], v144\n\t"
        "v_exp_f32 v60, v60\n\t"
        "v_exp_f32 v61, v61\n\t"
        "v_exp_f32 v62, v62\n\t"
        "v_exp_f32 v63, v63\n\t"
        "v_add_u32 v145, 18944, v145\n\t"
        "v_add_u32 v146, 18944, v146\n\t"
        "v_add_u32 v147, 18944, v147\n\t"
        "s_branch 26b\n\t"
        "36:\n\t"
        "ds_read_b32 v186, v237\n\t"
        "ds_read_b128 v[176:179], v235\n\t"
        "ds_read_b128 v[180:183], v235 offset:1024\n\t"
        "ds_read_b32 v184, v236\n\t"
        "s_waitcnt lgkmcnt(0)\n\t"
        "v_cmp_ne_u32 vcc, 3, v186\n\t"
        "s_cbranch_vccnz 36b\n\t"
        "s_branch 37b\n\t"
        "40:\n\t"
        "s_nop 1\n\t"
        "v_smfmac_f32_32x32x32_bf16 v[80:95], v[136:139], v[48:55], v144\n\t"
        "v_smfmac_f32_32x32x32_bf16 v[80:95], v[140:143], v[56:63], v144\n\t"
        "s_setprio 0\n\t"
        "s_nop 2\n\t"
        "ds_write_b128 v233, v[64:67] offset:0\n\t"
        "ds_write_b128 v233, v[68:71] offset:1024\n\t"
        "ds_write_b128 v233, v[72:75] offset:2048\n\t"
        "s_waitcnt lgkmcnt(0)\n\t"
        "s_barrier\n\t"
        "ds_read_b128 v[200:203], v234 offset:2048\n\t"
        "ds_read_b128 v[192:195], v234\n\t"
        "ds_read_b128 v[196:199], v234 offset:1024\n\t"
        "s_waitcnt lgkmcnt(2)\n\t"
        "v_add_f32 v90, v90, v202\n\t"
        "v_pk_add_f32 v[88:89], v[88:89], v[200:201]\n\t"
        "v_mov_b32 v188, v90\n\t"
        "s_waitcnt lgkmcnt(1)\n\t"
        "v_pk_add_f32 v[80:81], v[80:81], v[192:193]\n\t"
        "v_pk_add_f32 v[82:83], v[82:83], v[194:195]\n\t"
        "v_permlane32_swap_b32 v90, v188\n\t"
        "s_waitcnt lgkmcnt(0)\n\t"
        "v_pk_add_f32 v[84:85], v[84:85], v[196:197]\n\t"
        "v_rcp_f32 v188, v90\n\t"
        "v_pk_add_f32 v[86:87], v[86:87], v[198:199]\n\t"
        "v_pk_mul_f32 v[242:243], v[88:89], 0 op_sel_hi:[1,0]\n\t"
        "v_pk_mul_f32 v[192:193], v[80:81], v[188:189] op_sel_hi:[1,0]\n\t"
        "v_pk_mul_f32 v[194:195], v[82:83], v[188:189] op_sel_hi:[1,0]\n\t"
        "v_pk_mul_f32 v[196:197], v[84:85], v[188:189] op_sel_hi:[1,0]\n\t"
        "v_pk_mul_f32 v[198:199], v[86:87], v[188:189] op_sel_hi:[1,0]\n\t"
        "v_pk_mul_f32 v[200:201], v[88:89], v[188:189] op_sel_hi:[1,0]\n\t"
        "v_cvt_pk_bf16_f32 v192, v192, v193\n\t"
        "v_cvt_pk_bf16_f32 v193, v194, v195\n\t"
        "v_cvt_pk_bf16_f32 v194, v196, v197\n\t"
        "v_cvt_pk_bf16_f32 v195, v198, v199\n\t"
        "v_cvt_pk_bf16_f32 v196, v200, v201\n\t"
        "buffer_store_dwordx4 v[192:195], v232, s[40:43], s78 offen sc1\n\t"
        "s_mov_b32 exec_hi, 0\n\t"
        "buffer_store_dword v196, v232, s[40:43], s79 offen\n\t"
        "s_mov_b32 exec_hi, -1\n\t"
        "s_cmp_lg_u32 s53, 0\n\t"
        "s_cbranch_scc1 50f\n\t"
        "v_pk_fma_f32 v[242:243], v[80:81], 0, v[242:243] op_sel_hi:[1,0,1]\n\t"
        "v_pk_fma_f32 v[242:243], v[82:83], 0, v[242:243] op_sel_hi:[1,0,1]\n\t"
        "v_pk_fma_f32 v[242:243], v[84:85], 0, v[242:243] op_sel_hi:[1,0,1]\n\t"
        "v_pk_fma_f32 v[242:243], v[86:87], 0, v[242:243] op_sel_hi:[1,0,1]\n\t"
        "v_mov_b32 v226, 0x71800000\n\t"
        "v_add_f32 v242, v242, v243\n\t"
        "v_cmp_nge_f32 s[90:91], v226, v90\n\t"
        "v_cmp_neq_f32 vcc, 0, v242\n\t"
        "s_or_b64 vcc, vcc, s[90:91]\n\t"
        "s_cbranch_vccz 42f\n\t"
        "v_mov_b32 v226, 1\n\t"
        "ds_write_b32 v237, v226 offset:4\n\t"
        "42:\n\t"
        "s_waitcnt lgkmcnt(0)\n\t"
        "s_barrier\n\t"
        "ds_read_b32 v226, v237 offset:4\n\t"
        "s_waitcnt lgkmcnt(0)\n\t"
        "v_cmp_ne_u32 vcc, 0, v226\n\t"
        "s_cbranch_vccz 50f\n\t"
        "s_mov_b32 s53, 1\n\t"
        "s_branch 5b\n\t"
        "50:\n\t"
        "99:\n\t"
        :
        : "{v244}"(threadIdx.x), [karg] "s"((unsigned long long)g_ptrs), [b] "s"(b)
        : "v0", "v1", "v2", "v3", "v4", "v5", "v6", "v7", "v8", "v9", "v10", "v11", "v12", "v13", "v14", "v15", "v16", "v17", "v18", "v19", "v20", "v21", "v22", "v23", "v24", "v25", "v26", "v27", "v28", "v29", "v30", "v31", "v32", "v33", "v34", "v35", "v36", "v37", "v38", "v39", "v40", "v41", "v42", "v43", "v44", "v45", "v46", "v47", "v48", "v49", "v50", "v51", "v52", "v53", "v54", "v55", "v56", "v57", "v58", "v59", "v60", "v61", "v62", "v63", "v64", "v65", "v66", "v67", "v68", "v69", "v70", "v71", "v72", "v73", "v74", "v75", "v76", "v77", "v78", "v79", "v80", "v81", "v82", "v83", "v84", "v85", "v86", "v87", "v88", "v89", "v90", "v91", "v92", "v93", "v94", "v95", "v96", "v97", "v98", "v99", "v100", "v101", "v102", "v103", "v104", "v105", "v106", "v107", "v108", "v109", "v110", "v111", "v112", "v113", "v114", "v115", "v116", "v117", "v118", "v119", "v120", "v121", "v122", "v123", "v124", "v125", "v126", "v127", "v128", "v129", "v130", "v131", "v132", "v133", "v134", "v135", "v136", "v137", "v138", "v139", "v140", "v141", "v142", "v143", "v144", "v145", "v146", "v147", "v148", "v149", "v150", "v151", "v152", "v153", "v154", "v155", "v156", "v157", "v158", "v159", "v160", "v161", "v162", "v163", "v164", "v165", "v166", "v167", "v168", "v169", "v170", "v171", "v172", "v173", "v174", "v175", "v176", "v177", "v178", "v179", "v180", "v181", "v182", "v183", "v184", "v185", "v186", "v187", "v188", "v189", "v190", "v191", "v192", "v193", "v194", "v195", "v196", "v197", "v198", "v199", "v200", "v201", "v202", "v203", "v204", "v205", "v206", "v207", "v208", "v209", "v210", "v211", "v212", "v213", "v214", "v215", "v216", "v217", "v218", "v219", "v220", "v221", "v222", "v223", "v224", "v225", "v226", "v227", "v228", "v229", "v230", "v231", "v232", "v233", "v234", "v235", "v236", "v237", "v238", "v239", "v240", "v241", "v242", "v243", "s40", "s41", "s42", "s43", "s44", "s45", "s46", "s47", "s48", "s49", "s50", "s51", "s52", "s53", "s54", "s55", "s56", "s57", "s58", "s59", "s60", "s61", "s62", "s63", "s64", "s65", "s66", "s67", "s68", "s69", "s70", "s71", "s72", "s73", "s74", "s75", "s76", "s77", "s78", "s79", "s80", "s81", "s82", "s83", "s84", "s85", "s86", "s87", "s88", "s89", "s90", "s91", "scc", "vcc", "memory");

    STAMP(1, 1);
    STAMP(1, 2);
}

#define K3_LDS 163840
__global__ __launch_bounds__(256, 1) void k3_out(char* __restrict__ ws, u16* __restrict__ out) {
    int b; asm volatile("s_mov_b32 %0, %1" : "=s"(b) : "s"(__builtin_amdgcn_workgroup_id_x()));
    STAMP(2, 0);

    asm volatile(
        "s_load_dwordx4 s[40:43], %[karg], 0x0\n\t"
        "v_lshrrev_b32 v140, 6, v255\n\t"
        "v_and_b32 v141, 63, v255\n\t"
        "s_and_b32 s62, %[b], 7\n\t"
        "v_readfirstlane_b32 s46, v140\n\t"
        "s_lshr_b32 s63, %[b], 3\n\t"
        "v_lshlrev_b32 v128, 4, v141\n\t"
        "v_and_b32 v142, 15, v141\n\t"
        "v_lshrrev_b32 v143, 4, v141\n\t"
        "s_and_b32 s64, s63, 3\n\t"
        "s_lshl_b32 s62, s62, 2\n\t"
        "s_add_u32 s62, s62, s64\n\t"
        "s_lshr_b32 s63, s63, 2\n\t"
        "s_lshl_b32 s67, s62, 2\n\t"
        "s_add_u32 s67, s67, s46\n\t"
        "s_mul_i32 s66, s63, 165888\n\t"
        "s_add_u32 s66, s66, 8642560\n\t"
        "s_lshl_b32 s53, s46, 10\n\t"
        "s_add_u32 s52, s53, 82944\n\t"
        "s_mov_b32 s49, s52\n\t"
        "s_mul_i32 s50, s67, 36864\n\t"
        "s_add_u32 s50, s50, 9969664\n\t"
        "s_add_u32 s51, s50, 18432\n\t"
        "s_lshl_b32 s54, s67, 6\n\t"
        "s_add_u32 s54, s54, 8634368\n\t"
        "s_mul_i32 s69, s67, 18432\n\t"
        "s_mul_i32 s64, s63, 144\n\t"
        "s_add_u32 s69, s69, s64\n\t"
        "v_lshlrev_b32 v137, 2, v142\n\t"
        "s_waitcnt lgkmcnt(0)\n\t"
        "s_add_u32 s44, s42, s69\n\t"
        "s_addc_u32 s45, s43, 0\n\t"
        "s_add_u32 s56, s40, s66\n\t"
        "s_addc_u32 s57, s41, 0\n\t"
        "s_mov_b32 s42, 0x2000000\n\t"
        "s_mov_b32 s43, 0x00020000\n\t"
        "s_mov_b32 s58, 0x2000000\n\t"
        "s_mov_b32 s59, 0x00020000\n\t"
        "buffer_load_dwordx4 a[0:3], v128, s[40:43], s50 offen offset:0\n\t"
        "buffer_load_dwordx4 a[72:75], v128, s[40:43], s51 offen offset:0\n\t"
        "s_mov_b32 m0, s53\n\t"
        "s_nop 0\n\t"
        "buffer_load_dwordx4 v128, s[56:59], s53 offen lds\n\t"
        "s_add_u32 s53, s53, 4096\n\t"
        "s_mov_b32 m0, s53\n\t"
        "s_nop 0\n\t"
        "buffer_load_dwordx4 v128, s[56:59], s53 offen lds\n\t"
        "s_add_u32 s53, s53, 4096\n\t"
        "s_mov_b32 m0, s52\n\t"
        "s_nop 0\n\t"
        "buffer_load_dwordx4 v128, s[56:59], s49 offen lds\n\t"
        "s_add_u32 s52, s52, 4096\n\t"
        "s_add_u32 s49, s49, 4096\n\t"
        "s_mov_b32 m0, s52\n\t"
        "s_nop 0\n\t"
        "buffer_load_dwordx4 v128, s[56:59], s49 offen lds\n\t"
        "s_add_u32 s52, s52, 4096\n\t"
        "s_add_u32 s49, s49, 4096\n\t"
        "buffer_load_dword v136, v137, s[40:43], s54 offen\n\t"
        "buffer_load_dwordx4 a[4:7], v128, s[40:43], s50 offen offset:1024\n\t"
        "buffer_load_dwordx4 a[76:79], v128, s[40:43], s51 offen offset:1024\n\t"
        "s_mov_b32 m0, s53\n\t"
        "s_nop 0\n\t"
        "buffer_load_dwordx4 v128, s[56:59], s53 offen lds\n\t"
        "s_add_u32 s53, s53, 4096\n\t"
        "s_mov_b32 m0, s52\n\t"
        "s_nop 0\n\t"
        "buffer_load_dwordx4 v128, s[56:59], s49 offen lds\n\t"
        "s_add_u32 s52, s52, 4096\n\t"
        "s_add_u32 s49, s49, 4096\n\t"
        "buffer_load_dwordx4 a[8:11], v128, s[40:43], s50 offen offset:2048\n\t"
        "buffer_load_dwordx4 a[80:83], v128, s[40:43], s51 offen offset:2048\n\t"
        "s_mov_b32 m0, s53\n\t"
        "s_nop 0\n\t"
        "buffer_load_dwordx4 v128, s[56:59], s53 offen lds\n\t"
        "s_add_u32 s53, s53, 4096\n\t"
        "s_mov_b32 m0, s52\n\t"
        "s_nop 0\n\t"
        "buffer_load_dwordx4 v128, s[56:59], s49 offen lds\n\t"
        "s_add_u32 s52, s52, 4096\n\t"
        "s_add_u32 s49, s49, 4096\n\t"
        "buffer_load_dwordx4 a[12:15], v128, s[40:43], s50 offen offset:3072\n\t"
        "s_add_u32 s50, s50, 4096\n\t"
        "buffer_load_dwordx4 a[84:87], v128, s[40:43], s51 offen offset:3072\n\t"
        "s_add_u32 s51, s51, 4096\n\t"
        "s_mov_b32 m0, s53\n\t"
        "s_nop 0\n\t"
        "buffer_load_dwordx4 v128, s[56:59], s53 offen lds\n\t"
        "s_add_u32 s53, s53, 4096\n\t"
        "s_mov_b32 m0, s52\n\t"
        "s_nop 0\n\t"
        "buffer_load_dwordx4 v128, s[56:59], s49 offen lds\n\t"
        "s_add_u32 s52, s52, 4096\n\t"
        "s_add_u32 s49, s49, 4096\n\t"
        "buffer_load_dwordx4 a[16:19], v128, s[40:43], s50 offen offset:0\n\t"
        "buffer_load_dwordx4 a[88:91], v128, s[40:43], s51 offen offset:0\n\t"
        "s_mov_b32 m0, s53\n\t"
        "s_nop 0\n\t"
        "buffer_load_dwordx4 v128, s[56:59], s53 offen lds\n\t"
        "s_add_u32 s53, s53, 4096\n\t"
        "s_mov_b32 m0, s52\n\t"
        "s_nop 0\n\t"
        "buffer_load_dwordx4 v128, s[56:59], s49 offen lds\n\t"
        "s_add_u32 s52, s52, 4096\n\t"
        "s_add_u32 s49, s49, 4096\n\t"
        "v_mov_b32 v129, v128\n\t"
        "v_add_u32 v130, 82944, v128\n\t"
        "v_lshlrev_b32 v131, 7, v143\n\t"
        "v_and_b32 v140, 7, v142\n\t"
        "v_lshl_add_u32 v131, v140, 4, v131\n\t"
        "v_cmp_gt_u32 vcc, 8, v142\n\t"
        "v_add_u32 v133, 160768, v131\n\t"
        "v_add_u32 v132, 2048, v131\n\t"
        "v_cndmask_b32 v133, v132, v133, vcc\n\t"
        "v_lshrrev_b32 v140, 3, v142\n\t"
        "v_mul_u32_u24 v140, 4608, v140\n\t"
        "v_add_u32 v140, 4096, v140\n\t"
        "v_add_u32 v131, v131, v140\n\t"
        "v_add_u32 v132, 82944, v131\n\t"
        "v_mul_u32_u24 v140, 1152, v142\n\t"
        "v_lshl_add_u32 v138, v143, 4, v140\n\t"
        "v_lshl_add_u32 v139, v143, 3, v140\n\t"
        "v_add_u32 v139, 128, v139\n\t"
        "s_mov_b32 s62, 0\n\t"
        "s_mov_b32 s63, -1\n\t"
        "s_waitcnt vmcnt(17)\n\t"
        "s_barrier\n\t"
        "ds_read_b128 v[0:3], v129 offset:0\n\t"
        "ds_read_b128 v[4:7], v129 offset:1024\n\t"
        "ds_read_b128 v[8:11], v129 offset:2048\n\t"
        "ds_read_b128 v[12:15], v129 offset:3072\n\t"
        "ds_read_b128 v[16:19], v130 offset:0\n\t"
        "ds_read_b128 v[20:23], v130 offset:1024\n\t"
        "ds_read_b128 v[24:27], v130 offset:2048\n\t"
        "ds_read_b128 v[28:31], v130 offset:3072\n\t"
        "v_add_u32 v129, 4608, v129\n\t"
        "v_add_u32 v130, 4608, v130\n\t"
        "buffer_load_dwordx4 a[20:23], v128, s[40:43], s50 offen offset:1024\n\t"
        "buffer_load_dwordx4 a[92:95], v128, s[40:43], s51 offen offset:1024\n\t"
        "s_mov_b32 m0, s53\n\t"
        "s_nop 0\n\t"
        "buffer_load_dwordx4 v128, s[56:59], s53 offen lds\n\t"
        "s_add_u32 s53, s53, 4096\n\t"
        "s_mov_b32 m0, s52\n\t"
        "s_nop 0\n\t"
        "buffer_load_dwordx4 v128, s[56:59], s49 offen lds\n\t"
        "s_add_u32 s52, s52, 4096\n\t"
        "s_add_u32 s49, s49, 4096\n\t"
        "s_waitcnt vmcnt(12)\n\t"
        "s_barrier\n\t"
        "s_waitcnt lgkmcnt(0)\n\t"
        "v_mfma_f32_16x16x32_bf16 v[80:83], v[0:3], a[0:3], 0\n\t"
        "ds_read_b128 v[32:35], v129 offset:0\n\t"
        "ds_read_b128 v[36:39], v129 offset:1024\n\t"
        "v_mfma_f32_16x16x32_bf16 v[84:87], v[4:7], a[0:3], 0\n\t"
        "ds_read_b128 v[40:43], v129 offset:2048\n\t"
        "ds_read_b128 v[44:47], v129 offset:3072\n\t"
        "v_mfma_f32_16x16x32_bf16 v[88:91], v[8:11], a[0:3], 0\n\t"
        "ds_read_b128 v[48:51], v130 offset:0\n\t"
        "ds_read_b128 v[52:55], v130 offset:1024\n\t"
        "v_mfma_f32_16x16x32_bf16 v[92:95], v[12:15], a[0:3], 0\n\t"
        "ds_read_b128 v[56:59], v130 offset:2048\n\t"
        "ds_read_b128 v[60:63], v130 offset:3072\n\t"
        "v_mfma_f32_16x16x32_bf16 v[104:107], v[16:19], a[72:75], 0\n\t"
        "ds_read_b128 v[64:67], v131\n\t"
        "v_add_u32 v131, 9216, v131\n\t"
        "ds_read_b128 v[68:71], v132\n\t"
        "v_add_u32 v132, 9216, v132\n\t"
        "v_mfma_f32_16x16x32_bf16 v[108:111], v[20:23], a[72:75], 0\n\t"
        "v_add_u32 v129, 4608, v129\n\t"
        "v_add_u32 v130, 4608, v130\n\t"
        "buffer_load_dwordx4 a[24:27], v128, s[40:43], s50 offen offset:2048\n\t"
        "v_mfma_f32_16x16x32_bf16 v[112:115], v[24:27], a[72:75], 0\n\t"
        "buffer_load_dwordx4 a[96:99], v128, s[40:43], s51 offen offset:2048\n\t"
        "s_mov_b32 m0, s53\n\t"
        "s_nop 0\n\t"
        "buffer_load_dwordx4 v128, s[56:59], s53 offen lds\n\t"
        "s_add_u32 s53, s53, 4096\n\t"
        "v_mfma_f32_16x16x32_bf16 v[116:119], v[28:31], a[72:75], 0\n\t"
        "s_mov_b32 m0, s52\n\t"
        "s_nop 0\n\t"
        "buffer_load_dwordx4 v128, s[56:59], s49 offen lds\n\t"
        "s_add_u32 s52, s52, 4096\n\t"
        "s_add_u32 s49, s49, 4096\n\t"
        "s_waitcnt lgkmcnt(0)\n\t"
        "v_mfma_f32_16x16x32_bf16 v[80:83], v[32:35], a[4:7], v[80:83]\n\t"
        "ds_read_b128 v[0:3], v129 offset:0\n\t"
        "ds_read_b128 v[4:7], v129 offset:1024\n\t"
        "v_mfma_f32_16x16x32_bf16 v[84:87], v[36:39], a[4:7], v[84:87]\n\t"
        "ds_read_b128 v[8:11], v129 offset:2048\n\t"
        "v_mfma_f32_16x16x32_bf16 v[88:91], v[40:43], a[4:7], v[88:91]\n\t"
        "ds_read_b128 v[12:15], v129 offset:3072\n\t"
        "v_mfma_f32_16x16x32_bf16 v[92:95], v[44:47], a[4:7], v[92:95]\n\t"
        "ds_read_b128 v[16:19], v130 offset:0\n\t"
        "v_mfma_f32_16x16x32_bf16 v[104:107], v[48:51], a[76:79], v[104:107]\n\t"
        "ds_read_b128 v[20:23], v130 offset:1024\n\t"
        "v_mfma_f32_16x16x32_bf16 v[108:111], v[52:55], a[76:79], v[108:111]\n\t"
        "ds_read_b128 v[24:27], v130 offset:2048\n\t"
        "v_mfma_f32_16x16x32_bf16 v[112:115], v[56:59], a[76:79], v[112:115]\n\t"
        "ds_read_b128 v[28:31], v130 offset:3072\n\t"
        "v_mfma_f32_16x16x32_bf16 v[116:119], v[60:63], a[76:79], v[116:119]\n\t"
        "v_add_u32 v129, 4608, v129\n\t"
        "v_add_u32 v130, 4608, v130\n\t"
        "v_mfma_f32_16x16x32_bf16 v[96:99], v[64:67], a[0:3], 0\n\t"
        "buffer_load_dwordx4 a[28:31], v128, s[40:43], s50 offen offset:3072\n\t"
        "s_add_u32 s50, s50, 4096\n\t"
        "v_mfma_f32_16x16x32_bf16 v[120:123], v[68:71], a[72:75], 0\n\t"
        "buffer_load_dwordx4 a[100:103], v128, s[40:43], s51 offen offset:3072\n\t"
        "s_add_u32 s51, s51, 4096\n\t"
        "v_mfma_f32_16x16x32_bf16 v[100:103], v[64:67], a[4:7], 0\n\t"
        "s_mov_b32 m0, s53\n\t"
        "s_nop 0\n\t"
        "buffer_load_dwordx4 v128, s[56:59], s53 offen lds\n\t"
        "s_add_u32 s53, s53, 4096\n\t"
        "v_mfma_f32_16x16x32_bf16 v[124:127], v[68:71], a[76:79], 0\n\t"
        "s_mov_b32 m0, s52\n\t"
        "s_nop 0\n\t"
        "buffer_load_dwordx4 v128, s[56:59], s49 offen lds\n\t"
        "s_add_u32 s52, s52, 4096\n\t"
        "s_add_u32 s49, s49, 4096\n\t"
        "s_waitcnt vmcnt(12)\n\t"
        "s_barrier\n\t"
        "s_waitcnt lgkmcnt(0)\n\t"
        "v_mfma_f32_16x16x32_bf16 v[80:83], v[0:3], a[8:11], v[80:83]\n\t"
        "ds_read_b128 v[32:35], v129 offset:0\n\t"
        "ds_read_b128 v[36:39], v129 offset:1024\n\t"
        "ds_read_b128 v[40:43], v129 offset:2048\n\t"
        "v_mfma_f32_16x16x32_bf16 v[84:87], v[4:7], a[8:11], v[84:87]\n\t"
        "ds_read_b128 v[44:47], v129 offset:3072\n\t"
        "ds_read_b128 v[48:51], v130 offset:0\n\t"
        "v_mfma_f32_16x16x32_bf16 v[88:91], v[8:11], a[8:11], v[88:91]\n\t"
        "ds_read_b128 v[52:55], v130 offset:1024\n\t"
        "ds_read_b128 v[56:59], v130 offset:2048\n\t"
        "v_mfma_f32_16x16x32_bf16 v[92:95], v[12:15], a[8:11], v[92:95]\n\t"
        "ds_read_b128 v[60:63], v130 offset:3072\n\t"
        "ds_read_b128 v[72:75], v131\n\t"
        "v_add_u32 v131, 9216, v131\n\t"
        "v_mfma_f32_16x16x32_bf16 v[104:107], v[16:19], a[80:83], v[104:107]\n\t"
        "ds_read_b128 v[76:79], v132\n\t"
        "v_add_u32 v132, 9216, v132\n\t"
        "v_add_u32 v129, 4608, v129\n\t"
        "v_add_u32 v130, 4608, v130\n\t"
        "v_mfma_f32_16x16x32_bf16 v[108:111], v[20:23], a[80:83], v[108:111]\n\t"
        "buffer_load_dwordx4 a[32:35], v128, s[40:43], s50 offen offset:0\n\t"
        "buffer_load_dwordx4 a[104:107], v128, s[40:43], s51 offen offset:0\n\t"
        "v_mfma_f32_16x16x32_bf16 v[112:115], v[24:27], a[80:83], v[112:115]\n\t"
        "s_mov_b32 m0, s53\n\t"
        "s_nop 0\n\t"
        "buffer_load_dwordx4 v128, s[56:59], s53 offen lds\n\t"
        "s_add_u32 s53, s53, 4096\n\t"
        "s_mov_b32 m0, s53\n\t"
        "s_nop 0\n\t"
        "buffer_load_dwordx4 v128, s[56:59], s53 offen lds\n\t"
        "s_add_u32 s53, s53, 4096\n\t"
        "v_mfma_f32_16x16x32_bf16 v[116:119], v[28:31], a[80:83], v[116:119]\n\t"
        "s_mov_b32 m0, s52\n\t"
        "s_nop 0\n\t"
        "buffer_load_dwordx4 v128, s[56:59], s49 offen lds\n\t"
        "s_add_u32 s52, s52, 4096\n\t"
        "s_add_u32 s49, s49, 4096\n\t"
        "s_mov_b32 m0, s52\n\t"
        "s_nop 0\n\t"
        "buffer_load_dwordx4 v128, s[56:59], s49 offen lds\n\t"
        "s_add_u32 s52, s52, 4096\n\t"
        "s_add_u32 s49, s49, 4096\n\t"
        "s_waitcnt lgkmcnt(0)\n\t"
        "v_mfma_f32_16x16x32_bf16 v[80:83], v[32:35], a[12:15], v[80:83]\n\t"
        "ds_read_b128 v[0:3], v129 offset:0\n\t"
        "ds_read_b128 v[4:7], v129 offset:1024\n\t"
        "v_mfma_f32_16x16x32_bf16 v[84:87], v[36:39], a[12:15], v[84:87]\n\t"
        "ds_read_b128 v[8:11], v129 offset:2048\n\t"
        "v_mfma_f32_16x16x32_bf16 v[88:91], v[40:43], a[12:15], v[88:91]\n\t"
        "ds_read_b128 v[12:15], v129 offset:3072\n\t"
        "v_mfma_f32_16x16x32_bf16 v[92:95], v[44:47], a[12:15], v[92:95]\n\t"
        "ds_read_b128 v[16:19], v130 offset:0\n\t"
        "v_mfma_f32_16x16x32_bf16 v[104:107], v[48:51], a[84:87], v[104:107]\n\t"
        "ds_read_b128 v[20:23], v130 offset:1024\n\t"
        "v_mfma_f32_16x16x32_bf16 v[108:111], v[52:55], a[84:87], v[108:111]\n\t"
        "ds_read_b128 v[24:27], v130 offset:2048\n\t"
        "v_mfma_f32_16x16x32_bf16 v[112:115], v[56:59], a[84:87], v[112:115]\n\t"
        "ds_read_b128 v[28:31], v130 offset:3072\n\t"
        "v_mfma_f32_16x16x32_bf16 v[116:119], v[60:63], a[84:87], v[116:119]\n\t"
        "v_add_u32 v129, 4608, v129\n\t"
        "v_add_u32 v130, 4608, v130\n\t"
        "v_mfma_f32_16x16x32_bf16 v[96:99], v[72:75], a[8:11], v[96:99]\n\t"
        "buffer_load_dwordx4 a[36:39], v128, s[40:43], s50 offen offset:1024\n\t"
        "v_mfma_f32_16x16x32_bf16 v[120:123], v[76:79], a[80:83], v[120:123]\n\t"
        "buffer_load_dwordx4 a[108:111], v128, s[40:43], s51 offen offset:1024\n\t"
        "v_mfma_f32_16x16x32_bf16 v[100:103], v[72:75], a[12:15], v[100:103]\n\t"
        "s_mov_b32 m0, s53\n\t"
        "s_nop 0\n\t"
        "buffer_load_dwordx4 v128, s[56:59], s53 offen lds\n\t"
        "s_add_u32 s53, s53, 4096\n\t"
        "v_mfma_f32_16x16x32_bf16 v[124:127], v[76:79], a[84:87], v[124:127]\n\t"
        "s_mov_b32 m0, s52\n\t"
        "s_nop 0\n\t"
        "buffer_load_dwordx4 v128, s[56:59], s49 offen lds\n\t"
        "s_add_u32 s52, s52, 4096\n\t"
        "s_add_u32 s49, s49, 4096\n\t"
        "s_waitcnt vmcnt(14)\n\t"
        "s_barrier\n\t"
        "s_waitcnt lgkmcnt(0)\n\t"
        "v_mfma_f32_16x16x32_bf16 v[80:83], v[0:3], a[16:19], v[80:83]\n\t"
        "ds_read_b128 v[32:35], v129 offset:0\n\t"
        "ds_read_b128 v[36:39], v129 offset:1024\n\t"
        "v_mfma_f32_16x16x32_bf16 v[84:87], v[4:7], a[16:19], v[84:87]\n\t"
        "ds_read_b128 v[40:43], v129 offset:2048\n\t"
        "ds_read_b128 v[44:47], v129 offset:3072\n\t"
        "v_mfma_f32_16x16x32_bf16 v[88:91], v[8:11], a[16:19], v[88:91]\n\t"
        "ds_read_b128 v[48:51], v130 offset:0\n\t"
        "ds_read_b128 v[52:55], v130 offset:1024\n\t"
        "v_mfma_f32_16x16x32_bf16 v[92:95], v[12:15], a[16:19], v[92:95]\n\t"
        "ds_read_b128 v[56:59], v130 offset:2048\n\t"
        "ds_read_b128 v[60:63], v130 offset:3072\n\t"
        "v_mfma_f32_16x16x32_bf16 v[104:107], v[16:19], a[88:91], v[104:107]\n\t"
        "ds_read_b128 v[64:67], v131\n\t"
        "v_add_u32 v131, 9216, v131\n\t"
        "ds_read_b128 v[68:71], v132\n\t"
        "v_add_u32 v132, 9216, v132\n\t"
        "v_mfma_f32_16x16x32_bf16 v[108:111], v[20:23], a[88:91], v[108:111]\n\t"
        "v_add_u32 v129, 4608, v129\n\t"
        "v_add_u32 v130, 4608, v130\n\t"
        "buffer_load_dwordx4 a[40:43], v128, s[40:43], s50 offen offset:2048\n\t"
        "v_mfma_f32_16x16x32_bf16 v[112:115], v[24:27], a[88:91], v[112:115]\n\t"
        "buffer_load_dwordx4 a[112:115], v128, s[40:43], s51 offen offset:2048\n\t"
        "s_mov_b32 m0, s53\n\t"
        "s_nop 0\n\t"
        "buffer_load_dwordx4 v128, s[56:59], s53 offen lds\n\t"
        "s_add_u32 s53, s53, 4096\n\t"
        "v_mfma_f32_16x16x32_bf16 v[116:119], v[28:31], a[88:91], v[116:119]\n\t"
        "s_mov_b32 m0, s52\n\t"
        "s_nop 0\n\t"
        "buffer_load_dwordx4 v128, s[56:59], s49 offen lds\n\t"
        "s_add_u32 s52, s52, 4096\n\t"
        "s_add_u32 s49, s49, 4096\n\t"
        "s_waitcnt lgkmcnt(0)\n\t"
        "v_mfma_f32_16x16x32_bf16 v[80:83], v[32:35], a[20:23], v[80:83]\n\t"
        "ds_read_b128 v[0:3], v129 offset:0\n\t"
        "ds_read_b128 v[4:7], v129 offset:1024\n\t"
        "v_mfma_f32_16x16x32_bf16 v[84:87], v[36:39], a[20:23], v[84:87]\n\t"
        "ds_read_b128 v[8:11], v129 offset:2048\n\t"
        "v_mfma_f32_16x16x32_bf16 v[88:91], v[40:43], a[20:23], v[88:91]\n\t"
        "ds_read_b128 v[12:15], v129 offset:3072\n\t"
        "v_mfma_f32_16x16x32_bf16 v[92:95], v[44:47], a[20:23], v[92:95]\n\t"
        "ds_read_b128 v[16:19], v130 offset:0\n\t"
        "v_mfma_f32_16x16x32_bf16 v[104:107], v[48:51], a[92:95], v[104:107]\n\t"
        "ds_read_b128 v[20:23], v130 offset:1024\n\t"
        "v_mfma_f32_16x16x32_bf16 v[108:111], v[52:55], a[92:95], v[108:111]\n\t"
        "ds_read_b128 v[24:27], v130 offset:2048\n\t"
        "v_mfma_f32_16x16x32_bf16 v[112:115], v[56:59], a[92:95], v[112:115]\n\t"
        "ds_read_b128 v[28:31], v130 offset:3072\n\t"
        "v_mfma_f32_16x16x32_bf16 v[116:119], v[60:63], a[92:95], v[116:119]\n\t"
        "v_add_u32 v129, 4608, v129\n\t"
        "v_add_u32 v130, 4608, v130\n\t"
        "v_mfma_f32_16x16x32_bf16 v[96:99], v[64:67], a[16:19], v[96:99]\n\t"
        "buffer_load_dwordx4 a[44:47], v128, s[40:43], s50 offen offset:3072\n\t"
        "s_add_u32 s50, s50, 4096\n\t"
        "v_mfma_f32_16x16x32_bf16 v[120:123], v[68:71], a[88:91], v[120:123]\n\t"
        "buffer_load_dwordx4 a[116:119], v128, s[40:43], s51 offen offset:3072\n\t"
        "s_add_u32 s51, s51, 4096\n\t"
        "v_mfma_f32_16x16x32_bf16 v[100:103], v[64:67], a[20:23], v[100:103]\n\t"
        "s_mov_b32 m0, s53\n\t"
        "s_nop 0\n\t"
        "buffer_load_dwordx4 v128, s[56:59], s53 offen lds\n\t"
        "s_add_u32 s53, s53, 4096\n\t"
        "v_mfma_f32_16x16x32_bf16 v[124:127], v[68:71], a[92:95], v[124:127]\n\t"
        "s_mov_b32 m0, s52\n\t"
        "s_nop 0\n\t"
        "buffer_load_dwordx4 v128, s[56:59], s49 offen lds\n\t"
        "s_add_u32 s52, s52, 4096\n\t"
        "s_add_u32 s49, s49, 4096\n\t"
        "s_waitcnt vmcnt(12)\n\t"
        "s_barrier\n\t"
        "s_waitcnt lgkmcnt(0)\n\t"
        "v_mfma_f32_16x16x32_bf16 v[80:83], v[0:3], a[24:27], v[80:83]\n\t"
        "ds_read_b128 v[32:35], v129 offset:0\n\t"
        "ds_read_b128 v[36:39], v129 offset:1024\n\t"
        "v_mfma_f32_16x16x32_bf16 v[84:87], v[4:7], a[24:27], v[84:87]\n\t"
        "ds_read_b128 v[40:43], v129 offset:2048\n\t"
        "ds_read_b128 v[44:47], v129 offset:3072\n\t"
        "v_mfma_f32_16x16x32_bf16 v[88:91], v[8:11], a[24:27], v[88:91]\n\t"
        "ds_read_b128 v[48:51], v130 offset:0\n\t"
        "ds_read_b128 v[52:55], v130 offset:1024\n\t"
        "v_mfma_f32_16x16x32_bf16 v[92:95], v[12:15], a[24:27], v[92:95]\n\t"
        "ds_read_b128 v[56:59], v130 offset:2048\n\t"
        "ds_read_b128 v[60:63], v130 offset:3072\n\t"
        "v_mfma_f32_16x16x32_bf16 v[104:107], v[16:19], a[96:99], v[104:107]\n\t"
        "ds_read_b128 v[72:75], v131\n\t"
        "v_add_u32 v131, 9216, v131\n\t"
        "ds_read_b128 v[76:79], v132\n\t"
        "v_add_u32 v132, 9216, v132\n\t"
        "v_mfma_f32_16x16x32_bf16 v[108:111], v[20:23], a[96:99], v[108:111]\n\t"
        "v_add_u32 v129, 4608, v129\n\t"
        "v_add_u32 v130, 4608, v130\n\t"
        "buffer_load_dwordx4 a[48:51], v128, s[40:43], s50 offen offset:0\n\t"
        "v_mfma_f32_16x16x32_bf16 v[112:115], v[24:27], a[96:99], v[112:115]\n\t"
        "buffer_load_dwordx4 a[120:123], v128, s[40:43], s51 offen offset:0\n\t"
        "s_mov_b32 m0, s53\n\t"
        "s_nop 0\n\t"
        "buffer_load_dwordx4 v128, s[56:59], s53 offen lds\n\t"
        "s_add_u32 s53, s53, 4096\n\t"
        "v_mfma_f32_16x16x32_bf16 v[116:119], v[28:31], a[96:99], v[116:119]\n\t"
        "s_mov_b32 m0, s52\n\t"
        "s_nop 0\n\t"
        "buffer_load_dwordx4 v128, s[56:59], s49 offen lds\n\t"
        "s_add_u32 s52, s52, 4096\n\t"
        "s_add_u32 s49, s49, 4096\n\t"
        "s_waitcnt lgkmcnt(0)\n\t"
        "v_mfma_f32_16x16x32_bf16 v[80:83], v[32:35], a[28:31], v[80:83]\n\t"
        "ds_read_b128 v[0:3], v129 offset:0\n\t"
        "ds_read_b128 v[4:7], v129 offset:1024\n\t"
        "v_mfma_f32_16x16x32_bf16 v[84:87], v[36:39], a[28:31], v[84:87]\n\t"
        "ds_read_b128 v[8:11], v129 offset:2048\n\t"
        "v_mfma_f32_16x16x32_bf16 v[88:91], v[40:43], a[28:31], v[88:91]\n\t"
        "ds_read_b128 v[12:15], v129 offset:3072\n\t"
        "v_mfma_f32_16x16x32_bf16 v[92:95], v[44:47], a[28:31], v[92:95]\n\t"
        "ds_read_b128 v[16:19], v130 offset:0\n\t"
        "v_mfma_f32_16x16x32_bf16 v[104:107], v[48:51], a[100:103], v[104:107]\n\t"
        "ds_read_b128 v[20:23], v130 offset:1024\n\t"
        "v_mfma_f32_16x16x32_bf16 v[108:111], v[52:55], a[100:103], v[108:111]\n\t"
        "ds_read_b128 v[24:27], v130 offset:2048\n\t"
        "v_mfma_f32_16x16x32_bf16 v[112:115], v[56:59], a[100:103], v[112:115]\n\t"
        "ds_read_b128 v[28:31], v130 offset:3072\n\t"
        "v_mfma_f32_16x16x32_bf16 v[116:119], v[60:63], a[100:103], v[116:119]\n\t"
        "v_add_u32 v129, 4608, v129\n\t"
        "v_add_u32 v130, 4608, v130\n\t"
        "v_mfma_f32_16x16x32_bf16 v[96:99], v[72:75], a[24:27], v[96:99]\n\t"
        "buffer_load_dwordx4 a[52:55], v128, s[40:43], s50 offen offset:1024\n\t"
        "v_mfma_f32_16x16x32_bf16 v[120:123], v[76:79], a[96:99], v[120:123]\n\t"
        "buffer_load_dwordx4 a[124:127], v128, s[40:43], s51 offen offset:1024\n\t"
        "v_mfma_f32_16x16x32_bf16 v[100:103], v[72:75], a[28:31], v[100:103]\n\t"
        "s_mov_b32 m0, s53\n\t"
        "s_nop 0\n\t"
        "buffer_load_dwordx4 v128, s[56:59], s53 offen lds\n\t"
        "s_add_u32 s53, s53, 4096\n\t"
        "v_mfma_f32_16x16x32_bf16 v[124:127], v[76:79], a[100:103], v[124:127]\n\t"
        "s_mov_b32 m0, s52\n\t"
        "s_nop 0\n\t"
        "buffer_load_dwordx4 v128, s[56:59], s49 offen lds\n\t"
        "s_add_u32 s52, s52, 4096\n\t"
        "s_add_u32 s49, s49, 4096\n\t"
        "s_waitcnt vmcnt(12)\n\t"
        "s_barrier\n\t"
        "s_waitcnt lgkmcnt(0)\n\t"
        "v_mfma_f32_16x16x32_bf16 v[80:83], v[0:3], a[32:35], v[80:83]\n\t"
        "ds_read_b128 v[32:35], v129 offset:0\n\t"
        "ds_read_b128 v[36:39], v129 offset:1024\n\t"
        "v_mfma_f32_16x16x32_bf16 v[84:87], v[4:7], a[32:35], v[84:87]\n\t"
        "ds_read_b128 v[40:43], v129 offset:2048\n\t"
        "ds_read_b128 v[44:47], v129 offset:3072\n\t"
        "v_mfma_f32_16x16x32_bf16 v[88:91], v[8:11], a[32:35], v[88:91]\n\t"
        "ds_read_b128 v[48:51], v130 offset:0\n\t"
        "ds_read_b128 v[52:55], v130 offset:1024\n\t"
        "v_mfma_f32_16x16x32_bf16 v[92:95], v[12:15], a[32:35], v[92:95]\n\t"
        "ds_read_b128 v[56:59], v130 offset:2048\n\t"
        "ds_read_b128 v[60:63], v130 offset:3072\n\t"
        "v_mfma_f32_16x16x32_bf16 v[104:107], v[16:19], a[104:107], v[104:107]\n\t"
        "ds_read_b128 v[64:67], v131\n\t"
        "v_add_u32 v131, 9216, v131\n\t"
        "ds_read_b128 v[68:71], v132\n\t"
        "v_add_u32 v132, 9216, v132\n\t"
        "v_mfma_f32_16x16x32_bf16 v[108:111], v[20:23], a[104:107], v[108:111]\n\t"
        "v_add_u32 v129, 4608, v129\n\t"
        "v_add_u32 v130, 4608, v130\n\t"
        "buffer_load_dwordx4 a[56:59], v128, s[40:43], s50 offen offset:2048\n\t"
        "v_mfma_f32_16x16x32_bf16 v[112:115], v[24:27], a[104:107], v[112:115]\n\t"
        "buffer_load_dwordx4 a[128:131], v128, s[40:43], s51 offen offset:2048\n\t"
        "s_mov_b32 m0, s53\n\t"
        "s_nop 0\n\t"
        "buffer_load_dwordx4 v128, s[56:59], s53 offen lds\n\t"
        "s_add_u32 s53, s53, 4096\n\t"
        "v_mfma_f32_16x16x32_bf16 v[116:119], v[28:31], a[104:107], v[116:119]\n\t"
        "s_mov_b32 m0, s52\n\t"
        "s_nop 0\n\t"
        "buffer_load_dwordx4 v128, s[56:59], s49 offen lds\n\t"
        "s_add_u32 s52, s52, 4096\n\t"
        "s_add_u32 s49, s49, 4096\n\t"
        "s_waitcnt lgkmcnt(0)\n\t"
        "v_mfma_f32_16x16x32_bf16 v[80:83], v[32:35], a[36:39], v[80:83]\n\t"
        "ds_read_b128 v[0:3], v129 offset:0\n\t"
        "ds_read_b128 v[4:7], v129 offset:1024\n\t"
        "v_mfma_f32_16x16x32_bf16 v[84:87], v[36:39], a[36:39], v[84:87]\n\t"
        "ds_read_b128 v[8:11], v129 offset:2048\n\t"
        "v_mfma_f32_16x16x32_bf16 v[88:91], v[40:43], a[36:39], v[88:91]\n\t"
        "ds_read_b128 v[12:15], v129 offset:3072\n\t"
        "v_mfma_f32_16x16x32_bf16 v[92:95], v[44:47], a[36:39], v[92:95]\n\t"
        "ds_read_b128 v[16:19], v130 offset:0\n\t"
        "v_mfma_f32_16x16x32_bf16 v[104:107], v[48:51], a[108:111], v[104:107]\n\t"
        "ds_read_b128 v[20:23], v130 offset:1024\n\t"
        "v_mfma_f32_16x16x32_bf16 v[108:111], v[52:55], a[108:111], v[108:111]\n\t"
        "ds_read_b128 v[24:27], v130 offset:2048\n\t"
        "v_mfma_f32_16x16x32_bf16 v[112:115], v[56:59], a[108:111], v[112:115]\n\t"
        "ds_read_b128 v[28:31], v130 offset:3072\n\t"
        "v_mfma_f32_16x16x32_bf16 v[116:119], v[60:63], a[108:111], v[116:119]\n\t"
        "v_add_u32 v129, 4608, v129\n\t"
        "v_add_u32 v130, 4608, v130\n\t"
        "v_mfma_f32_16x16x32_bf16 v[96:99], v[64:67], a[32:35], v[96:99]\n\t"
        "buffer_load_dwordx4 a[60:63], v128, s[40:43], s50 offen offset:3072\n\t"
        "s_add_u32 s50, s50, 4096\n\t"
        "v_mfma_f32_16x16x32_bf16 v[120:123], v[68:71], a[104:107], v[120:123]\n\t"
        "buffer_load_dwordx4 a[132:135], v128, s[40:43], s51 offen offset:3072\n\t"
        "s_add_u32 s51, s51, 4096\n\t"
        "v_mfma_f32_16x16x32_bf16 v[100:103], v[64:67], a[36:39], v[100:103]\n\t"
        "s_mov_b32 m0, s53\n\t"
        "s_nop 0\n\t"
        "buffer_load_dwordx4 v128, s[56:59], s53 offen lds\n\t"
        "s_add_u32 s53, s53, 4096\n\t"
        "v_mfma_f32_16x16x32_bf16 v[124:127], v[68:71], a[108:111], v[124:127]\n\t"
        "s_mov_b32 m0, s52\n\t"
        "s_nop 0\n\t"
        "buffer_load_dwordx4 v128, s[56:59], s49 offen lds\n\t"
        "s_add_u32 s52, s52, 4096\n\t"
        "s_add_u32 s49, s49, 4096\n\t"
        "s_waitcnt vmcnt(12)\n\t"
        "s_barrier\n\t"
        "s_waitcnt lgkmcnt(0)\n\t"
        "v_mfma_f32_16x16x32_bf16 v[80:83], v[0:3], a[40:43], v[80:83]\n\t"
        "ds_read_b128 v[32:35], v129 offset:0\n\t"
        "ds_read_b128 v[36:39], v129 offset:1024\n\t"
        "ds_read_b128 v[40:43], v129 offset:2048\n\t"
        "v_mfma_f32_16x16x32_bf16 v[84:87], v[4:7], a[40:43], v[84:87]\n\t"
        "ds_read_b128 v[44:47], v129 offset:3072\n\t"
        "ds_read_b128 v[48:51], v130 offset:0\n\t"
        "v_mfma_f32_16x16x32_bf16 v[88:91], v[8:11], a[40:43], v[88:91]\n\t"
        "ds_read_b128 v[52:55], v130 offset:1024\n\t"
        "ds_read_b128 v[56:59], v130 offset:2048\n\t"
        "v_mfma_f32_16x16x32_bf16 v[92:95], v[12:15], a[40:43], v[92:95]\n\t"
        "ds_read_b128 v[60:63], v130 offset:3072\n\t"
        "ds_read_b128 v[72:75], v131\n\t"
        "v_add_u32 v131, 9216, v131\n\t"
        "v_mfma_f32_16x16x32_bf16 v[104:107], v[16:19], a[112:115], v[104:107]\n\t"
        "ds_read_b128 v[76:79], v132\n\t"
        "v_add_u32 v132, 9216, v132\n\t"
        "v_add_u32 v129, 4608, v129\n\t"
        "v_add_u32 v130, 4608, v130\n\t"
        "v_mfma_f32_16x16x32_bf16 v[108:111], v[20:23], a[112:115], v[108:111]\n\t"
        "buffer_load_dwordx4 a[64:67], v128, s[40:43], s50 offen offset:0\n\t"
        "buffer_load_dwordx4 a[136:139], v128, s[40:43], s51 offen offset:0\n\t"
        "v_mfma_f32_16x16x32_bf16 v[112:115], v[24:27], a[112:115], v[112:115]\n\t"
        "s_mov_b32 m0, s53\n\t"
        "s_nop 0\n\t"
        "buffer_load_dwordx4 v128, s[56:59], s53 offen lds\n\t"
        "s_add_u32 s53, s53, 4096\n\t"
        "s_mov_b32 m0, s53\n\t"
        "s_nop 0\n\t"
        "buffer_load_dwordx4 v128, s[56:59], s53 offen lds\n\t"
        "s_add_u32 s53, s53, 4096\n\t"
        "v_mfma_f32_16x16x32_bf16 v[116:119], v[28:31], a[112:115], v[116:119]\n\t"
        "s_mov_b32 m0, s52\n\t"
        "s_nop 0\n\t"
        "buffer_load_dwordx4 v128, s[56:59], s49 offen lds\n\t"
        "s_add_u32 s52, s52, 4096\n\t"
        "s_add_u32 s49, s49, 4096\n\t"
        "s_cmp_eq_u32 s46, 3\n\t"
        "s_cselect_b32 s49, 163328, s49\n\t"
        "s_cselect_b32 s52, 0, s52\n\t"
        "s_mov_b32 m0, s52\n\t"
        "s_nop 0\n\t"
        "buffer_load_dwordx4 v128, s[56:59], s49 offen lds\n\t"
        "s_waitcnt lgkmcnt(0)\n\t"
        "v_mfma_f32_16x16x32_bf16 v[80:83], v[32:35], a[44:47], v[80:83]\n\t"
        "ds_read_b128 v[0:3], v129 offset:0\n\t"
        "ds_read_b128 v[4:7], v129 offset:1024\n\t"
        "v_mfma_f32_16x16x32_bf16 v[84:87], v[36:39], a[44:47], v[84:87]\n\t"
        "ds_read_b128 v[8:11], v129 offset:2048\n\t"
        "v_mfma_f32_16x16x32_bf16 v[88:91], v[40:43], a[44:47], v[88:91]\n\t"
        "ds_read_b128 v[12:15], v129 offset:3072\n\t"
        "v_mfma_f32_16x16x32_bf16 v[92:95], v[44:47], a[44:47], v[92:95]\n\t"
        "ds_read_b128 v[16:19], v130 offset:0\n\t"
        "v_mfma_f32_16x16x32_bf16 v[104:107], v[48:51], a[116:119], v[104:107]\n\t"
        "ds_read_b128 v[20:23], v130 offset:1024\n\t"
        "v_mfma_f32_16x16x32_bf16 v[108:111], v[52:55], a[116:119], v[108:111]\n\t"
        "ds_read_b128 v[24:27], v130 offset:2048\n\t"
        "v_mfma_f32_16x16x32_bf16 v[112:115], v[56:59], a[116:119], v[112:115]\n\t"
        "ds_read_b128 v[28:31], v130 offset:3072\n\t"
        "v_mfma_f32_16x16x32_bf16 v[116:119], v[60:63], a[116:119], v[116:119]\n\t"
        "v_add_u32 v129, 4608, v129\n\t"
        "v_add_u32 v130, 4608, v130\n\t"
        "v_mfma_f32_16x16x32_bf16 v[96:99], v[72:75], a[40:43], v[96:99]\n\t"
        "buffer_load_dwordx4 a[68:71], v128, s[40:43], s50 offen offset:1024\n\t"
        "v_mfma_f32_16x16x32_bf16 v[120:123], v[76:79], a[112:115], v[120:123]\n\t"
        "buffer_load_dwordx4 a[140:143], v128, s[40:43], s51 offen offset:1024\n\t"
        "v_mfma_f32_16x16x32_bf16 v[100:103], v[72:75], a[44:47], v[100:103]\n\t"
        "s_mov_b32 m0, s53\n\t"
        "s_nop 0\n\t"
        "buffer_load_dwordx4 v128, s[56:59], s53 offen lds\n\t"
        "s_add_u32 s53, s53, 4096\n\t"
        "v_mfma_f32_16x16x32_bf16 v[124:127], v[76:79], a[116:119], v[124:127]\n\t"
        "s_add_u32 s52, s46, 1\n\t"
        "s_min_u32 s52, s52, 2\n\t"
        "s_lshl_b32 s52, s52, 10\n\t"
        "s_add_u32 s49, s52, 163328\n\t"
        "s_mov_b32 m0, s52\n\t"
        "s_nop 0\n\t"
        "buffer_load_dwordx4 v128, s[56:59], s49 offen lds\n\t"
        "s_waitcnt vmcnt(14)\n\t"
        "s_barrier\n\t"
        "s_waitcnt lgkmcnt(0)\n\t"
        "v_mfma_f32_16x16x32_bf16 v[80:83], v[0:3], a[48:51], v[80:83]\n\t"
        "ds_read_b128 v[32:35], v129 offset:0\n\t"
        "ds_read_b128 v[36:39], v129 offset:1024\n\t"
        "v_mfma_f32_16x16x32_bf16 v[84:87], v[4:7], a[48:51], v[84:87]\n\t"
        "ds_read_b128 v[40:43], v129 offset:2048\n\t"
        "v_mfma_f32_16x16x32_bf16 v[88:91], v[8:11], a[48:51], v[88:91]\n\t"
        "ds_read_b128 v[44:47], v129 offset:3072\n\t"
        "ds_read_b128 v[48:51], v130 offset:0\n\t"
        "v_mfma_f32_16x16x32_bf16 v[92:95], v[12:15], a[48:51], v[92:95]\n\t"
        "ds_read_b128 v[52:55], v130 offset:1024\n\t"
        "v_mfma_f32_16x16x32_bf16 v[104:107], v[16:19], a[120:123], v[104:107]\n\t"
        "ds_read_b128 v[56:59], v130 offset:2048\n\t"
        "v_mfma_f32_16x16x32_bf16 v[108:111], v[20:23], a[120:123], v[108:111]\n\t"
        "ds_read_b128 v[60:63], v130 offset:3072\n\t"
        "ds_read_b128 v[64:67], v131\n\t"
        "v_add_u32 v131, 9216, v131\n\t"
        "v_mfma_f32_16x16x32_bf16 v[112:115], v[24:27], a[120:123], v[112:115]\n\t"
        "ds_read_b128 v[68:71], v132\n\t"
        "v_add_u32 v132, 9216, v132\n\t"
        "v_mfma_f32_16x16x32_bf16 v[116:119], v[28:31], a[120:123], v[116:119]\n\t"
        "v_add_u32 v129, 4608, v129\n\t"
        "v_add_u32 v130, 4608, v130\n\t"
        "s_waitcnt lgkmcnt(0)\n\t"
        "v_mfma_f32_16x16x32_bf16 v[80:83], v[32:35], a[52:55], v[80:83]\n\t"
        "ds_read_b128 v[0:3], v129 offset:0\n\t"
        "v_mfma_f32_16x16x32_bf16 v[84:87], v[36:39], a[52:55], v[84:87]\n\t"
        "ds_read_b128 v[4:7], v129 offset:1024\n\t"
        "v_mfma_f32_16x16x32_bf16 v[88:91], v[40:43], a[52:55], v[88:91]\n\t"
        "ds_read_b128 v[8:11], v129 offset:2048\n\t"
        "v_mfma_f32_16x16x32_bf16 v[92:95], v[44:47], a[52:55], v[92:95]\n\t"
        "v_mfma_f32_16x16x32_bf16 v[104:107], v[48:51], a[124:127], v[104:107]\n\t"
        "ds_read_b128 v[12:15], v129 offset:3072\n\t"
        "v_mfma_f32_16x16x32_bf16 v[108:111], v[52:55], a[124:127], v[108:111]\n\t"
        "ds_read_b128 v[16:19], v130 offset:0\n\t"
        "v_mfma_f32_16x16x32_bf16 v[112:115], v[56:59], a[124:127], v[112:115]\n\t"
        "ds_read_b128 v[20:23], v130 offset:1024\n\t"
        "v_mfma_f32_16x16x32_bf16 v[116:119], v[60:63], a[124:127], v[116:119]\n\t"
        "v_mfma_f32_16x16x32_bf16 v[96:99], v[64:67], a[48:51], v[96:99]\n\t"
        "ds_read_b128 v[24:27], v130 offset:2048\n\t"
        "v_mfma_f32_16x16x32_bf16 v[120:123], v[68:71], a[120:123], v[120:123]\n\t"
        "ds_read_b128 v[28:31], v130 offset:3072\n\t"
        "v_mfma_f32_16x16x32_bf16 v[100:103], v[64:67], a[52:55], v[100:103]\n\t"
        "v_add_u32 v129, 4608, v129\n\t"
        "v_add_u32 v130, 4608, v130\n\t"
        "v_mfma_f32_16x16x32_bf16 v[124:127], v[68:71], a[124:127], v[124:127]\n\t"
        "s_waitcnt vmcnt(10)\n\t"
        "s_barrier\n\t"
        "s_waitcnt lgkmcnt(0)\n\t"
        "v_mfma_f32_16x16x32_bf16 v[80:83], v[0:3], a[56:59], v[80:83]\n\t"
        "ds_read_b128 v[32:35], v129 offset:0\n\t"
        "ds_read_b128 v[36:39], v129 offset:1024\n\t"
        "v_mfma_f32_16x16x32_bf16 v[84:87], v[4:7], a[56:59], v[84:87]\n\t"
        "ds_read_b128 v[40:43], v129 offset:2048\n\t"
        "v_mfma_f32_16x16x32_bf16 v[88:91], v[8:11], a[56:59], v[88:91]\n\t"
        "ds_read_b128 v[44:47], v129 offset:3072\n\t"
        "ds_read_b128 v[48:51], v130 offset:0\n\t"
        "v_mfma_f32_16x16x32_bf16 v[92:95], v[12:15], a[56:59], v[92:95]\n\t"
        "ds_read_b128 v[52:55], v130 offset:1024\n\t"
        "v_mfma_f32_16x16x32_bf16 v[104:107], v[16:19], a[128:131], v[104:107]\n\t"
        "ds_read_b128 v[56:59], v130 offset:2048\n\t"
        "v_mfma_f32_16x16x32_bf16 v[108:111], v[20:23], a[128:131], v[108:111]\n\t"
        "ds_read_b128 v[60:63], v130 offset:3072\n\t"
        "ds_read_b128 v[72:75], v131\n\t"
        "v_add_u32 v131, 9216, v131\n\t"
        "v_mfma_f32_16x16x32_bf16 v[112:115], v[24:27], a[128:131], v[112:115]\n\t"
        "ds_read_b128 v[76:79], v132\n\t"
        "v_add_u32 v132, 9216, v132\n\t"
        "v_mfma_f32_16x16x32_bf16 v[116:119], v[28:31], a[128:131], v[116:119]\n\t"
        "v_add_u32 v129, 4608, v129\n\t"
        "v_add_u32 v130, 4608, v130\n\t"
        "s_waitcnt vmcnt(4)\n\t"
        "s_barrier\n\t"
        "s_waitcnt lgkmcnt(0)\n\t"
        "v_mfma_f32_16x16x32_bf16 v[80:83], v[32:35], a[60:63], v[80:83]\n\t"
        "ds_read_b128 v[0:3], v129 offset:0\n\t"
        "v_mfma_f32_16x16x32_bf16 v[84:87], v[36:39], a[60:63], v[84:87]\n\t"
        "ds_read_b128 v[4:7], v129 offset:1024\n\t"
        "v_mfma_f32_16x16x32_bf16 v[88:91], v[40:43], a[60:63], v[88:91]\n\t"
        "ds_read_b128 v[8:11], v129 offset:2048\n\t"
        "v_mfma_f32_16x16x32_bf16 v[92:95], v[44:47], a[60:63], v[92:95]\n\t"
        "v_mfma_f32_16x16x32_bf16 v[104:107], v[48:51], a[132:135], v[104:107]\n\t"
        "ds_read_b128 v[12:15], v129 offset:3072\n\t"
        "v_mfma_f32_16x16x32_bf16 v[108:111], v[52:55], a[132:135], v[108:111]\n\t"
        "ds_read_b128 v[16:19], v130 offset:0\n\t"
        "v_mfma_f32_16x16x32_bf16 v[112:115], v[56:59], a[132:135], v[112:115]\n\t"
        "ds_read_b128 v[20:23], v130 offset:1024\n\t"
        "v_mfma_f32_16x16x32_bf16 v[116:119], v[60:63], a[132:135], v[116:119]\n\t"
        "v_mfma_f32_16x16x32_bf16 v[96:99], v[72:75], a[56:59], v[96:99]\n\t"
        "ds_read_b128 v[24:27], v130 offset:2048\n\t"
        "v_mfma_f32_16x16x32_bf16 v[120:123], v[76:79], a[128:131], v[120:123]\n\t"
        "ds_read_b128 v[28:31], v130 offset:3072\n\t"
        "v_mfma_f32_16x16x32_bf16 v[100:103], v[72:75], a[60:63], v[100:103]\n\t"
        "v_add_u32 v129, 4608, v129\n\t"
        "v_add_u32 v130, 4608, v130\n\t"
        "v_mfma_f32_16x16x32_bf16 v[124:127], v[76:79], a[132:135], v[124:127]\n\t"
        "s_waitcnt vmcnt(0)\n\t"
        "s_barrier\n\t"
        "s_waitcnt lgkmcnt(0)\n\t"
        "v_mfma_f32_16x16x32_bf16 v[80:83], v[0:3], a[64:67], v[80:83]\n\t"
        "ds_read_b128 v[32:35], v129 offset:0\n\t"
        "ds_read_b128 v[36:39], v129 offset:1024\n\t"
        "v_mfma_f32_16x16x32_bf16 v[84:87], v[4:7], a[64:67], v[84:87]\n\t"
        "ds_read_b128 v[40:43], v129 offset:2048\n\t"
        "v_mfma_f32_16x16x32_bf16 v[88:91], v[8:11], a[64:67], v[88:91]\n\t"
        "ds_read_b128 v[44:47], v129 offset:3072\n\t"
        "ds_read_b128 v[48:51], v130 offset:0\n\t"
        "v_mfma_f32_16x16x32_bf16 v[92:95], v[12:15], a[64:67], v[92:95]\n\t"
        "ds_read_b128 v[52:55], v130 offset:1024\n\t"
        "v_mfma_f32_16x16x32_bf16 v[104:107], v[16:19], a[136:139], v[104:107]\n\t"
        "ds_read_b128 v[56:59], v128 offset:0\n\t"
        "v_mfma_f32_16x16x32_bf16 v[108:111], v[20:23], a[136:139], v[108:111]\n\t"
        "ds_read_b128 v[60:63], v128 offset:1024\n\t"
        "ds_read_b128 v[64:67], v131\n\t"
        "v_add_u32 v131, 9216, v131\n\t"
        "v_mfma_f32_16x16x32_bf16 v[112:115], v[24:27], a[136:139], v[112:115]\n\t"
        "ds_read_b128 v[68:71], v133\n\t"
        "v_mfma_f32_16x16x32_bf16 v[116:119], v[28:31], a[136:139], v[116:119]\n\t"
        "v_add_u32 v129, 4608, v129\n\t"
        "v_add_u32 v130, 4608, v130\n\t"
        "s_waitcnt lgkmcnt(0)\n\t"
        "v_mfma_f32_16x16x32_bf16 v[80:83], v[32:35], a[68:71], v[80:83]\n\t"
        "v_mfma_f32_16x16x32_bf16 v[104:107], v[48:51], a[140:143], v[104:107]\n\t"
        "v_mfma_f32_16x16x32_bf16 v[84:87], v[36:39], a[68:71], v[84:87]\n\t"
        "v_mfma_f32_16x16x32_bf16 v[108:111], v[52:55], a[140:143], v[108:111]\n\t"
        "v_mfma_f32_16x16x32_bf16 v[88:91], v[40:43], a[68:71], v[88:91]\n\t"
        "v_mfma_f32_16x16x32_bf16 v[112:115], v[56:59], a[140:143], v[112:115]\n\t"
        "v_mfma_f32_16x16x32_bf16 v[92:95], v[44:47], a[68:71], v[92:95]\n\t"
        "v_mfma_f32_16x16x32_bf16 v[116:119], v[60:63], a[140:143], v[116:119]\n\t"
        "v_mfma_f32_16x16x32_bf16 v[96:99], v[64:67], a[64:67], v[96:99]\n\t"
        "v_mfma_f32_16x16x32_bf16 v[120:123], v[68:71], a[136:139], v[120:123]\n\t"
        "v_mfma_f32_16x16x32_bf16 v[100:103], v[64:67], a[68:71], v[100:103]\n\t"
        "v_mfma_f32_16x16x32_bf16 v[124:127], v[68:71], a[140:143], v[124:127]\n\t"
        "s_nop 0\n\t"
        "v_pk_add_f32 v[80:81], v[80:81], v[104:105] neg_lo:[0,1] neg_hi:[0,1]\n\t"
        "v_pk_add_f32 v[82:83], v[82:83], v[106:107] neg_lo:[0,1] neg_hi:[0,1]\n\t"
        "v_pk_fma_f32 v[80:81], v[136:137], v[80:81], v[104:105] op_sel_hi:[0,1,1]\n\t"
        "v_pk_fma_f32 v[82:83], v[136:137], v[82:83], v[106:107] op_sel_hi:[0,1,1]\n\t"
        "v_cvt_pk_bf16_f32 v144, v80, v81\n\t"
        "v_cvt_pk_bf16_f32 v145, v82, v83\n\t"
        "v_pk_add_f32 v[84:85], v[84:85], v[108:109] neg_lo:[0,1] neg_hi:[0,1]\n\t"
        "v_pk_add_f32 v[86:87], v[86:87], v[110:111] neg_lo:[0,1] neg_hi:[0,1]\n\t"
        "v_pk_fma_f32 v[84:85], v[136:137], v[84:85], v[108:109] op_sel_hi:[0,1,1]\n\t"
        "v_pk_fma_f32 v[86:87], v[136:137], v[86:87], v[110:111] op_sel_hi:[0,1,1]\n\t"
        "v_cvt_pk_bf16_f32 v146, v84, v85\n\t"
        "v_cvt_pk_bf16_f32 v147, v86, v87\n\t"
        "global_store_dwordx4 v138, v[144:147], s[44:45] offset:0\n\t"
        "v_pk_add_f32 v[88:89], v[88:89], v[112:113] neg_lo:[0,1] neg_hi:[0,1]\n\t"
        "v_pk_add_f32 v[90:91], v[90:91], v[114:115] neg_lo:[0,1] neg_hi:[0,1]\n\t"
        "v_pk_fma_f32 v[88:89], v[136:137], v[88:89], v[112:113] op_sel_hi:[0,1,1]\n\t"
        "v_pk_fma_f32 v[90:91], v[136:137], v[90:91], v[114:115] op_sel_hi:[0,1,1]\n\t"
        "v_cvt_pk_bf16_f32 v148, v88, v89\n\t"
        "v_cvt_pk_bf16_f32 v149, v90, v91\n\t"
        "v_pk_add_f32 v[92:93], v[92:93], v[116:117] neg_lo:[0,1] neg_hi:[0,1]\n\t"
        "v_pk_add_f32 v[94:95], v[94:95], v[118:119] neg_lo:[0,1] neg_hi:[0,1]\n\t"
        "v_pk_fma_f32 v[92:93], v[136:137], v[92:93], v[116:117] op_sel_hi:[0,1,1]\n\t"
        "v_pk_fma_f32 v[94:95], v[136:137], v[94:95], v[118:119] op_sel_hi:[0,1,1]\n\t"
        "v_cvt_pk_bf16_f32 v150, v92, v93\n\t"
        "v_cvt_pk_bf16_f32 v151, v94, v95\n\t"
        "global_store_dwordx4 v138, v[148:151], s[44:45] offset:64\n\t"
        "v_cndmask_b32 v96, v96, v100, s[62:63]\n\t"
        "v_cndmask_b32 v97, v97, v101, s[62:63]\n\t"
        "v_cndmask_b32 v98, v98, v102, s[62:63]\n\t"
        "v_cndmask_b32 v99, v99, v103, s[62:63]\n\t"
        "v_cndmask_b32 v120, v120, v124, s[62:63]\n\t"
        "v_cndmask_b32 v121, v121, v125, s[62:63]\n\t"
        "v_cndmask_b32 v122, v122, v126, s[62:63]\n\t"
        "v_cndmask_b32 v123, v123, v127, s[62:63]\n\t"
        "v_pk_add_f32 v[96:97], v[96:97], v[120:121] neg_lo:[0,1] neg_hi:[0,1]\n\t"
        "v_pk_add_f32 v[98:99], v[98:99], v[122:123] neg_lo:[0,1] neg_hi:[0,1]\n\t"
        "v_pk_fma_f32 v[96:97], v[136:137], v[96:97], v[120:121] op_sel_hi:[0,1,1]\n\t"
        "v_pk_fma_f32 v[98:99], v[136:137], v[98:99], v[122:123] op_sel_hi:[0,1,1]\n\t"
        "v_mov_b32 v120, v96\n\t"
        "v_mov_b32 v121, v97\n\t"
        "v_mov_b32 v122, v98\n\t"
        "v_mov_b32 v123, v99\n\t"
        "s_nop 0\n\t"
        "v_permlane32_swap_b32 v96, v120\n\t"
        "v_permlane32_swap_b32 v97, v121\n\t"
        "v_permlane32_swap_b32 v98, v122\n\t"
        "v_permlane32_swap_b32 v99, v123\n\t"
        "s_nop 0\n\t"
        "v_add_f32 v96, v96, v120\n\t"
        "v_add_f32 v97, v97, v121\n\t"
        "v_add_f32 v98, v98, v122\n\t"
        "v_add_f32 v99, v99, v123\n\t"
        "v_cvt_pk_bf16_f32 v152, v96, v97\n\t"
        "v_cvt_pk_bf16_f32 v153, v98, v99\n\t"
        "s_mov_b32 exec_hi, 0\n\t"
        "global_store_dwordx2 v139, v[152:153], s[44:45]\n\t"
        "s_mov_b32 exec_hi, -1\n\t"
        :
        : "{v255}"(threadIdx.x), [karg] "s"((unsigned long long)g_ptrs), [b] "s"(b)
        : "v0", "v1", "v2", "v3", "v4", "v5", "v6", "v7", "v8", "v9", "v10", "v11", "v12", "v13", "v14", "v15", "v16", "v17", "v18", "v19", "v20", "v21", "v22", "v23", "v24", "v25", "v26", "v27", "v28", "v29", "v30", "v31", "v32", "v33", "v34", "v35", "v36", "v37", "v38", "v39", "v40", "v41", "v42", "v43", "v44", "v45", "v46", "v47", "v48", "v49", "v50", "v51", "v52", "v53", "v54", "v55", "v56", "v57", "v58", "v59", "v60", "v61", "v62", "v63", "v64", "v65", "v66", "v67", "v68", "v69", "v70", "v71", "v72", "v73", "v74", "v75", "v76", "v77", "v78", "v79", "v80", "v81", "v82", "v83", "v84", "v85", "v86", "v87", "v88", "v89", "v90", "v91", "v92", "v93", "v94", "v95", "v96", "v97", "v98", "v99", "v100", "v101", "v102", "v103", "v104", "v105", "v106", "v107", "v108", "v109", "v110", "v111", "v112", "v113", "v114", "v115", "v116", "v117", "v118", "v119", "v120", "v121", "v122", "v123", "v124", "v125", "v126", "v127", "v128", "v129", "v130", "v131", "v132", "v133", "v134", "v135", "v136", "v137", "v138", "v139", "v140", "v141", "v142", "v143", "v144", "v145", "v146", "v147", "v148", "v149", "v150", "v151", "v152", "v153", "v154", "v155", "v156", "v157", "v158", "v159", "a0", "a1", "a2", "a3", "a4", "a5", "a6", "a7", "a8", "a9", "a10", "a11", "a12", "a13", "a14", "a15", "a16", "a17", "a18", "a19", "a20", "a21", "a22", "a23", "a24", "a25", "a26", "a27", "a28", "a29", "a30", "a31", "a32", "a33", "a34", "a35", "a36", "a37", "a38", "a39", "a40", "a41", "a42", "a43", "a44", "a45", "a46", "a47", "a48", "a49", "a50", "a51", "a52", "a53", "a54", "a55", "a56", "a57", "a58", "a59", "a60", "a61", "a62", "a63", "a64", "a65", "a66", "a67", "a68", "a69", "a70", "a71", "a72", "a73", "a74", "a75", "a76", "a77", "a78", "a79", "a80", "a81", "a82", "a83", "a84", "a85", "a86", "a87", "a88", "a89", "a90", "a91", "a92", "a93", "a94", "a95", "a96", "a97", "a98", "a99", "a100", "a101", "a102", "a103", "a104", "a105", "a106", "a107", "a108", "a109", "a110", "a111", "a112", "a113", "a114", "a115", "a116", "a117", "a118", "a119", "a120", "a121", "a122", "a123", "a124", "a125", "a126", "a127", "a128", "a129", "a130", "a131", "a132", "a133", "a134", "a135", "a136", "a137", "a138", "a139", "a140", "a141", "a142", "a143", "s40", "s41", "s42", "s43", "s44", "s45", "s46", "s47", "s48", "s49", "s50", "s51", "s52", "s53", "s54", "s55", "s56", "s57", "s58", "s59", "s60", "s61", "s62", "s63", "s64", "s65", "s66", "s67", "s68", "s69", "s70", "s71", "scc", "vcc", "memory");

    STAMP(2, 1);
    STAMP(2, 2);
}

extern "C" void kernel_launch(void* const* d_in, const int* in_sizes, int n_in,
                              void* d_out, int out_size, void* d_ws, size_t ws_size,
                              hipStream_t stream) {
    const u16* x = (const u16*)d_in[0]; const u16* wqkv = (const u16*)d_in[1]; const u16* wphi = (const u16*)d_in[2];
    const u16* woA = (const u16*)d_in[3]; const u16* wq = (const u16*)d_in[5]; const u16* wk = (const u16*)d_in[6];
    const u16* wv = (const u16*)d_in[7]; const u16* woB = (const u16*)d_in[8]; const u16* gw = (const u16*)d_in[9];
    char* ws = (char*)d_ws;
    hipFuncSetAttribute((const void*)k1_proj, hipFuncAttributeMaxDynamicSharedMemorySize, K1_LDS);
    k1_proj<<<256, 512, K1_LDS, stream>>>(x, wqkv, wphi, woA, wq, wk, wv, woB, gw, ws, (u16*)d_out);
    hipFuncSetAttribute((const void*)k2_attn, hipFuncAttributeMaxDynamicSharedMemorySize, K2_LDS);
    k2_attn<<<256, 512, K2_LDS, stream>>>(ws);
    hipFuncSetAttribute((const void*)k3_out, hipFuncAttributeMaxDynamicSharedMemorySize, K3_LDS);
    k3_out<<<256, 256, K3_LDS, stream>>>(ws, (u16*)d_out);
}
```
